```python
import torch

def _softmax(x, axis=-1):
    return torch.softmax(x, dim=axis)
def _gelu(x):
    return torch.nn.functional.gelu(x, approximate="tanh")
def _silu(x):
    return torch.nn.functional.silu(x)
def _sigmoid(x):
    return torch.sigmoid(x)
def _layernorm(x, axis=-1, eps=1e-5):
    m = x.mean(dim=axis, keepdim=True)
    v = x.var(dim=axis, keepdim=True, unbiased=False)
    return (x - m) / torch.sqrt(v + eps)
def _scatter_add(tgt, idx, src, axis=0):
    return tgt.clone().index_add_(axis, idx, src)
def _segment_sum(src, seg, n):
    out = torch.zeros((n,) + src.shape[1:], dtype=src.dtype)
    return out.index_add_(0, seg, src)
def _pad(x, pw):
    flat = tuple(v for pair in reversed(pw) for v in pair)
    return torch.nn.functional.pad(x, flat)

kernel_name = 'sequential_preLN__sliding_block__dense_activation_squared_relu__prompt'

def setup_inputs():

    _w = (lambda a: (lambda t: t.to(torch.bfloat16) if t.is_floating_point() else t)(torch.as_tensor(a)))
    def _A_setup():
        B, S, D = 1, 2048, 448
        return {
            "x": _w((lambda *s: torch.randn(*s, dtype=torch.bfloat16) * 0.05)(B, S, D)),
            "wqkv": _w((lambda *s: torch.randn(*s, dtype=torch.bfloat16) * 0.05)(D, 3 * D)),
            "wo": _w((lambda *s: torch.randn(*s, dtype=torch.bfloat16) * 0.05)(D, D)),
        }
    _A_in = _A_setup()

    def _B_setup():
        B, S, D, F = 1, 2048, 448, 1792
        return {
            "x": _w((lambda *s: torch.randn(*s, dtype=torch.bfloat16) * 0.05)(B, S, D)),
            "w1": _w((lambda *s: torch.randn(*s, dtype=torch.bfloat16) * 0.05)(D, F)),
            "b1": _w((lambda *s: torch.randn(*s, dtype=torch.bfloat16) * 0.4)(F)),
            "w2": _w((lambda *s: torch.randn(*s, dtype=torch.bfloat16) * 0.05)(F, D)),
            "b2": _w((lambda *s: torch.randn(*s, dtype=torch.bfloat16) * 1.6)(D)),
        }
    _B_in = _B_setup()

    return {
        "A_x": _A_in["x"],
        "A_wqkv": _A_in["wqkv"],
        "A_wo": _A_in["wo"],
        "B_x": _B_in["x"],
        "B_w1": _B_in["w1"],
        "B_b1": _B_in["b1"],
        "B_w2": _B_in["w2"],
        "B_b2": _B_in["b2"],
    }

def reference(A_x, A_wqkv, A_wo, B_x, B_w1, B_b1, B_w2, B_b2):
    def _A_ref(x, wqkv, wo):
        B, S, D = x.shape
        H = 16; Dh = D // H; W = 256
        qkv = (lambda x, *d: x.permute(*d))((x @ wqkv).reshape(B, S, 3, H, Dh), 2, 0, 3, 1, 4)
        q, k, v = qkv[0], qkv[1], qkv[2]
        scores = torch.einsum("bhqd,bhkd->bhqk", q, k) * (Dh ** -0.5)
        idx = torch.arange(S)
        mask = (idx[:, None] - idx[None, :] >= 0) & (idx[:, None] - idx[None, :] < W)
        scores = torch.where(mask[None, None], scores, -1e9)
        p = _softmax(scores, axis=-1)
        o = torch.einsum("bhqk,bhkd->bhqd", p, v)
        return (lambda x, *d: x.permute(*d))(o, 0, 2, 1, 3).reshape(B, S, D) @ wo

    def _B_ref(x, w1, b1, w2, b2):
        h = torch.einsum("bsd,df->bsf", x, w1) + b1
        zero = torch.zeros_like(h)
        r = torch.where(h > zero, h, zero)
        h = r * r
        out = torch.einsum("bsf,fd->bsd", h, w2) + b2
        return out

    x = A_x
    h = x + _A_ref(_layernorm(x, axis=-1), A_wqkv, A_wo)
    h2 = h + _B_ref(_layernorm(h, axis=-1), B_w1, B_b1, B_w2, B_b2)
    return h2
```

```cpp
#include <hip/hip_runtime.h>
#include <hip/hip_bf16.h>
typedef unsigned short u16;
__device__ __forceinline__ float b2f(u16 v){ return __uint_as_float(((unsigned)v)<<16); }
__device__ __forceinline__ u16 f2b(float f){ unsigned u=__float_as_uint(f); u += 0x7fffu + ((u>>16)&1u); return (u16)(u>>16); }
constexpr int S=2048,D=448,H=16,DH=28,W=256,F=1792;
__global__ void ln_k(const u16* x,u16* y){
  int r=blockIdx.x,l=threadIdx.x; float s=0,q=0; float v[7];
  for(int i=0;i<7;i++){ v[i]=b2f(x[r*D+l+64*i]); s+=v[i]; }
  for(int o=32;o;o>>=1) s+=__shfl_xor(s,o);
  float m=s/D;
  for(int i=0;i<7;i++){ float d=v[i]-m; q+=d*d; }
  for(int o=32;o;o>>=1) q+=__shfl_xor(q,o);
  float rs=rsqrtf(q/D+1e-5f);
  for(int i=0;i<7;i++) y[r*D+l+64*i]=f2b((v[i]-m)*rs);
}
__global__ void gemm_k(const u16* A,const u16* B,u16* C,int N,int K,int mode,const u16* bias,const u16* res){
  int n=blockIdx.x*64+threadIdx.x; int m=blockIdx.y; float acc=0;
  for(int k=0;k<K;k++) acc+=b2f(A[m*K+k])*b2f(B[k*N+n]);
  if(mode==1) acc+=b2f(res[m*N+n]);
  if(mode==2){ acc+=b2f(bias[n]); acc=acc>0?acc*acc:0.f; }
  if(mode==3){ acc+=b2f(bias[n])+b2f(res[m*N+n]); }
  C[m*N+n]=f2b(acc);
}
__global__ void attn_k(const u16* qkv,u16* o){
  int i=blockIdx.x,h=blockIdx.y,l=threadIdx.x;
  __shared__ float p[256]; __shared__ float q[DH];
  if(l<DH) q[l]=b2f(qkv[i*3*D+h*DH+l]);
  __syncthreads();
  float mx=-1e30f;
  for(int t=0;t<4;t++){ int jj=l+64*t; int j=i-jj; float s=-1e30f; if(j>=0){ s=0; for(int d=0;d<DH;d++) s+=q[d]*b2f(qkv[j*3*D+D+h*DH+d]); s*=rsqrtf((float)DH);} p[jj]=s; mx=fmaxf(mx,s);}
  for(int o2=32;o2;o2>>=1) mx=fmaxf(mx,__shfl_xor(mx,o2));
  float sum=0;
  for(int t=0;t<4;t++){ int jj=l+64*t; float e=(i-jj>=0)?__expf(p[jj]-mx):0.f; p[jj]=e; sum+=e; }
  for(int o2=32;o2;o2>>=1) sum+=__shfl_xor(sum,o2);
  __syncthreads();
  if(l<DH){ float a=0; for(int jj=0;jj<256;jj++){ int j=i-jj; if(j>=0) a+=p[jj]*b2f(qkv[j*3*D+2*D+h*DH+l]); } o[i*D+h*DH+l]=f2b(a/sum); }
}
extern "C" void kernel_launch(void* const* d_in, const int* in_sizes, int n_in, void* d_out, int out_size, void* d_ws, size_t ws_size, hipStream_t stream) {
  const u16* x=(const u16*)d_in[0]; const u16* wqkv=(const u16*)d_in[1]; const u16* wo=(const u16*)d_in[2];
  const u16* w1=(const u16*)d_in[4]; const u16* b1=(const u16*)d_in[5]; const u16* w2=(const u16*)d_in[6]; const u16* b2=(const u16*)d_in[7];
  u16* ws=(u16*)d_ws; u16* xn=ws; u16* qkv=xn+S*D; u16* o=qkv+S*3*D; u16* h=o+S*D; u16* hn=h+S*D; u16* hh=hn+S*D;
  ln_k<<<S,64,0,stream>>>(x,xn);
  gemm_k<<<dim3(3*D/64,S),64,0,stream>>>(xn,wqkv,qkv,3*D,D,0,nullptr,nullptr);
  attn_k<<<dim3(S,H),64,0,stream>>>(qkv,o);
  gemm_k<<<dim3(D/64,S),64,0,stream>>>(o,wo,h,D,D,1,nullptr,x);
  ln_k<<<S,64,0,stream>>>(h,hn);
  gemm_k<<<dim3(F/64,S),64,0,stream>>>(hn,w1,hh,F,D,2,b1,nullptr);
  gemm_k<<<dim3(D/64,S),64,0,stream>>>(hh,w2,(u16*)d_out,D,F,3,b2,h);
}
```

```cpp
#include <hip/hip_runtime.h>
#include <stdint.h>

typedef unsigned short u16;
typedef unsigned int u32;
typedef __attribute__((ext_vector_type(8))) __bf16 bf16x8;
typedef __attribute__((ext_vector_type(2))) __bf16 bf16x2;
typedef __attribute__((ext_vector_type(4))) short s16x4;
typedef __attribute__((ext_vector_type(4))) float f32x4;
typedef __attribute__((ext_vector_type(2))) float f32x2;
typedef __attribute__((ext_vector_type(4))) u32 u32x4;
typedef __attribute__((ext_vector_type(2))) u32 u32x2;
#define LAS __attribute__((address_space(3)))
#define GAS __attribute__((address_space(1)))
#define DI __device__ __forceinline__

constexpr int S = 2048, D = 448, F = 1792;
constexpr size_t WS_KV = 0;
constexpr size_t WS_Q = WS_KV + 16 * 16 * 16384;
constexpr size_t WS_O = WS_Q + 16 * 16 * 8192;
constexpr size_t WS_H = WS_O + 128 * 16 * 1024;
constexpr size_t WS_ST = WS_H + 128 * 14 * 1024;
constexpr size_t WS_HH = WS_ST + 128 * 14 * 128;
constexpr size_t WS_END = WS_HH + 128 * 56 * 1024;

DI f32x4 mfma16(u32x4 a, u32x4 b, f32x4 c) {
    return __builtin_amdgcn_mfma_f32_16x16x32_bf16(__builtin_bit_cast(bf16x8, a), __builtin_bit_cast(bf16x8, b), c, 0, 0, 0);
}
DI u32 pack2(float lo, float hi) {
    f32x2 v = {lo, hi};
    bf16x2 r = __builtin_convertvector(v, bf16x2);
    return __builtin_bit_cast(u32, r);
}
DI float bflo(u32 u) { return __uint_as_float(u << 16); }
DI float bfhi(u32 u) { return __uint_as_float(u & 0xffff0000u); }
DI u32x2 trread(const char* smem, u32 off) {
    s16x4 r = __builtin_amdgcn_ds_read_tr16_b64_v4i16((LAS s16x4*)(smem + off));
    return __builtin_bit_cast(u32x2, r);
}
DI u32x4 trfrag(const char* smem, u32 off0, u32 off1) {
    u32x2 a = trread(smem, off0), b = trread(smem, off1);
    u32x4 r = {a[0], a[1], b[0], b[1]};
    return r;
}
DI void dma16(const void* g, char* lds) {
    __builtin_amdgcn_global_load_lds((const GAS void*)g, (LAS void*)lds, 16, 0, 0);
}
DI float xor16(float v) { return __shfl_xor(v, 16); }
DI float xor32(float v) { return __shfl_xor(v, 32); }

__global__ __launch_bounds__(512, 2) void k_p1(const u16* __restrict__ x, const u16* __restrict__ wqkv, char* __restrict__ ws) {
    extern __shared__ __attribute__((aligned(16))) char smem[];
    const int b = blockIdx.x, xcd = b & 7, loc = b >> 3;
    const int RB = 2 * xcd + (loc >> 4), h = loc & 15;
    const int tid = threadIdx.x, w = __builtin_amdgcn_readfirstlane(tid >> 6), l = tid & 63, t = l & 15, g = l >> 4;
    const int woff = 56 * h - (h & 1) * 8;
    for (int p = w; p < 84; p += 8) {
        int mat = p / 28, rblk = p % 28;
        int row = rblk * 16 + (l >> 2);
        int ch = (l & 3) ^ (((row >> 3) & 1) << 1);
        dma16((const char*)wqkv + row * 2688 + mat * 896 + woff + ch * 16, smem + p * 1024);
    }
    const char* xrow = (const char*)x + (size_t)(128 * RB + 16 * w + t) * 896 + g * 16;
    u32x4 xf[14];
#pragma unroll
    for (int s = 0; s < 14; ++s) xf[s] = *(const u32x4*)(xrow + 64 * s);
    float sum = 0.f, sq = 0.f;
#pragma unroll
    for (int s = 0; s < 14; ++s)
#pragma unroll
        for (int j = 0; j < 4; ++j) {
            float a = bflo(xf[s][j]), c = bfhi(xf[s][j]);
            sum += a; sum += c; sq = fmaf(a, a, sq); sq = fmaf(c, c, sq);
        }
    sum += xor16(sum); sum += xor32(sum);
    sq += xor16(sq); sq += xor32(sq);
    const float mean = sum * (1.f / 448.f);
    const float var = fmaxf(sq * (1.f / 448.f) - mean * mean, 0.f);
    const float rstd = rsqrtf(var + 1e-5f);
    const float nb = -mean * rstd;
#pragma unroll
    for (int s = 0; s < 14; ++s)
#pragma unroll
        for (int j = 0; j < 4; ++j) {
            float a = bflo(xf[s][j]), c = bfhi(xf[s][j]);
            xf[s][j] = pack2(fmaf(a, rstd, nb), fmaf(c, rstd, nb));
        }
    __syncthreads();
    f32x4 acc[6];
#pragma unroll
    for (int c = 0; c < 6; ++c) acc[c] = (f32x4){0.f, 0.f, 0.f, 0.f};
    const int q4 = (l & 15) >> 2, p4 = l & 3;
    const u32 lb0 = (8 * g + q4) * 64 + ((0 ^ (g & 1)) * 32) + p4 * 8;
    const u32 lb1 = (8 * g + q4) * 64 + ((1 ^ (g & 1)) * 32) + p4 * 8;
#pragma unroll
    for (int s = 0; s < 14; ++s) {
#pragma unroll
        for (int c = 0; c < 6; ++c) {
            const u32 base = (c >> 1) * 28672 + s * 2048 + ((c & 1) ? lb1 : lb0);
            u32x4 wf = trfrag(smem, base, base + 256);
            if (c < 4) acc[c] = mfma16(wf, xf[s], acc[c]);
            else acc[c] = mfma16(xf[s], wf, acc[c]);
        }
    }
    __syncthreads();
    const bool odd = h & 1;
    const float qs = 0.18898223650461362f * 1.4426950408889634f;
    {
        const float m0 = (odd && g == 0) ? 0.f : 1.f, m1 = (!odd && g == 3) ? 0.f : 1.f;
        u32x4 qf = {pack2(acc[0][0] * qs * m0, acc[0][1] * qs * m0), pack2(acc[0][2] * qs * m0, acc[0][3] * qs * m0),
                    pack2(acc[1][0] * qs * m1, acc[1][1] * qs * m1), pack2(acc[1][2] * qs * m1, acc[1][3] * qs * m1)};
        u32x4 kf = {pack2(acc[2][0] * m0, acc[2][1] * m0), pack2(acc[2][2] * m0, acc[2][3] * m0),
                    pack2(acc[3][0] * m1, acc[3][1] * m1), pack2(acc[3][2] * m1, acc[3][3] * m1)};
        *(u32x4*)(ws + WS_Q + ((size_t)(RB * 16 + h) * 8 + w) * 1024 + l * 16) = qf;
        *(u32x4*)(smem + w * 1024 + l * 16) = kf;
#pragma unroll
        for (int dt = 0; dt < 2; ++dt) {
            const bool pad = dt == 0 ? (odd && t < 4) : (!odd && t >= 12);
            f32x4 v = acc[4 + dt];
            u32x2 vv = {pack2(v[0], v[1]), pack2(v[2], v[3])};
            if (pad) vv = (u32x2){0x3f803f80u, 0x3f803f80u};
            *(u32x2*)(smem + 8192 + ((w >> 1) * 2 + dt) * 1024 + l * 16 + (w & 1) * 8) = vv;
        }
    }
    __syncthreads();
    char* blob = ws + WS_KV + (size_t)(RB * 16 + h) * 16384;
    *(u32x4*)(blob + tid * 16) = *(const u32x4*)(smem + tid * 16);
    *(u32x4*)(blob + 8192 + tid * 16) = *(const u32x4*)(smem + 8192 + tid * 16);
}

__global__ __launch_bounds__(512, 2) void k_p2(char* __restrict__ ws) {
    extern __shared__ __attribute__((aligned(16))) char smem[];
    const int b = blockIdx.x, xcd = b & 7, loc = b >> 3;
    const int RB = 2 * xcd + (loc >> 4), h = loc & 15;
    const int tid = threadIdx.x, w = __builtin_amdgcn_readfirstlane(tid >> 6), l = tid & 63, t = l & 15, g = l >> 4;
#pragma unroll
    for (int blk = 0; blk < 3; ++blk) {
        const int rb = RB - 2 + blk;
        u32x4 k = {0, 0, 0, 0}, v = {0, 0, 0, 0};
        if (rb >= 0) {
            const char* blob = ws + WS_KV + (size_t)(rb * 16 + h) * 16384;
            k = *(const u32x4*)(blob + tid * 16);
            v = *(const u32x4*)(blob + 8192 + tid * 16);
        }
        *(u32x4*)(smem + blk * 8192 + tid * 16) = k;
        *(u32x4*)(smem + 24576 + blk * 8192 + tid * 16) = v;
    }
    const u32x4 qf = *(const u32x4*)(ws + WS_Q + ((size_t)(RB * 16 + h) * 8 + w) * 1024 + l * 16);
    __syncthreads();
    const int base = w & ~1;
    f32x4 sc[18];
#pragma unroll
    for (int a = 0; a < 18; ++a) {
        u32x4 kf = *(const u32x4*)(smem + (base + a) * 1024 + l * 16);
        sc[a] = mfma16(kf, qf, (f32x4){0.f, 0.f, 0.f, 0.f});
    }
    const float NEG = -1e30f;
    const int qq = 256 + 16 * w + t;
#pragma unroll
    for (int a = 0; a < 2; ++a)
#pragma unroll
        for (int i = 0; i < 4; ++i) { int kk = 16 * (base + a) + 4 * g + i; if (kk <= qq - 256) sc[a][i] = NEG; }
#pragma unroll
    for (int a = 16; a < 18; ++a)
#pragma unroll
        for (int i = 0; i < 4; ++i) { int kk = 16 * (base + a) + 4 * g + i; if (kk > qq) sc[a][i] = NEG; }
    if (RB < 2) {
        const int tmin = 8 * (2 - RB);
#pragma unroll
        for (int a = 0; a < 18; ++a) if (base + a < tmin) sc[a] = (f32x4){NEG, NEG, NEG, NEG};
    }
    float m = NEG;
#pragma unroll
    for (int a = 0; a < 18; ++a) m = fmaxf(fmaxf(fmaxf(sc[a][0], sc[a][1]), fmaxf(sc[a][2], sc[a][3])), m);
    m = fmaxf(m, xor16(m)); m = fmaxf(m, xor32(m));
    f32x4 o0 = {0.f, 0.f, 0.f, 0.f}, o1 = {0.f, 0.f, 0.f, 0.f};
#pragma unroll
    for (int a = 0; a < 9; ++a) {
        u32x4 pf;
        pf[0] = pack2(__builtin_amdgcn_exp2f(sc[2 * a][0] - m), __builtin_amdgcn_exp2f(sc[2 * a][1] - m));
        pf[1] = pack2(__builtin_amdgcn_exp2f(sc[2 * a][2] - m), __builtin_amdgcn_exp2f(sc[2 * a][3] - m));
        pf[2] = pack2(__builtin_amdgcn_exp2f(sc[2 * a + 1][0] - m), __builtin_amdgcn_exp2f(sc[2 * a + 1][1] - m));
        pf[3] = pack2(__builtin_amdgcn_exp2f(sc[2 * a + 1][2] - m), __builtin_amdgcn_exp2f(sc[2 * a + 1][3] - m));
        const u32 vb = 24576 + ((base >> 1) + a) * 2048 + l * 16;
        u32x4 v0 = *(const u32x4*)(smem + vb), v1 = *(const u32x4*)(smem + vb + 1024);
        o0 = mfma16(v0, pf, o0);
        o1 = mfma16(v1, pf, o1);
    }
    const bool odd = h & 1;
    float den = odd ? o0[0] : o1[0];
    den = __shfl(den, odd ? t : (48 + t));
    const float r = 1.f / den;
    const float m0 = (odd && g == 0) ? 0.f : r, m1 = (!odd && g == 3) ? 0.f : r;
    u32x4 of = {pack2(o0[0] * m0, o0[1] * m0), pack2(o0[2] * m0, o0[3] * m0), pack2(o1[0] * m1, o1[1] * m1), pack2(o1[2] * m1, o1[3] * m1)};
    *(u32x4*)(ws + WS_O + ((size_t)(RB * 8 + w) * 16 + h) * 1024 + l * 16) = of;
}

__global__ __launch_bounds__(512, 2) void k_p3(const u16* __restrict__ x, const u16* __restrict__ wo, char* __restrict__ ws) {
    extern __shared__ __attribute__((aligned(16))) char smem[];
    const int b = blockIdx.x, xcd = b & 7, loc = b >> 3;
    if (loc >= 28) return;
    const int rq = loc / 7, cg = loc % 7;
    const int tid = threadIdx.x, w = __builtin_amdgcn_readfirstlane(tid >> 6), l = tid & 63, t = l & 15, g = l >> 4;
    const int tt = w & 3, kh = w >> 2;
    const int TT = 16 * xcd + 4 * rq + tt;
    for (int p = w; p < 56; p += 8) {
        int row = 8 * p + (l >> 3), chp = l & 7;
        int ch = (((chp >> 1) ^ ((row >> 1) & 3)) << 1) | (chp & 1);
        dma16((const char*)wo + row * 896 + cg * 128 + ch * 16, smem + p * 1024);
    }
    u32x4 of[8];
#pragma unroll
    for (int i = 0; i < 8; ++i) of[i] = *(const u32x4*)(ws + WS_O + ((size_t)TT * 16 + 8 * kh + i) * 1024 + l * 16);
    u32x2 xr[2];
#pragma unroll
    for (int c = 0; c < 2; ++c) xr[c] = *(const u32x2*)((const char*)x + (size_t)(16 * TT + t) * 896 + cg * 128 + (2 * kh + c) * 32 + g * 8);
    __syncthreads();
    const int q4 = (l & 15) >> 2, p4 = l & 3;
    const int sw = (2 * g + (q4 >> 1)) & 3;
    f32x4 acc[4];
#pragma unroll
    for (int c = 0; c < 4; ++c) acc[c] = (f32x4){0.f, 0.f, 0.f, 0.f};
#pragma unroll
    for (int i = 0; i < 8; ++i) {
        const int hh = 8 * kh + i;
        const u32 r0 = (28 * hh - 4 * (hh & 1)) * 128;
#pragma unroll
        for (int c = 0; c < 4; ++c) {
            const u32 a = r0 + (4 * g + q4) * 128 + ((c ^ sw) << 5) + 8 * p4;
            acc[c] = mfma16(trfrag(smem, a, a + 2048), of[i], acc[c]);
        }
    }
    char* red = smem + 57344;
    {
        const int o = 2 * (1 - kh);
        *(f32x4*)(red + (w * 2 + 0) * 1024 + l * 16) = kh ? acc[0] : acc[2];
        *(f32x4*)(red + (w * 2 + 1) * 1024 + l * 16) = kh ? acc[1] : acc[3];
        (void)o;
    }
    __syncthreads();
    const int pw = w ^ 4;
    f32x4 r0v = *(const f32x4*)(red + (pw * 2 + 0) * 1024 + l * 16), r1v = *(const f32x4*)(red + (pw * 2 + 1) * 1024 + l * 16);
    f32x4 a0 = (kh ? acc[2] : acc[0]) + r0v, a1 = (kh ? acc[3] : acc[1]) + r1v;
    float hv[8];
    hv[0] = bflo(xr[0][0]) + a0[0]; hv[1] = bfhi(xr[0][0]) + a0[1]; hv[2] = bflo(xr[0][1]) + a0[2]; hv[3] = bfhi(xr[0][1]) + a0[3];
    hv[4] = bflo(xr[1][0]) + a1[0]; hv[5] = bfhi(xr[1][0]) + a1[1]; hv[6] = bflo(xr[1][1]) + a1[2]; hv[7] = bfhi(xr[1][1]) + a1[3];
    u32x4 hf = {pack2(hv[0], hv[1]), pack2(hv[2], hv[3]), pack2(hv[4], hv[5]), pack2(hv[6], hv[7])};
    float sum = 0.f, sq = 0.f;
#pragma unroll
    for (int j = 0; j < 4; ++j) { float a = bflo(hf[j]), c = bfhi(hf[j]); sum += a + c; sq = fmaf(a, a, sq); sq = fmaf(c, c, sq); }
    sum += xor16(sum); sum += xor32(sum); sq += xor16(sq); sq += xor32(sq);
    const int ks = 2 * cg + kh;
    *(u32x4*)(ws + WS_H + ((size_t)TT * 14 + ks) * 1024 + l * 16) = hf;
    if (g == 0) *(f32x2*)(ws + WS_ST + ((size_t)TT * 14 + ks) * 128 + t * 8) = (f32x2){sum, sq};
}

__global__ __launch_bounds__(512, 2) void k_p4(const u16* __restrict__ w1, const u16* __restrict__ b1, char* __restrict__ ws) {
    extern __shared__ __attribute__((aligned(16))) char smem[];
    const int b = blockIdx.x, xcd = b & 7, loc = b >> 3;
    if (loc >= 28) return;
    const int rb2 = loc / 14, cg = loc % 14;
    const int tid = threadIdx.x, w = __builtin_amdgcn_readfirstlane(tid >> 6), l = tid & 63, t = l & 15, g = l >> 4;
    const int TT = 16 * xcd + 8 * rb2 + w;
    for (int p = w; p < 112; p += 8) {
        int row = 4 * p + (l >> 4), chp = l & 15;
        int ch = (((chp >> 1) ^ (row & 7)) << 1) | (chp & 1);
        dma16((const char*)w1 + row * 3584 + cg * 256 + ch * 16, smem + p * 1024);
    }
    u32x4 hf[14];
#pragma unroll
    for (int s = 0; s < 14; ++s) hf[s] = *(const u32x4*)(ws + WS_H + ((size_t)TT * 14 + s) * 1024 + l * 16);
    float sum = 0.f, sq = 0.f;
#pragma unroll
    for (int i = 0; i < 4; ++i) {
        int ks = g + 4 * i;
        if (ks < 14) { f32x2 v = *(const f32x2*)(ws + WS_ST + ((size_t)TT * 14 + ks) * 128 + t * 8); sum += v[0]; sq += v[1]; }
    }
    sum += xor16(sum); sum += xor32(sum); sq += xor16(sq); sq += xor32(sq);
    const float mean = sum * (1.f / 448.f);
    const float rstd = rsqrtf(fmaxf(sq * (1.f / 448.f) - mean * mean, 0.f) + 1e-5f);
    __syncthreads();
    const int q4 = (l & 15) >> 2, p4 = l & 3;
    const int r7 = (4 * g + q4) & 7;
    float* cs = (float*)(smem + 114688);
    {
        f32x4 c4 = {0.f, 0.f, 0.f, 0.f};
        const u32x4 ones = {0x3f803f80u, 0x3f803f80u, 0x3f803f80u, 0x3f803f80u};
#pragma unroll
        for (int s = 0; s < 14; ++s) {
            const u32 a = s * 8192 + (4 * g + q4) * 256 + ((w ^ r7) << 5) + 8 * p4;
            c4 = mfma16(trfrag(smem, a, a + 4096), ones, c4);
        }
        if (t == 0) *(f32x4*)(cs + 16 * w + 4 * g) = c4;
        if (tid < 128) cs[128 + tid] = __uint_as_float((u32)b1[cg * 128 + tid] << 16);
    }
    f32x4 acc[8];
#pragma unroll
    for (int c = 0; c < 8; ++c) acc[c] = (f32x4){0.f, 0.f, 0.f, 0.f};
#pragma unroll
    for (int s = 0; s < 14; ++s)
#pragma unroll
        for (int c = 0; c < 8; ++c) {
            const u32 a = s * 8192 + (4 * g + q4) * 256 + ((c ^ r7) << 5) + 8 * p4;
            acc[c] = mfma16(trfrag(smem, a, a + 4096), hf[s], acc[c]);
        }
    __syncthreads();
    const float mr = -mean * rstd;
#pragma unroll
    for (int a = 0; a < 4; ++a) {
        u32x4 o;
#pragma unroll
        for (int u = 0; u < 2; ++u) {
            const int c = 2 * a + u;
            f32x4 csv = *(const f32x4*)(cs + 16 * c + 4 * g), bv = *(const f32x4*)(cs + 128 + 16 * c + 4 * g);
            float v[4];
#pragma unroll
            for (int i = 0; i < 4; ++i) { float z = fmaf(acc[c][i], rstd, fmaf(csv[i], mr, bv[i])); z = fmaxf(z, 0.f); v[i] = z * z; }
            o[2 * u] = pack2(v[0], v[1]); o[2 * u + 1] = pack2(v[2], v[3]);
        }
        *(u32x4*)(ws + WS_HH + ((size_t)TT * 56 + 4 * cg + a) * 1024 + l * 16) = o;
    }
}

__global__ __launch_bounds__(512, 2) void k_p5(const u16* __restrict__ w2, const u16* __restrict__ b2, char* __restrict__ ws, u16* __restrict__ out) {
    extern __shared__ __attribute__((aligned(16))) char smem[];
    const int b = blockIdx.x, xcd = b & 7, loc = b >> 3;
    if (loc >= 28) return;
    const int rq = loc / 7, cg = loc % 7;
    const int tid = threadIdx.x, w = __builtin_amdgcn_readfirstlane(tid >> 6), l = tid & 63, t = l & 15, g = l >> 4;
    const int tt = w & 3, kh = w >> 2;
    const int TT = 16 * xcd + 4 * rq + tt;
    const int q4 = (l & 15) >> 2, p4 = l & 3;
    const int sw = (2 * g + (q4 >> 1)) & 3;
    f32x4 acc[4];
#pragma unroll
    for (int c = 0; c < 4; ++c) acc[c] = (f32x4){0.f, 0.f, 0.f, 0.f};
    for (int rnd = 0; rnd < 2; ++rnd) {
        if (rnd) __syncthreads();
        for (int p = w; p < 112; p += 8) {
            int half = p / 56, lp = p % 56;
            int lrow = 8 * lp + (l >> 3), chp = l & 7;
            int row = half * 896 + rnd * 448 + lrow;
            int ch = (((chp >> 1) ^ ((lrow >> 1) & 3)) << 1) | (chp & 1);
            dma16((const char*)w2 + (size_t)row * 896 + cg * 128 + ch * 16, smem + p * 1024);
        }
        u32x4 hf[14];
#pragma unroll
        for (int s = 0; s < 14; ++s) hf[s] = *(const u32x4*)(ws + WS_HH + ((size_t)TT * 56 + 28 * kh + 14 * rnd + s) * 1024 + l * 16);
        __syncthreads();
#pragma unroll
        for (int s = 0; s < 14; ++s)
#pragma unroll
            for (int c = 0; c < 4; ++c) {
                const u32 a = kh * 57344 + s * 4096 + (4 * g + q4) * 128 + ((c ^ sw) << 5) + 8 * p4;
                acc[c] = mfma16(trfrag(smem, a, a + 2048), hf[s], acc[c]);
            }
    }
    char* red = smem + 114688;
    *(f32x4*)(red + (w * 2 + 0) * 1024 + l * 16) = kh ? acc[0] : acc[2];
    *(f32x4*)(red + (w * 2 + 1) * 1024 + l * 16) = kh ? acc[1] : acc[3];
    __syncthreads();
    const int pw = w ^ 4;
    f32x4 r0v = *(const f32x4*)(red + (pw * 2 + 0) * 1024 + l * 16), r1v = *(const f32x4*)(red + (pw * 2 + 1) * 1024 + l * 16);
    f32x4 a0 = (kh ? acc[2] : acc[0]) + r0v, a1 = (kh ? acc[3] : acc[1]) + r1v;
    const u32x4 hr = *(const u32x4*)(ws + WS_H + ((size_t)TT * 14 + 2 * cg + kh) * 1024 + l * 16);
    const u32x2 bb0 = *(const u32x2*)((const char*)b2 + cg * 128 + (2 * kh) * 32 + g * 8);
    const u32x2 bb1 = *(const u32x2*)((const char*)b2 + cg * 128 + (2 * kh + 1) * 32 + g * 8);
    u32x2 o0 = {pack2(a0[0] + bflo(bb0[0]) + bflo(hr[0]), a0[1] + bfhi(bb0[0]) + bfhi(hr[0])),
                pack2(a0[2] + bflo(bb0[1]) + bflo(hr[1]), a0[3] + bfhi(bb0[1]) + bfhi(hr[1]))};
    u32x2 o1 = {pack2(a1[0] + bflo(bb1[0]) + bflo(hr[2]), a1[1] + bfhi(bb1[0]) + bfhi(hr[2])),
                pack2(a1[2] + bflo(bb1[1]) + bflo(hr[3]), a1[3] + bfhi(bb1[1]) + bfhi(hr[3]))};
    char* orow = (char*)out + (size_t)(16 * TT + t) * 896 + cg * 128 + (2 * kh) * 32 + g * 8;
    *(u32x2*)(orow) = o0;
    *(u32x2*)(orow + 32) = o1;
}

extern "C" void kernel_launch(void* const* d_in, const int* in_sizes, int n_in, void* d_out, int out_size, void* d_ws, size_t ws_size,
                              hipStream_t stream) {
    const u16* x = (const u16*)d_in[0];
    const u16* wqkv = (const u16*)d_in[1];
    const u16* wo = (const u16*)d_in[2];
    const u16* w1 = (const u16*)d_in[4];
    const u16* b1 = (const u16*)d_in[5];
    const u16* w2 = (const u16*)d_in[6];
    const u16* b2 = (const u16*)d_in[7];
    char* ws = (char*)d_ws;
    {
        (void)hipFuncSetAttribute((const void*)k_p1, hipFuncAttributeMaxDynamicSharedMemorySize, 86016);
        (void)hipFuncSetAttribute((const void*)k_p2, hipFuncAttributeMaxDynamicSharedMemorySize, 49152);
        (void)hipFuncSetAttribute((const void*)k_p3, hipFuncAttributeMaxDynamicSharedMemorySize, 73728);
        (void)hipFuncSetAttribute((const void*)k_p4, hipFuncAttributeMaxDynamicSharedMemorySize, 115712);
        (void)hipFuncSetAttribute((const void*)k_p5, hipFuncAttributeMaxDynamicSharedMemorySize, 131072);
    }
    k_p1<<<256, 512, 86016, stream>>>(x, wqkv, ws);
    k_p2<<<256, 512, 49152, stream>>>(ws);
    k_p3<<<256, 512, 73728, stream>>>(x, wo, ws);
    k_p4<<<256, 512, 115712, stream>>>(w1, b1, ws);
    k_p5<<<256, 512, 131072, stream>>>(w2, b2, ws, (u16*)d_out);
}
```

```cpp
#include <hip/hip_runtime.h>
#include <stdint.h>

typedef unsigned short u16;
typedef unsigned int u32;
typedef __attribute__((ext_vector_type(8))) __bf16 bf16x8;
typedef __attribute__((ext_vector_type(2))) __bf16 bf16x2;
typedef __attribute__((ext_vector_type(4))) short s16x4;
typedef __attribute__((ext_vector_type(4))) float f32x4;
typedef __attribute__((ext_vector_type(2))) float f32x2;
typedef __attribute__((ext_vector_type(4))) u32 u32x4;
typedef __attribute__((ext_vector_type(2))) u32 u32x2;
#define LAS __attribute__((address_space(3)))
#define GAS __attribute__((address_space(1)))
#define DI __device__ __forceinline__

#define FSTRIDE 64
constexpr u32 FGRP = 16 * FSTRIDE < 128 ? 128 : 16 * FSTRIDE;
constexpr u32 WS_GEN = 0;
constexpr u32 WS_FW = 4096;
constexpr u32 WS_F1 = WS_FW + 448 * 64;
constexpr u32 WS_F2 = WS_F1 + 256 * FGRP;
constexpr u32 WS_F3 = WS_F2 + 128 * FGRP;
constexpr u32 WS_F4 = WS_F3 + 128 * FGRP;
constexpr u32 WS_KV = WS_F4 + 16 * FGRP;
constexpr u32 WS_O = WS_KV + 16 * 16 * 16384;
constexpr u32 WS_H = WS_O + 128 * 16 * 1024;
constexpr u32 WS_ST = WS_H + 128 * 14 * 1024;
constexpr u32 WS_HH = WS_ST + 128 * 7 * 128;
constexpr u32 WS_W1F = WS_HH + 128 * 56 * 1024;
constexpr u32 WS_W2F = WS_W1F + 112 * 14 * 1024;
constexpr u32 WS_WOF = WS_W2F + 28 * 56 * 1024;
constexpr u32 WS_END = WS_WOF + 28 * 16 * 1024;
constexpr u32 L_WA = 114688;
constexpr u32 L_GEN = 157696;
constexpr u32 L_ST1 = 157952;
constexpr u32 L_CS1 = 158976;
constexpr u32 L_Q = 49152;
constexpr u32 L_PP = 98304;
constexpr u32 L_K = 0, L_V = 24576;
constexpr u32 L_RED3 = 65536;
constexpr u32 L_ST4 = 114688;
constexpr u32 L_H5 = 81920;
constexpr u32 L_RED5 = 32768;
constexpr u32 LDS_BYTES = 163840;
constexpr int SPIN_MAX = 1 << 14;

DI f32x4 mfma16(u32x4 a, u32x4 b, f32x4 c) {
    return __builtin_amdgcn_mfma_f32_16x16x32_bf16(__builtin_bit_cast(bf16x8, a), __builtin_bit_cast(bf16x8, b), c, 0, 0, 0);
}
DI void mfma_acc(f32x4& acc, const u32x4& a, const u32x4& b) {
    asm volatile("v_mfma_f32_16x16x32_bf16 %0, %1, %2, %0" : "+v"(acc) : "v"(a), "v"(b));
}
DI void mfma_first(f32x4& acc, const u32x4& a, const u32x4& b) {
    asm volatile("v_mfma_f32_16x16x32_bf16 %0, %1, %2, 0" : "=&v"(acc) : "v"(a), "v"(b));
}
#define MFMA_FENCE4(a) asm volatile("s_nop 7\n\ts_nop 1" : "+v"(a[0]), "+v"(a[1]), "+v"(a[2]), "+v"(a[3]))
#define OPAQUE(x) asm volatile("" : "+v"(x))
DI u32 pack2(float lo, float hi) {
    f32x2 v = {lo, hi};
    bf16x2 r = __builtin_convertvector(v, bf16x2);
    return __builtin_bit_cast(u32, r);
}
DI float dot2bf(u32 a, u32 b, float c) {
    return __builtin_amdgcn_fdot2_f32_bf16(__builtin_bit_cast(bf16x2, a), __builtin_bit_cast(bf16x2, b), c, false);
}
DI float bflo(u32 u) { return __uint_as_float(u << 16); }
DI float bfhi(u32 u) { return __uint_as_float(u & 0xffff0000u); }
DI u32x2 trread(u32 off) {
    s16x4 r = __builtin_amdgcn_ds_read_tr16_b64_v4i16((LAS s16x4*)(uintptr_t)off);
    return __builtin_bit_cast(u32x2, r);
}
DI u32x4 trfrag(u32 off0, u32 off1) {
    u32x2 a = trread(off0), b = trread(off1);
    u32x4 r = {a[0], a[1], b[0], b[1]};
    return r;
}
DI u32x4 lds_ld128(u32 off) { return *(LAS u32x4*)(uintptr_t)off; }
DI f32x4 lds_ldf4(u32 off) { return *(LAS f32x4*)(uintptr_t)off; }
DI void lds_st128(u32 off, u32x4 v) { *(LAS u32x4*)(uintptr_t)off = v; }
DI void lds_stf4(u32 off, f32x4 v) { *(LAS f32x4*)(uintptr_t)off = v; }
DI void lds_st64(u32 off, u32x2 v) { *(LAS u32x2*)(uintptr_t)off = v; }
DI void lds_stf(u32 off, float v) { *(LAS float*)(uintptr_t)off = v; }
template <int N>
DI void glds16_run(const void* sbase, u32 voff, u32 lds, u32 gstep, u32 lstep) {
    u32 keep;
    asm volatile("s_mov_b32 %0, m0\n\ts_mov_b32 m0, %3\n\t"
                 ".rept %c6\n\t"
                 "s_nop 0\n\t"
                 "global_load_lds_dwordx4 %1, %2\n\t"
                 "s_add_u32 m0, m0, %5\n\t"
                 "v_add_u32 %1, %4, %1\n\t"
                 ".endr\n\t"
                 "s_mov_b32 m0, %0"
                 : "=&s"(keep), "+v"(voff) : "s"(sbase), "s"(lds), "s"(gstep), "s"(lstep), "i"(N) : "memory", "scc");
}
template <int N>
DI void glds16_run_sc1(const void* sbase, u32 voff, u32 lds, u32 gstep, u32 lstep) {
    u32 keep;
    asm volatile("s_mov_b32 %0, m0\n\ts_mov_b32 m0, %3\n\t"
                 ".rept %c6\n\t"
                 "s_nop 0\n\t"
                 "global_load_lds_dwordx4 %1, %2 sc1\n\t"
                 "s_add_u32 m0, m0, %5\n\t"
                 "v_add_u32 %1, %4, %1\n\t"
                 ".endr\n\t"
                 "s_mov_b32 m0, %0"
                 : "=&s"(keep), "+v"(voff) : "s"(sbase), "s"(lds), "s"(gstep), "s"(lstep), "i"(N) : "memory", "scc");
}
template <int N>
DI void glds4_run(const void* sbase, u32 voff, u32 lds, u32 gstep) {
    u32 keep;
    asm volatile("s_mov_b32 %0, m0\n\ts_mov_b32 m0, %3\n\ts_nop 0\n\t"
                 ".rept %c5\n\t"
                 "global_load_lds_dword %1, %2\n\t"
                 "v_add_u32 %1, %4, %1\n\t"
                 ".endr\n\t"
                 "s_mov_b32 m0, %0"
                 : "=&s"(keep), "+v"(voff) : "s"(sbase), "s"(lds), "s"(gstep), "i"(N) : "memory");
}
#define SB() __builtin_amdgcn_sched_barrier(0)
#define WAIT_VM(n) asm volatile("s_waitcnt vmcnt(" #n ")" ::: "memory")
#define BAR() asm volatile("s_waitcnt lgkmcnt(0)\n\ts_barrier" ::: "memory")
DI float xor16(float v) { return __shfl_xor(v, 16); }
DI float xor32(float v) { return __shfl_xor(v, 32); }

typedef __amdgpu_buffer_rsrc_t rsrc_t;
DI u32x4 ld_sc1(rsrc_t r, u32 off) { return __builtin_amdgcn_raw_buffer_load_b128(r, off, 0, 16); }
DI u32x2 ld_sc1_64(rsrc_t r, u32 off) { return __builtin_amdgcn_raw_buffer_load_b64(r, off, 0, 16); }
DI void st_sc1(rsrc_t r, u32 off, u32x4 v) { __builtin_amdgcn_raw_buffer_store_b128(v, r, off, 0, 16); }
DI void st_sc1_64(rsrc_t r, u32 off, u32x2 v) { __builtin_amdgcn_raw_buffer_store_b64(v, r, off, 0, 16); }
DI void set_flag(char* ws, u32 off, u32 tag, int lane) {
    if (lane == 0) __hip_atomic_store((GAS u32*)(ws + off), tag, __ATOMIC_RELAXED, __HIP_MEMORY_SCOPE_AGENT);
}
DI void wait_flags(char* ws, u32 off, u32 tag) {
    for (int it = 0; it < SPIN_MAX; ++it) {
        u32 v = __hip_atomic_load((GAS u32*)(ws + off), __ATOMIC_RELAXED, __HIP_MEMORY_SCOPE_AGENT);
        if (__all(v == tag)) break;
        __builtin_amdgcn_s_sleep(1);
    }
    asm volatile("" ::: "memory");
}

#define STAMP_DECL do {} while (0)
#define STAMP(I) do {} while (0)
#define STAMP_FLUSH() do {} while (0)

__global__ __launch_bounds__(512, 2) __attribute__((amdgpu_waves_per_eu(2, 2)))
void fused_kernel(const u16* __restrict__ x, const u16* __restrict__ wqkv, const u16* __restrict__ wo, const u16* __restrict__ w1,
                  const u16* __restrict__ b1, const u16* __restrict__ w2, const u16* __restrict__ b2, u16* __restrict__ out,
                  char* __restrict__ ws) {
    const int b = blockIdx.x, xcd = b & 7, loc = b >> 3;
    const int tid = threadIdx.x, w = __builtin_amdgcn_readfirstlane(tid >> 6), l = tid & 63, t = l & 15, g = l >> 4;
    const int q4 = t >> 2, p4 = l & 3;
    const rsrc_t rs = __builtin_amdgcn_make_buffer_rsrc(ws, 0, WS_END, 0x00020000);
    STAMP_DECL;
    STAMP(0);
    const int RB = 4 * (xcd >> 1) + (loc >> 3), h = 8 * (xcd & 1) + (loc & 7);
    const bool odd = h & 1;
    const int tp = w >> 1, ch = w & 1;
    const char* wqb = (const char*)wqkv + 56 * h - (h & 1) * 8;
    const u32 wqv = (l >> 2) * 2688 + (((l & 3) ^ ((l >> 5) << 1)) * 16);
    {
        const char* xb = (const char*)x + (size_t)(128 * RB + 16 * w) * 896;
        glds16_run<14>(xb, (l >> 2) * 896 + (((l & 3) ^ ((-(l >> 4)) & 3)) * 16), w * 14336, 64, 1024);
        asm volatile("s_barrier" ::: "memory");
        if (w < 7) {
#pragma unroll
            for (int hf = 0; hf < 2; ++hf) glds16_run<3>(wqb + (32 * w + 16 * hf) * 2688, wqv, L_WA + w * 6144 + hf * 1024, 896, 2048);
        } else {
            asm volatile("s_mov_b32 m0, %2\n\ts_nop 0\n\tglobal_load_lds_dword %0, %1 sc1" :: "v"(0), "s"(ws + WS_GEN), "s"(L_GEN) : "memory");
            WAIT_VM(1);
        }
        WAIT_VM(6);
        BAR();
    }
    STAMP(1);
    u32x4 xf[2][14];
    {
        const u32 xl = t * 64 + ((g ^ ((-(t >> 2)) & 3)) * 16);
        const u32 xa = w * 14336 + xl, xb2 = (w ^ 1) * 14336 + xl;
#pragma unroll
        for (int s = 0; s < 14; ++s) { xf[0][s] = lds_ld128(xa + s * 1024); xf[1][s] = lds_ld128(xb2 + s * 1024); }
    }
    const u32 padoff = (l >> 5) * 2048 + (l & 31) * 64 + ((((odd ? 0 : 1) ^ ((l >> 3) & 1)) * 32) + (odd ? 0 : 24));
    WAIT_VM(0);
    if (w < 7) lds_st64(L_WA + w * 6144 + padoff, (u32x2){0x3f803f80u, 0x3f803f80u});
    BAR();
    const u32 tag = __builtin_amdgcn_readfirstlane(*(LAS u32*)(uintptr_t)L_GEN) + 1u;
    STAMP(2);
    if (w < 7) {
#pragma unroll
        for (int hf = 0; hf < 2; ++hf) glds16_run<3>(wqb + (32 * (7 + w) + 16 * hf) * 2688, wqv, w * 14336 + hf * 1024, 896, 2048);
    }
    const int pp = b + 256 * (w >> 2);
    {
        const char* src; u32 pitch;
        if (pp < 196) { src = (const char*)w1 + (pp / 7) * 128 + (size_t)(pp % 7) * (64 * 3584); pitch = 3584; }
        else if (pp < 392) { const int i = pp - 196; src = (const char*)w2 + (i / 28) * 128 + (size_t)(i % 28) * (64 * 896); pitch = 896; }
        else { const int i = pp - 392; src = (const char*)wo + (i >> 3) * 128 + (size_t)(i & 7) * (56 * 896); pitch = 896; }
        const int j = w & 3;
        const u32 r0 = (pp >= 392 && j == 3) ? 0u : 8u;
        if (pp < 448) glds16_run<2>(src + (size_t)(16 * j) * pitch, (l >> 3) * pitch + (l & 7) * 16, L_PP + (w >> 2) * 8192 + j * 2048, r0 * pitch, r0 * 128);
    }
    SB();
    {
        f32x4 acc[2][3], cs[3];
        u32 lbA[2], lbB[2], lbC[2];
        {
            const u32 h0 = (8 * g + q4) * 64 + ((g & 1) * 32) + p4 * 8, h1 = (8 * g + q4) * 64 + (((g & 1) ^ 1) * 32) + p4 * 8;
            lbA[0] = h0 + ch * 2048; lbA[1] = h1 + ch * 2048;
            lbB[0] = (ch ? h1 : h0) + 4096;
        }
        u32 b1[3] = {lbA[0] + L_WA, lbA[1] + L_WA, lbB[0] + L_WA};
        u32 b2[3] = {lbA[0], lbA[1], lbB[0]};
        u32 b3[3] = {lbA[0] + 57344, lbA[1] + 57344, lbB[0] + 57344};
        OPAQUE(b1[0]); OPAQUE(b1[1]); OPAQUE(b1[2]); OPAQUE(b2[0]); OPAQUE(b2[1]); OPAQUE(b2[2]); OPAQUE(b3[0]); OPAQUE(b3[1]); OPAQUE(b3[2]);
        (void)lbC;
#define WQ_ADDR(s, c) ((s) < 7 ? b1[c] + (s) * 6144 : (s) < 11 ? b2[c] + ((s) - 7) * 14336 : b3[c] + ((s) - 11) * 14336)
        const u32x4 ones = {0x3f803f80u, 0x3f803f80u, 0x3f803f80u, 0x3f803f80u};
        f32x4 gram;
        u32x4 wf[2][3];
#pragma unroll
        for (int c = 0; c < 3; ++c) { const u32 a = WQ_ADDR(0, c); wf[0][c] = trfrag(a, a + 256); }
#pragma unroll
        for (int c = 0; c < 3; ++c) cs[c] = (f32x4){0.f, 0.f, 0.f, 0.f};
        SB();
#pragma unroll
        for (int s = 0; s < 14; ++s) {
            if (s == 6) {
                WAIT_VM(0);
                if (w < 7) lds_st64(w * 14336 + padoff, (u32x2){0x3f803f80u, 0x3f803f80u});
                BAR();
            }
#pragma unroll
            for (int i = 0; i < 6; ++i) {
                const int c = i >> 1, tt = i & 1;
                if (c < 2) { if (s == 0) mfma_first(acc[tt][c], wf[s & 1][c], xf[tt][s]); else mfma_acc(acc[tt][c], wf[s & 1][c], xf[tt][s]); }
                else { if (s == 0) mfma_first(acc[tt][c], xf[tt][s], wf[s & 1][c]); else mfma_acc(acc[tt][c], xf[tt][s], wf[s & 1][c]); }
                if (s + 1 < 14) {
                    const u32x2 r = trread(WQ_ADDR(s + 1, c) + tt * 256);
                    wf[(s + 1) & 1][c][2 * tt] = r[0]; wf[(s + 1) & 1][c][2 * tt + 1] = r[1];
                }
                if (i == 5) { if (s == 0) mfma_first(gram, xf[0][s], xf[0][s]); else mfma_acc(gram, xf[0][s], xf[0][s]); }
                SB();
            }
            if ((s == 8 || s == 9) && pp < 448) {
                const int kk = s - 8, c = w & 3;
                const u32 lb_ = L_PP + (w >> 2) * 8192 + c * 32 + 8 * p4;
                if (pp < 392) {
                    const u32 a = lb_ + (32 * kk + 8 * g + q4) * 128;
                    const u32 dst = pp < 196 ? WS_W1F + (u32)((((pp / 7) >> 1) * 8 + ((pp / 7) & 1) * 4 + c) * 14 + 2 * (pp % 7) + kk) * 1024
                                             : WS_W2F + (u32)((((pp - 196) / 28) * 4 + c) * 56 + 2 * ((pp - 196) % 28) + kk) * 1024;
                    st_sc1(rs, dst + l * 16, trfrag(a, a + 512));
                } else {
                    const u32 a = lb_ + (24 * kk + 4 * g + q4) * 128;
                    st_sc1(rs, WS_WOF + (u32)((((pp - 392) >> 3) * 4 + c) * 16 + 2 * ((pp - 392) & 7) + kk) * 1024 + l * 16, trfrag(a, a + 2048));
                }
            }
            if ((s & 3) == tp) {
                cs[0] = mfma16(wf[s & 1][0], ones, cs[0]); cs[1] = mfma16(wf[s & 1][1], ones, cs[1]); cs[2] = mfma16(ones, wf[s & 1][2], cs[2]);
            }
            SB();
        }
        asm volatile("s_nop 7\n\ts_nop 1" : "+v"(acc[0][0]), "+v"(acc[0][1]), "+v"(acc[0][2]), "+v"(acc[1][0]), "+v"(acc[1][1]), "+v"(acc[1][2]), "+v"(gram));
        STAMP(3);
        {
            const float sum = __shfl(odd ? acc[0][0][0] : acc[0][1][0], odd ? t : 48 + t);
            const float dg = (t & 2) ? ((t & 1) ? gram[3] : gram[2]) : ((t & 1) ? gram[1] : gram[0]);
            const float sq = __shfl(dg, 16 * (t >> 2) + t);
            const float mean = sum * (1.f / 448.f);
            const float rstd = rsqrtf(fmaxf(sq * (1.f / 448.f) - mean * mean, 0.f) + 1e-5f);
            if (g == 0) lds_st64(L_ST1 + (16 * w + t) * 8, (u32x2){__float_as_uint(rstd), __float_as_uint(-mean * rstd)});
            if (t == 0) { lds_stf4(L_CS1 + ((tp * 6 + ch * 3 + 0) * 16 + 4 * g) * 4, cs[0]); lds_stf4(L_CS1 + ((tp * 6 + ch * 3 + 1) * 16 + 4 * g) * 4, cs[1]); }
            if (g == 0) lds_stf(L_CS1 + ((tp * 6 + ch * 3 + 2) * 16 + t) * 4, cs[2][0]);
        }
        BAR();
        STAMP(4);
        f32x4 csum[2]; float csv = 0.f;
        csum[0] = (f32x4){0.f, 0.f, 0.f, 0.f}; csum[1] = csum[0];
#pragma unroll
        for (int p = 0; p < 4; ++p) {
            csum[0] += lds_ldf4(L_CS1 + ((p * 6 + ch * 3 + 0) * 16 + 4 * g) * 4);
            csum[1] += lds_ldf4(L_CS1 + ((p * 6 + ch * 3 + 1) * 16 + 4 * g) * 4);
            csv += *(LAS float*)(uintptr_t)(L_CS1 + ((p * 6 + ch * 3 + 2) * 16 + t) * 4);
        }
        const float qs = ch ? 1.f : 0.18898223650461362f * 1.4426950408889634f;
        const float m0 = (odd && g == 0) ? 0.f : qs, m1 = (!odd && g == 3) ? 0.f : qs;
        const bool vpad = ch == 0 ? (odd && t < 4) : (!odd && t >= 12);
        const u32 blob = WS_KV + (u32)(RB * 16 + h) * 16384;
        u32x2 vv[2];
#pragma unroll
        for (int tt = 0; tt < 2; ++tt) {
            const int tile = w ^ tt;
            const u32x2 st = *(LAS u32x2*)(uintptr_t)(L_ST1 + (16 * tile + t) * 8);
            const float rstd = __uint_as_float(st[0]), mr = __uint_as_float(st[1]);
            float a[8];
#pragma unroll
            for (int i = 0; i < 4; ++i) { a[i] = fmaf(acc[tt][0][i], rstd, csum[0][i] * mr) * m0; a[4 + i] = fmaf(acc[tt][1][i], rstd, csum[1][i] * mr) * m1; }
            const u32x4 f = {pack2(a[0], a[1]), pack2(a[2], a[3]), pack2(a[4], a[5]), pack2(a[6], a[7])};
            if (ch) { st_sc1(rs, blob + tile * 1024 + l * 16, f); lds_st128(L_K + (16 + tile) * 1024 + l * 16, f); }
            else lds_st128(L_Q + tile * 1024 + l * 16, f);
            const u32x4 sa = lds_ld128(L_ST1 + (16 * tile + 4 * g) * 8), sb = lds_ld128(L_ST1 + (16 * tile + 4 * g + 2) * 8);
            const float v0 = fmaf(acc[tt][2][0], __uint_as_float(sa[0]), csv * __uint_as_float(sa[1]));
            const float v1 = fmaf(acc[tt][2][1], __uint_as_float(sa[2]), csv * __uint_as_float(sa[3]));
            const float v2 = fmaf(acc[tt][2][2], __uint_as_float(sb[0]), csv * __uint_as_float(sb[1]));
            const float v3 = fmaf(acc[tt][2][3], __uint_as_float(sb[2]), csv * __uint_as_float(sb[3]));
            vv[tt] = (u32x2){pack2(v0, v1), pack2(v2, v3)};
            if (vpad) vv[tt] = (u32x2){0x3f803f80u, 0x3f803f80u};
        }
        const u32x4 vfr = ch ? (u32x4){vv[1][0], vv[1][1], vv[0][0], vv[0][1]} : (u32x4){vv[0][0], vv[0][1], vv[1][0], vv[1][1]};
        st_sc1(rs, blob + 8192 + (tp * 2 + ch) * 1024 + l * 16, vfr);
        lds_st128(L_V + ((8 + tp) * 2 + ch) * 1024 + l * 16, vfr);
    }
    {
        WAIT_VM(0);
        set_flag(ws, WS_F1 + (u32)(RB * 16 + h) * FGRP + w * FSTRIDE, tag, l);
        BAR();
        if ((tid & 255) == 0 && pp < 448) __hip_atomic_store((GAS u32*)(ws + WS_FW + pp * 64), tag, __ATOMIC_RELAXED, __HIP_MEMORY_SCOPE_AGENT);
    }
    const u32x4 qf = lds_ld128(L_Q + w * 1024 + l * 16);
    STAMP(5);
    SB();
    const int cg4 = 7 * (xcd & 1) + loc % 7, RB4 = 4 * (xcd >> 1) + loc / 7;
    const int rq = loc / 7, cg = loc % 7;
    const int cw = w & 3, kh = w >> 2;
    const bool worker = loc < 28;
    {
        const int rbl = RB - 2 + (l & 1);
        const int rsafe = rbl >= 0 ? rbl : RB;
        wait_flags(ws, WS_F1 + (u32)(rsafe * 16 + h) * FGRP + ((l & 2) ? (w | 1) : w) * FSTRIDE, tag);
        STAMP(6);
#pragma unroll
        for (int blk = 0; blk < 2; ++blk) {
            const int rb = RB - 2 + blk;
            if (rb >= 0) glds16_run_sc1<2>(ws + WS_KV + (size_t)(rb * 16 + h) * 16384 + w * 1024, l * 16, L_K + blk * 8192 + w * 1024, 8192, L_V - L_K);
            else { lds_st128(L_K + blk * 8192 + tid * 16, (u32x4){0, 0, 0, 0}); lds_st128(L_V + blk * 8192 + tid * 16, (u32x4){0, 0, 0, 0}); }
        }
        WAIT_VM(0);
    }
    BAR();
    STAMP(7);
    u32 fwo;
    {
        const int cgs = worker ? cg : 0, cg4s = worker ? cg4 : 0;
        int pi = 392 + cgs * 8 + 4 * kh + (l & 3);
        if (l >= 4 && l < 11) pi = (2 * cg4s + (w >> 2)) * 7 + (l - 4);
        if (l >= 11 && l < 25) pi = 196 + cgs * 28 + 14 * kh + (l - 11);
        fwo = WS_FW + (u32)pi * 64;
    }
    const u32 fwv = __hip_atomic_load((GAS u32*)(ws + fwo), __ATOMIC_RELAXED, __HIP_MEMORY_SCOPE_AGENT);
    SB();
    u32x4 wof[8];
    u32x4 w1f[14];
    const u32 wofa = WS_WOF + (u32)(((worker ? cg : 0) * 4 + cw) * 16 + 8 * kh) * 1024 + l * 16;
    const u32 w1fa = WS_W1F + (u32)(((worker ? cg4 : 0) * 8 + w) * 14) * 1024 + l * 16;
#define PF2(i) do { if ((i) < 8) wof[(i) < 8 ? (i) : 0] = ld_sc1(rs, wofa + (i) * 1024); else w1f[(i) >= 8 ? (i) - 8 : 0] = ld_sc1(rs, w1fa + ((i) - 8) * 1024); } while (0)
    {
        const int base = w & ~1;
        f32x4 sc[18];
        {
            u32x4 kf[18];
#pragma unroll
            for (int a = 0; a < 18; ++a) kf[a] = lds_ld128(L_K + (base + a) * 1024 + l * 16);
            SB();
#pragma unroll
            for (int a = 0; a < 18; ++a) {
                sc[a] = mfma16(kf[a], qf, (f32x4){0.f, 0.f, 0.f, 0.f});
                SB();
            }
        }
        if (!__all(fwv == tag)) wait_flags(ws, fwo, tag);
        asm volatile("" ::: "memory");
        PF2(0); PF2(1);
        SB();
        u32x4 vf[18];
#pragma unroll
        for (int a = 0; a < 8; ++a) vf[a] = lds_ld128(L_V + (base >> 1) * 2048 + a * 1024 + l * 16);
        SB();
        const float NEG = -1e30f;
        const int qq = 256 + 16 * w + t;
#pragma unroll
        for (int a = 0; a < 2; ++a)
#pragma unroll
            for (int i = 0; i < 4; ++i) { const int kk = 16 * (base + a) + 4 * g + i; if (kk <= qq - 256) sc[a][i] = NEG; }
#pragma unroll
        for (int a = 16; a < 18; ++a)
#pragma unroll
            for (int i = 0; i < 4; ++i) { const int kk = 16 * (base + a) + 4 * g + i; if (kk > qq) sc[a][i] = NEG; }
        if (RB < 2) {
            asm volatile("" ::: "memory");
            const int tmin = 8 * (2 - RB);
#pragma unroll
            for (int a = 0; a < 18; ++a) if (base + a < tmin) sc[a] = (f32x4){NEG, NEG, NEG, NEG};
        }
        PF2(2); PF2(3);
        SB();
        float ma = NEG, mb = NEG;
#pragma unroll
        for (int a = 0; a < 18; a += 2) {
            ma = fmaxf(fmaxf(sc[a][0], sc[a][1]), ma); ma = fmaxf(fmaxf(sc[a][2], sc[a][3]), ma);
            mb = fmaxf(fmaxf(sc[a + 1][0], sc[a + 1][1]), mb); mb = fmaxf(fmaxf(sc[a + 1][2], sc[a + 1][3]), mb);
        }
        PF2(4); PF2(5);
        SB();
        float m = fmaxf(ma, mb);
        m = fmaxf(m, xor16(m)); m = fmaxf(m, xor32(m));
        f32x4 o0 = {0.f, 0.f, 0.f, 0.f}, o1 = {0.f, 0.f, 0.f, 0.f};
#pragma unroll
        for (int a = 0; a < 9; ++a) {
            if (2 * a + 8 < 18) {
                vf[2 * a + 8] = lds_ld128(L_V + (base >> 1) * 2048 + (2 * a + 8) * 1024 + l * 16);
                vf[2 * a + 9] = lds_ld128(L_V + (base >> 1) * 2048 + (2 * a + 9) * 1024 + l * 16);
            }
            if (a < 8) { PF2(6 + 2 * a); PF2(7 + 2 * a); }
            u32x4 pf;
            pf[0] = pack2(__builtin_amdgcn_exp2f(sc[2 * a][0] - m), __builtin_amdgcn_exp2f(sc[2 * a][1] - m));
            pf[1] = pack2(__builtin_amdgcn_exp2f(sc[2 * a][2] - m), __builtin_amdgcn_exp2f(sc[2 * a][3] - m));
            pf[2] = pack2(__builtin_amdgcn_exp2f(sc[2 * a + 1][0] - m), __builtin_amdgcn_exp2f(sc[2 * a + 1][1] - m));
            pf[3] = pack2(__builtin_amdgcn_exp2f(sc[2 * a + 1][2] - m), __builtin_amdgcn_exp2f(sc[2 * a + 1][3] - m));
            o0 = mfma16(vf[2 * a], pf, o0);
            o1 = mfma16(vf[2 * a + 1], pf, o1);
            SB();
        }
        float den = odd ? o0[0] : o1[0];
        den = __shfl(den, odd ? t : (48 + t));
        const float r = 1.f / den;
        const float m0 = (odd && g == 0) ? 0.f : r, m1 = (!odd && g == 3) ? 0.f : r;
        const u32x4 of = {pack2(o0[0] * m0, o0[1] * m0), pack2(o0[2] * m0, o0[3] * m0), pack2(o1[0] * m1, o1[1] * m1), pack2(o1[2] * m1, o1[3] * m1)};
        STAMP(8);
        st_sc1(rs, WS_O + (u32)((RB * 8 + w) * 16 + h) * 1024 + l * 16, of);
        WAIT_VM(0);
        set_flag(ws, WS_F2 + (u32)(RB * 8 + w) * FGRP + h * FSTRIDE, tag, l);
    }
    STAMP(9);
    SB();
    if (!worker) {
        if (b == 255) {
#pragma unroll
            for (int i = 0; i < 4; ++i) wait_flags(ws, WS_F1 + (u32)(w * 32 + i * 8 + (l >> 3)) * FGRP + (l & 7) * FSTRIDE, tag);
            BAR();
            if (tid == 0) __hip_atomic_store((GAS u32*)(ws + WS_GEN), tag, __ATOMIC_RELAXED, __HIP_MEMORY_SCOPE_AGENT);
        }
        STAMP_FLUSH();
        return;
    }
    const int fr = l >> 3, fo8 = l & 7;
    const int tok = 256 * xcd + 64 * rq + 8 * w + fr;
    const u32x4 xr = *(const u32x4*)((const char*)x + (size_t)tok * 896 + cg * 128 + fo8 * 16);
    SB();
    BAR();
    const int TT0 = 16 * xcd + 4 * rq;
    wait_flags(ws, WS_F2 + (u32)(TT0 + cw) * FGRP + (8 * kh + (l & 7)) * FSTRIDE, tag);
    STAMP(10);
    SB();
    glds16_run_sc1<8>(ws + WS_O + (size_t)((TT0 + cw) * 16 + 8 * kh) * 1024, l * 16, (cw * 16 + 8 * kh) * 1024, 1024, 1024);
    WAIT_VM(0);
    BAR();
    u32x4 w2f[28];
    const u32 w2fa = WS_W2F + (u32)((cg * 4 + cw) * 56 + 28 * kh) * 1024 + l * 16;
    u32x4 hres;
    {
        f32x4 acc[4];
        u32x4 af[8];
        const u32 ab = (8 * kh) * 1024 + l * 16;
#pragma unroll
        for (int n = 0; n < 6; ++n) af[n] = lds_ld128(ab + (n & 3) * 16384 + (n >> 2) * 1024);
        SB();
#pragma unroll
        for (int n = 0; n < 32; ++n) {
            if (n < 4) mfma_first(acc[n & 3], wof[n >> 2], af[n & 7]); else mfma_acc(acc[n & 3], wof[n >> 2], af[n & 7]);
            if (n + 6 < 32) af[(n + 6) & 7] = lds_ld128(ab + ((n + 6) & 3) * 16384 + ((n + 6) >> 2) * 1024);
            if ((n & 3) == 1 && (n >> 2) < 7) w2f[n >> 2] = ld_sc1(rs, w2fa + (n >> 2) * 1024);
            SB();
        }
        MFMA_FENCE4(acc);
        STAMP(11);
#pragma unroll
        for (int tt = 0; tt < 4; ++tt) lds_stf4(L_RED3 + (w * 4 + tt) * 1024 + l * 16, acc[tt]);
        BAR();
        const u32 ra = L_RED3 + (w >> 1) * 1024 + (fo8 >> 1) * 4096 + ((2 * (fo8 & 1)) * 16 + 8 * (w & 1) + fr) * 16;
        const f32x4 a0 = lds_ldf4(ra) + lds_ldf4(ra + 16384), a1 = lds_ldf4(ra + 256) + lds_ldf4(ra + 16384 + 256);
        hres = (u32x4){pack2(bflo(xr[0]) + a0[0], bfhi(xr[0]) + a0[1]), pack2(bflo(xr[1]) + a0[2], bfhi(xr[1]) + a0[3]),
                       pack2(bflo(xr[2]) + a1[0], bfhi(xr[2]) + a1[1]), pack2(bflo(xr[3]) + a1[2], bfhi(xr[3]) + a1[3])};
        const int TT = TT0 + (w >> 1), tl = 8 * (w & 1) + fr;
        st_sc1(rs, WS_H + (u32)(TT * 14 + 2 * cg + (fo8 >> 2)) * 1024 + ((fo8 & 3) * 16 + tl) * 16, hres);
        float sum = 0.f, sq = 0.f;
#pragma unroll
        for (int j = 0; j < 4; ++j) { const float a = bflo(hres[j]), c = bfhi(hres[j]); sum += a + c; sq = fmaf(a, a, sq); sq = fmaf(c, c, sq); }
        sum += __shfl_xor(sum, 1); sq += __shfl_xor(sq, 1); sum += __shfl_xor(sum, 2); sq += __shfl_xor(sq, 2);
        sum += __shfl_xor(sum, 4); sq += __shfl_xor(sq, 4);
        if (fo8 == 0) st_sc1_64(rs, WS_ST + (u32)(TT * 7 + cg) * 128 + tl * 8, (u32x2){__float_as_uint(sum), __float_as_uint(sq)});
        WAIT_VM(0);
        set_flag(ws, WS_F3 + (u32)TT * FGRP + (2 * cg + (w & 1)) * FSTRIDE, tag, l);
    }
    STAMP(12);
    SB();
#pragma unroll
    for (int s = 7; s < 14; ++s) w2f[s] = ld_sc1(rs, w2fa + s * 1024);
    SB();
    f32x4 cs4, bias4;
    {
        const u32x4 ones = {0x3f803f80u, 0x3f803f80u, 0x3f803f80u, 0x3f803f80u};
        f32x4 c4 = {0.f, 0.f, 0.f, 0.f}, c5 = {0.f, 0.f, 0.f, 0.f};
#pragma unroll
        for (int s = 0; s < 14; s += 2) { c4 = mfma16(w1f[s], ones, c4); c5 = mfma16(w1f[s + 1], ones, c5); }
        cs4 = c4 + c5;
        const u32x2 bb = *(const u32x2*)((const char*)b1 + cg4 * 256 + w * 32 + g * 8);
        bias4 = (f32x4){bflo(bb[0]), bfhi(bb[0]), bflo(bb[1]), bfhi(bb[1])};
    }
    const int TT4 = 8 * RB4 + w;
    BAR();
    STAMP(13);
    wait_flags(ws, WS_F3 + (u32)TT4 * FGRP + (l < 14 ? l : 0) * FSTRIDE, tag);
    STAMP(14);
    SB();
    {
        u32x2 stv[2];
#pragma unroll
        for (int i = 0; i < 2; ++i) { const int c = g + 4 * i; stv[i] = ld_sc1_64(rs, WS_ST + (u32)(TT4 * 7 + (c < 7 ? c : 0)) * 128 + t * 8); }
        SB();
        glds16_run_sc1<14>(ws + WS_H + (size_t)(TT4 * 14) * 1024, l * 16, w * 1024, 1024, 8192);
        f32x4 acc[8];
        u32x4 af[8];
        u32 ab0 = l * 16, ab1 = l * 16 + 65536;
        OPAQUE(ab1);
#define H4_ADDR(n) ((n) < 64 ? ab0 + (n) * 1024 : ab1 + ((n) - 64) * 1024)
        WAIT_VM(12);
        STAMP(20);
        BAR();
#pragma unroll
        for (int n = 0; n < 6; ++n) af[n] = lds_ld128(H4_ADDR(n));
        SB();
#pragma unroll
        for (int n = 0; n < 112; ++n) {
            if (n + 6 == 16) { WAIT_VM(8); BAR(); }
            if (n + 6 == 48) { WAIT_VM(4); BAR(); }
            if (n + 6 == 80) { WAIT_VM(0); BAR(); }
            if (n < 8) mfma_first(acc[n & 7], w1f[n >> 3], af[n & 7]); else mfma_acc(acc[n & 7], w1f[n >> 3], af[n & 7]);
            if (n + 6 < 112) af[(n + 6) & 7] = lds_ld128(H4_ADDR(n + 6));
            if (n >= 76 && (n & 3) == 0 && (n - 76) / 4 < 7) w2f[14 + (n - 76) / 4] = ld_sc1(rs, w2fa + (14 + (n - 76) / 4) * 1024);
            SB();
        }
        asm volatile("s_nop 7\n\ts_nop 1" : "+v"(acc[0]), "+v"(acc[1]), "+v"(acc[2]), "+v"(acc[3]), "+v"(acc[4]), "+v"(acc[5]), "+v"(acc[6]), "+v"(acc[7]));
        STAMP(15);
        {
            float sum = __uint_as_float(stv[0][0]), sq = __uint_as_float(stv[0][1]);
            if (g < 3) { sum += __uint_as_float(stv[1][0]); sq += __uint_as_float(stv[1][1]); }
            sum += xor16(sum); sq += xor16(sq); sum += xor32(sum); sq += xor32(sq);
            const float mean = sum * (1.f / 448.f);
            const float rstd = rsqrtf(fmaxf(sq * (1.f / 448.f) - mean * mean, 0.f) + 1e-5f);
            if (g == 0) lds_st64(L_ST4 + (16 * w + t) * 8, (u32x2){__float_as_uint(rstd), __float_as_uint(-mean * rstd)});
        }
        BAR();
#pragma unroll
        for (int m = 0; m < 4; ++m) {
            u32 a[2][2];
#pragma unroll
            for (int u = 0; u < 2; ++u) {
                const int tt = 2 * m + u;
                const u32x2 st = *(LAS u32x2*)(uintptr_t)(L_ST4 + (16 * tt + t) * 8);
                const float rstd = __uint_as_float(st[0]), mr = __uint_as_float(st[1]);
                float v[4];
#pragma unroll
                for (int i = 0; i < 4; ++i) { float z = fmaf(acc[tt][i], rstd, fmaf(cs4[i], mr, bias4[i])); z = fmaxf(z, 0.f); v[i] = z * z; }
                a[u][0] = pack2(v[0], v[1]); a[u][1] = pack2(v[2], v[3]);
            }
            const auto r0 = __builtin_amdgcn_permlane16_swap(a[0][0], a[1][0], false, false);
            const auto r1 = __builtin_amdgcn_permlane16_swap(a[0][1], a[1][1], false, false);
            const u32x4 o = {r0[0], r1[0], r0[1], r1[1]};
            const int TTo = 8 * RB4 + 2 * m + (g & 1);
            st_sc1(rs, WS_HH + (u32)(TTo * 56 + 4 * cg4 + (w >> 1)) * 1024 + ((2 * (w & 1) + (g >> 1)) * 16 + t) * 16, o);
        }
        WAIT_VM(0);
        BAR();
        if (tid == 0) __hip_atomic_store((GAS u32*)(ws + WS_F4 + RB4 * FGRP + cg4 * FSTRIDE), tag, __ATOMIC_RELAXED, __HIP_MEMORY_SCOPE_AGENT);
    }
    STAMP(16);
    SB();
#pragma unroll
    for (int s = 21; s < 28; ++s) w2f[s] = ld_sc1(rs, w2fa + s * 1024);
    SB();
    {
        const u32x4 bb = *(const u32x4*)((const char*)b2 + cg * 128 + fo8 * 16);
        wait_flags(ws, WS_F4 + (u32)(TT0 >> 3) * FGRP + (7 * kh + (l < 7 ? l : 0)) * FSTRIDE, tag);
        STAMP(17);
        SB();
        const char* hsrc = ws + WS_HH + (size_t)((TT0 + cw) * 56 + 28 * kh) * 1024;
        glds16_run_sc1<20>(hsrc, l * 16, kh * L_H5 + cw * 1024, 1024, 4096);
        f32x4 acc[4];
        u32x4 af[8];
        u32 ab0 = kh * L_H5 + l * 16, ab1 = kh * L_H5 + l * 16 + 65536;
        OPAQUE(ab0); OPAQUE(ab1);
#define H5_SLOT(n) ((((n) >> 2) < 20 ? ((n) >> 2) : ((n) >> 2) - 20) * 4 + ((n) & 3))
#define H5_ADDR(n) (H5_SLOT(n) < 64 ? ab0 + H5_SLOT(n) * 1024 : ab1 + (H5_SLOT(n) - 64) * 1024)
        WAIT_VM(17);
        BAR();
#pragma unroll
        for (int n = 0; n < 6; ++n) af[n] = lds_ld128(H5_ADDR(n));
        SB();
#pragma unroll
        for (int n = 0; n < 112; ++n) {
            if (n + 6 == 12) { WAIT_VM(13); BAR(); }
            if (n + 6 == 28) { WAIT_VM(8); BAR(); }
            if (n + 6 == 40) {
                BAR();
                glds16_run_sc1<8>(hsrc + 20 * 1024, l * 16, kh * L_H5 + cw * 1024, 1024, 4096);
            }
            if (n + 6 == 48) { WAIT_VM(11); BAR(); }
            if (n + 6 == 68) { WAIT_VM(6); BAR(); }
            if (n + 6 == 88) { WAIT_VM(0); BAR(); }
            if (n < 4) mfma_first(acc[n & 3], w2f[n >> 2], af[n & 7]); else mfma_acc(acc[n & 3], w2f[n >> 2], af[n & 7]);
            if (n + 6 < 112) af[(n + 6) & 7] = lds_ld128(H5_ADDR(n + 6));
            SB();
        }
        MFMA_FENCE4(acc);
        STAMP(18);
#pragma unroll
        for (int tt = 0; tt < 4; ++tt) lds_stf4(L_RED5 + (w * 4 + tt) * 1024 + l * 16, acc[tt]);
        BAR();
        const u32 ra = L_RED5 + (w >> 1) * 1024 + (fo8 >> 1) * 4096 + ((2 * (fo8 & 1)) * 16 + 8 * (w & 1) + fr) * 16;
        const f32x4 a0 = lds_ldf4(ra) + lds_ldf4(ra + 16384), a1 = lds_ldf4(ra + 256) + lds_ldf4(ra + 16384 + 256);
        const u32x4 o = {pack2(a0[0] + bflo(bb[0]) + bflo(hres[0]), a0[1] + bfhi(bb[0]) + bfhi(hres[0])),
                         pack2(a0[2] + bflo(bb[1]) + bflo(hres[1]), a0[3] + bfhi(bb[1]) + bfhi(hres[1])),
                         pack2(a1[0] + bflo(bb[2]) + bflo(hres[2]), a1[1] + bfhi(bb[2]) + bfhi(hres[2])),
                         pack2(a1[2] + bflo(bb[3]) + bflo(hres[3]), a1[3] + bfhi(bb[3]) + bfhi(hres[3]))};
        *(u32x4*)((char*)out + (size_t)tok * 896 + cg * 128 + fo8 * 16) = o;
    }
    STAMP(19);
    STAMP_FLUSH();
}

extern "C" void kernel_launch(void* const* d_in, const int* in_sizes, int n_in, void* d_out, int out_size, void* d_ws, size_t ws_size,
                              hipStream_t stream) {
    (void)hipFuncSetAttribute((const void*)fused_kernel, hipFuncAttributeMaxDynamicSharedMemorySize, LDS_BYTES);
    fused_kernel<<<256, 512, LDS_BYTES, stream>>>((const u16*)d_in[0], (const u16*)d_in[1], (const u16*)d_in[2], (const u16*)d_in[4],
                                                  (const u16*)d_in[5], (const u16*)d_in[6], (const u16*)d_in[7], (u16*)d_out, (char*)d_ws);
}
```

```cpp
#include <hip/hip_runtime.h>
#include <stdint.h>

typedef unsigned short u16;
typedef unsigned int u32;
typedef __attribute__((ext_vector_type(8))) __bf16 bf16x8;
typedef __attribute__((ext_vector_type(2))) __bf16 bf16x2;
typedef __attribute__((ext_vector_type(4))) short s16x4;
typedef __attribute__((ext_vector_type(4))) float f32x4;
typedef __attribute__((ext_vector_type(2))) float f32x2;
typedef __attribute__((ext_vector_type(4))) u32 u32x4;
typedef __attribute__((ext_vector_type(2))) u32 u32x2;
#define LAS __attribute__((address_space(3)))
#define GAS __attribute__((address_space(1)))
#define DI __device__ __forceinline__

#define FSTRIDE 64
constexpr u32 FGRP = 16 * FSTRIDE < 128 ? 128 : 16 * FSTRIDE;
constexpr u32 WS_GEN = 0;
constexpr u32 WS_FW = 4096;
constexpr u32 WS_F1 = WS_FW + 448 * 64;
constexpr u32 WS_F2 = WS_F1 + 256 * FGRP;
constexpr u32 WS_F3 = WS_F2 + 128 * FGRP;
constexpr u32 WS_F4 = WS_F3 + 128 * FGRP;
constexpr u32 WS_KV = WS_F4 + 32 * FGRP;
constexpr u32 WS_O = WS_KV + 16 * 16 * 16384;
constexpr u32 WS_H = WS_O + 128 * 16 * 1024;
constexpr u32 WS_ST = WS_H + 128 * 14 * 1024;
constexpr u32 WS_HH = WS_ST + 128 * 7 * 128;
constexpr u32 WS_W1F = WS_HH + 128 * 56 * 1024;
constexpr u32 WS_W2F = WS_W1F + 112 * 14 * 1024;
constexpr u32 WS_WOF = WS_W2F + 28 * 56 * 1024;
constexpr u32 WS_END = WS_WOF + 28 * 16 * 1024;
constexpr u32 L_WA = 114688;
constexpr u32 L_GEN = 157696;
constexpr u32 L_ST1 = 157952;
constexpr u32 L_CS1 = 158976;
constexpr u32 L_Q = 49152;
constexpr u32 L_PP = 98304;
constexpr u32 L_K = 0, L_V = 24576;
constexpr u32 L_RED3 = 65536;
constexpr u32 L_ST4 = 114688;
constexpr u32 L_H5 = 81920;
constexpr u32 L_RED5 = 32768;
constexpr u32 LDS_BYTES = 163840;
constexpr int SPIN_MAX = 1 << 14;

DI f32x4 mfma16(u32x4 a, u32x4 b, f32x4 c) {
    return __builtin_amdgcn_mfma_f32_16x16x32_bf16(__builtin_bit_cast(bf16x8, a), __builtin_bit_cast(bf16x8, b), c, 0, 0, 0);
}
DI void mfma_acc(f32x4& acc, const u32x4& a, const u32x4& b) {
    asm volatile("v_mfma_f32_16x16x32_bf16 %0, %1, %2, %0" : "+v"(acc) : "v"(a), "v"(b));
}
DI void mfma_first(f32x4& acc, const u32x4& a, const u32x4& b) {
    asm volatile("v_mfma_f32_16x16x32_bf16 %0, %1, %2, 0" : "=&v"(acc) : "v"(a), "v"(b));
}
#define MFMA_FENCE4(a) asm volatile("s_nop 7\n\ts_nop 1" : "+v"(a[0]), "+v"(a[1]), "+v"(a[2]), "+v"(a[3]))
#define OPAQUE(x) asm volatile("" : "+v"(x))
DI u32 pack2(float lo, float hi) {
    f32x2 v = {lo, hi};
    bf16x2 r = __builtin_convertvector(v, bf16x2);
    return __builtin_bit_cast(u32, r);
}
DI float dot2bf(u32 a, u32 b, float c) {
    return __builtin_amdgcn_fdot2_f32_bf16(__builtin_bit_cast(bf16x2, a), __builtin_bit_cast(bf16x2, b), c, false);
}
DI float bflo(u32 u) { return __uint_as_float(u << 16); }
DI float bfhi(u32 u) { return __uint_as_float(u & 0xffff0000u); }
DI u32x2 trread(u32 off) {
    s16x4 r = __builtin_amdgcn_ds_read_tr16_b64_v4i16((LAS s16x4*)(uintptr_t)off);
    return __builtin_bit_cast(u32x2, r);
}
DI u32x4 trfrag(u32 off0, u32 off1) {
    u32x2 a = trread(off0), b = trread(off1);
    u32x4 r = {a[0], a[1], b[0], b[1]};
    return r;
}
DI u32x4 lds_ld128(u32 off) { return *(LAS u32x4*)(uintptr_t)off; }
DI f32x4 lds_ldf4(u32 off) { return *(LAS f32x4*)(uintptr_t)off; }
DI void lds_st128(u32 off, u32x4 v) { *(LAS u32x4*)(uintptr_t)off = v; }
DI void lds_stf4(u32 off, f32x4 v) { *(LAS f32x4*)(uintptr_t)off = v; }
DI void lds_st64(u32 off, u32x2 v) { *(LAS u32x2*)(uintptr_t)off = v; }
DI void lds_stf(u32 off, float v) { *(LAS float*)(uintptr_t)off = v; }
template <int N>
DI void glds16_run(const void* sbase, u32 voff, u32 lds, u32 gstep, u32 lstep) {
    u32 keep;
    asm volatile("s_mov_b32 %0, m0\n\ts_mov_b32 m0, %3\n\t"
                 ".rept %c6\n\t"
                 "s_nop 0\n\t"
                 "global_load_lds_dwordx4 %1, %2\n\t"
                 "s_add_u32 m0, m0, %5\n\t"
                 "v_add_u32 %1, %4, %1\n\t"
                 ".endr\n\t"
                 "s_mov_b32 m0, %0"
                 : "=&s"(keep), "+v"(voff) : "s"(sbase), "s"(lds), "s"(gstep), "s"(lstep), "i"(N) : "memory", "scc");
}
template <int N>
DI void glds16_run_sc1(const void* sbase, u32 voff, u32 lds, u32 gstep, u32 lstep) {
    u32 keep;
    asm volatile("s_mov_b32 %0, m0\n\ts_mov_b32 m0, %3\n\t"
                 ".rept %c6\n\t"
                 "s_nop 0\n\t"
                 "global_load_lds_dwordx4 %1, %2 sc1\n\t"
                 "s_add_u32 m0, m0, %5\n\t"
                 "v_add_u32 %1, %4, %1\n\t"
                 ".endr\n\t"
                 "s_mov_b32 m0, %0"
                 : "=&s"(keep), "+v"(voff) : "s"(sbase), "s"(lds), "s"(gstep), "s"(lstep), "i"(N) : "memory", "scc");
}
DI void glds16_one(const void* sbase, u32 voff, u32 goff, u32 lds) {
    u32 tmp;
    asm volatile("s_mov_b32 m0, %4\n\tv_add_u32 %0, %3, %1\n\tglobal_load_lds_dwordx4 %0, %2" : "=&v"(tmp) : "v"(voff), "s"(sbase), "s"(goff), "s"(lds) : "memory");
}
DI void dma_seek(u32 lds) { asm volatile("s_mov_b32 m0, %0" :: "s"(lds) : "memory"); }
template <int LSTEP, int GSTEP>
DI void dma_next_sc1(const void* sbase, u32& voff) {
    asm volatile("s_add_u32 m0, m0, %c2\n\tv_add_u32 %0, %c3, %0\n\tglobal_load_lds_dwordx4 %0, %1 sc1" : "+v"(voff) : "s"(sbase), "i"(LSTEP), "i"(GSTEP) : "memory", "scc");
}
template <int LSTEP>
DI void dma_next_sc1_b(const void* sbase, u32 voff) {
    asm volatile("s_add_u32 m0, m0, %c2\n\ts_nop 0\n\tglobal_load_lds_dwordx4 %0, %1 sc1" :: "v"(voff), "s"(sbase), "i"(LSTEP) : "memory", "scc");
}
#define PIN_S(x) asm volatile("" : "+s"(x))
template <int N>
DI void glds4_run(const void* sbase, u32 voff, u32 lds, u32 gstep) {
    u32 keep;
    asm volatile("s_mov_b32 %0, m0\n\ts_mov_b32 m0, %3\n\ts_nop 0\n\t"
                 ".rept %c5\n\t"
                 "global_load_lds_dword %1, %2\n\t"
                 "v_add_u32 %1, %4, %1\n\t"
                 ".endr\n\t"
                 "s_mov_b32 m0, %0"
                 : "=&s"(keep), "+v"(voff) : "s"(sbase), "s"(lds), "s"(gstep), "i"(N) : "memory");
}
#define SB() __builtin_amdgcn_sched_barrier(0)
#define WAIT_VM(n) asm volatile("s_waitcnt vmcnt(" #n ")" ::: "memory")
template <int N> DI void wait_vm() { asm volatile("s_waitcnt vmcnt(%c0)" :: "i"(N) : "memory"); }
#define BAR0() asm volatile("s_barrier" ::: "memory")
#define BAR() asm volatile("s_waitcnt lgkmcnt(0)\n\ts_barrier" ::: "memory")
DI float xor16(float v) { return __shfl_xor(v, 16); }
DI float xor32(float v) { return __shfl_xor(v, 32); }

typedef __amdgpu_buffer_rsrc_t rsrc_t;
DI u32x4 ld_sc1(rsrc_t r, u32 off) { return __builtin_amdgcn_raw_buffer_load_b128(r, off, 0, 16); }
DI u32x2 ld_sc1_64(rsrc_t r, u32 off) { return __builtin_amdgcn_raw_buffer_load_b64(r, off, 0, 16); }
DI void st_sc1(rsrc_t r, u32 off, u32x4 v) { __builtin_amdgcn_raw_buffer_store_b128(v, r, off, 0, 16); }
DI void st_sc1_64(rsrc_t r, u32 off, u32x2 v) { __builtin_amdgcn_raw_buffer_store_b64(v, r, off, 0, 16); }
DI void set_flag(char* ws, u32 off, u32 tag, int lane) {
    if (lane == 0) __hip_atomic_store((GAS u32*)(ws + off), tag, __ATOMIC_RELAXED, __HIP_MEMORY_SCOPE_AGENT);
}
DI void wait_flags(char* ws, u32 off, u32 tag) {
    for (int it = 0; it < SPIN_MAX; ++it) {
        u32 v = __hip_atomic_load((GAS u32*)(ws + off), __ATOMIC_RELAXED, __HIP_MEMORY_SCOPE_AGENT);
        if (__all(v == tag)) break;
#define PSLEEP 1
        if (PSLEEP) __builtin_amdgcn_s_sleep(PSLEEP);
    }
    asm volatile("" ::: "memory");
}

#define STAMP_DECL do {} while (0)
#define STAMP(I) do {} while (0)
#define STAMP_FLUSH() do {} while (0)

__global__ __launch_bounds__(512, 2) __attribute__((amdgpu_waves_per_eu(2, 2)))
void fused_kernel(const u16* __restrict__ x, const u16* __restrict__ wqkv, const u16* __restrict__ wo, const u16* __restrict__ w1,
                  const u16* __restrict__ b1, const u16* __restrict__ w2, const u16* __restrict__ b2, u16* __restrict__ out,
                  char* __restrict__ ws) {
    const int b = blockIdx.x, xcd = b & 7, loc = b >> 3;
    const int tid = threadIdx.x, w = __builtin_amdgcn_readfirstlane(tid >> 6), l = tid & 63, t = l & 15, g = l >> 4;
    const int q4 = t >> 2, p4 = l & 3;
    const rsrc_t rs = __builtin_amdgcn_make_buffer_rsrc(ws, 0, WS_END, 0x00020000);
    STAMP_DECL;
    STAMP(0);
    const int RB = 4 * (xcd >> 1) + (loc >> 3), h = 8 * (xcd & 1) + (loc & 7);
    const bool odd = h & 1;
    const int tp = w >> 1, ch = w & 1;
    const char* wqb = (const char*)wqkv + 56 * h - (h & 1) * 8;
    const u32 wqv = (l >> 2) * 2688 + (((l & 3) ^ ((l >> 5) << 1)) * 16);
    {
        const char* xb = (const char*)x + (size_t)(128 * RB + 16 * w) * 896;
        glds16_run<14>(xb, (l >> 2) * 896 + (((l & 3) ^ ((-(l >> 4)) & 3)) * 16), w * 14336, 64, 1024);
        asm volatile("s_barrier" ::: "memory");
        if (w < 7) {
#pragma unroll
            for (int hf = 0; hf < 2; ++hf) glds16_run<3>(wqb + (32 * w + 16 * hf) * 2688, wqv, L_WA + w * 6144 + hf * 1024, 896, 2048);
        } else {
            asm volatile("s_mov_b32 m0, %2\n\ts_nop 0\n\tglobal_load_lds_dword %0, %1 sc1" :: "v"(0), "s"(ws + WS_GEN), "s"(L_GEN) : "memory");
            WAIT_VM(1);
        }
    }
    const int pp = b + 256 * (w >> 2);
    const char* ppsrc; u32 pppitch, ppdst;
    {
        const int pe = pp < 448 ? pp : 447;
        const int c = w & 3;
        if (pe < 196) { ppsrc = (const char*)w1 + (pe / 7) * 128 + (size_t)(pe % 7) * (64 * 3584); pppitch = 3584;
                        ppdst = WS_W1F + (u32)((((pe / 7) >> 1) * 8 + ((pe / 7) & 1) * 4 + c) * 14 + 2 * (pe % 7)) * 1024; }
        else if (pe < 392) { const int i = pe - 196; ppsrc = (const char*)w2 + (i / 28) * 128 + (size_t)(i % 28) * (64 * 896); pppitch = 896;
                             ppdst = WS_W2F + (u32)(((i / 28) * 4 + c) * 56 + 2 * (i % 28)) * 1024; }
        else { const int i = pe - 392; ppsrc = (const char*)wo + (i >> 3) * 128 + (size_t)(i & 7) * (56 * 896); pppitch = 896;
               ppdst = WS_WOF + (u32)(((i >> 3) * 4 + c) * 16 + 2 * (i & 7)) * 1024; }
    }
    const bool ppshort = pp >= 392 && (w & 3) == 3;
    const u32 ppv = (l >> 3) * pppitch + (l & 7) * 16;
    u32 ppg0 = (u32)(16 * (w & 3)) * pppitch, ppg1 = ppshort ? ppg0 : ppg0 + 8 * pppitch;
    u32 ppl0 = L_PP + (w >> 2) * 8192 + (w & 3) * 2048, ppl1 = ppshort ? ppl0 : ppl0 + 1024;
    const u32 ppa = L_PP + (w >> 2) * 8192 + (w & 3) * 32 + 8 * p4 + (pp < 392 ? (8 * g + q4) * 128 : (4 * g + q4) * 128);
    const u32 ppk = pp < 392 ? 4096 : 3072, pph = pp < 392 ? 512 : 2048;
    u32 wg[6], wl[6], wpa[6]; bool wpat[6];
#pragma unroll
    for (int j = 0; j < 6; ++j) {
        const int idx = 7 * j + (w < 7 ? w : 6), ks = idx / 6, r = idx - 6 * ks;
        wg[j] = (u32)((32 * (7 + ks) + 16 * (r & 1)) * 2688 + (r >> 1) * 896);
        wl[j] = (u32)(ks * 14336 + (r >> 1) * 2048 + (r & 1) * 1024);
        wpat[j] = r < 4;
        wpa[j] = wl[j] + (l & 15) * 64 + ((((odd ? 0 : 1) ^ (((r & 1) * 16 + (l & 15)) >> 3 & 1)) * 32) + (odd ? 0 : 24));
        PIN_S(wg[j]); PIN_S(wl[j]);
    }
    PIN_S(ppg0); PIN_S(ppg1); PIN_S(ppl0); PIN_S(ppl1); PIN_S(ppdst);
    WAIT_VM(6);
    BAR();
    STAMP(1);
    u32x4 xf[2][14];
    {
        const u32 xl = t * 64 + ((g ^ ((-(t >> 2)) & 3)) * 16);
        const u32 xa = w * 14336 + xl, xb2 = (w ^ 1) * 14336 + xl;
#pragma unroll
        for (int s = 0; s < 14; ++s) { xf[0][s] = lds_ld128(xa + s * 1024); xf[1][s] = lds_ld128(xb2 + s * 1024); }
    }
    const u32 padoff = (l >> 5) * 2048 + (l & 31) * 64 + ((((odd ? 0 : 1) ^ ((l >> 3) & 1)) * 32) + (odd ? 0 : 24));
    WAIT_VM(0);
    if (w < 7) lds_st64(L_WA + w * 6144 + padoff, (u32x2){0x3f803f80u, 0x3f803f80u});
    BAR();
    const u32 tag = __builtin_amdgcn_readfirstlane(*(LAS u32*)(uintptr_t)L_GEN) + 1u;
    STAMP(2);
    SB();
    {
        f32x4 acc[2][3], cs[3];
        u32 lbA[2], lbB[2], lbC[2];
        {
            const u32 h0 = (8 * g + q4) * 64 + ((g & 1) * 32) + p4 * 8, h1 = (8 * g + q4) * 64 + (((g & 1) ^ 1) * 32) + p4 * 8;
            lbA[0] = h0 + ch * 2048; lbA[1] = h1 + ch * 2048;
            lbB[0] = (ch ? h1 : h0) + 4096;
        }
        u32 b1[3] = {lbA[0] + L_WA, lbA[1] + L_WA, lbB[0] + L_WA};
        u32 b2[3] = {lbA[0], lbA[1], lbB[0]};
        u32 b3[3] = {lbA[0] + 57344, lbA[1] + 57344, lbB[0] + 57344};
        OPAQUE(b1[0]); OPAQUE(b1[1]); OPAQUE(b1[2]); OPAQUE(b2[0]); OPAQUE(b2[1]); OPAQUE(b2[2]); OPAQUE(b3[0]); OPAQUE(b3[1]); OPAQUE(b3[2]);
        (void)lbC;
#define WQ_ADDR(s, c) ((s) < 7 ? b1[c] + (s) * 6144 : (s) < 11 ? b2[c] + ((s) - 7) * 14336 : b3[c] + ((s) - 11) * 14336)
        const u32x4 ones = {0x3f803f80u, 0x3f803f80u, 0x3f803f80u, 0x3f803f80u};
        f32x4 gram;
        u32x4 wf[2][3];
#pragma unroll
        for (int c = 0; c < 3; ++c) { const u32 a = WQ_ADDR(0, c); wf[0][c] = trfrag(a, a + 256); }
#pragma unroll
        for (int c = 0; c < 3; ++c) cs[c] = (f32x4){0.f, 0.f, 0.f, 0.f};
        SB();
#pragma unroll
        for (int s = 0; s < 14; ++s) {
            if (s == 6) glds16_one(ppsrc, ppv, ppg0, ppl0);
            if (s == 7) glds16_one(ppsrc, ppv, ppg1, ppl1);
            if (s == 6 || s == 8 || s == 11) {
                if (s == 6) WAIT_VM(5);
                if (s == 8) WAIT_VM(3);
                if (s == 11) WAIT_VM(0);
#pragma unroll
                for (int j = (s == 6 ? 0 : s == 8 ? 2 : 5); j < (s == 6 ? 2 : s == 8 ? 5 : 6); ++j)
                    if (wpat[j] && l < 16) lds_st64(wpa[j], (u32x2){0x3f803f80u, 0x3f803f80u});
                BAR();
            }
#pragma unroll
            for (int i = 0; i < 6; ++i) {
                const int c = i >> 1, tt = i & 1;
                if (c < 2) { if (s == 0) mfma_first(acc[tt][c], wf[s & 1][c], xf[tt][s]); else mfma_acc(acc[tt][c], wf[s & 1][c], xf[tt][s]); }
                else { if (s == 0) mfma_first(acc[tt][c], xf[tt][s], wf[s & 1][c]); else mfma_acc(acc[tt][c], xf[tt][s], wf[s & 1][c]); }
                if (s + 1 < 14) {
                    const u32x2 r = trread(WQ_ADDR(s + 1, c) + tt * 256);
                    wf[(s + 1) & 1][c][2 * tt] = r[0]; wf[(s + 1) & 1][c][2 * tt + 1] = r[1];
                }
                if (s < 6 && i == 2) glds16_one(wqb, wqv, wg[s < 6 ? s : 0], wl[s < 6 ? s : 0]);
                if (i == 5) { if (s == 0) mfma_first(gram, xf[0][s], xf[0][s]); else mfma_acc(gram, xf[0][s], xf[0][s]); }
                SB();
            }
            if ((s == 12 || s == 13) && pp < 448)
                st_sc1(rs, ppdst + (s - 12) * 1024 + l * 16, trfrag(ppa + (s - 12) * ppk, ppa + (s - 12) * ppk + pph));
            if ((s & 3) == tp) {
                cs[0] = mfma16(wf[s & 1][0], ones, cs[0]); cs[1] = mfma16(wf[s & 1][1], ones, cs[1]); cs[2] = mfma16(ones, wf[s & 1][2], cs[2]);
            }
            SB();
        }
        asm volatile("s_nop 7\n\ts_nop 1" : "+v"(acc[0][0]), "+v"(acc[0][1]), "+v"(acc[0][2]), "+v"(acc[1][0]), "+v"(acc[1][1]), "+v"(acc[1][2]), "+v"(gram));
        STAMP(3);
        {
            const float sum = __shfl(odd ? acc[0][0][0] : acc[0][1][0], odd ? t : 48 + t);
            const float dg = (t & 2) ? ((t & 1) ? gram[3] : gram[2]) : ((t & 1) ? gram[1] : gram[0]);
            const float sq = __shfl(dg, 16 * (t >> 2) + t);
            const float mean = sum * (1.f / 448.f);
            const float rstd = rsqrtf(fmaxf(sq * (1.f / 448.f) - mean * mean, 0.f) + 1e-5f);
            if (g == 0) lds_st64(L_ST1 + (16 * w + t) * 8, (u32x2){__float_as_uint(rstd), __float_as_uint(-mean * rstd)});
            if (t == 0) { lds_stf4(L_CS1 + ((tp * 6 + ch * 3 + 0) * 16 + 4 * g) * 4, cs[0]); lds_stf4(L_CS1 + ((tp * 6 + ch * 3 + 1) * 16 + 4 * g) * 4, cs[1]); }
            if (g == 0) lds_stf(L_CS1 + ((tp * 6 + ch * 3 + 2) * 16 + t) * 4, cs[2][0]);
        }
        BAR();
        STAMP(4);
        f32x4 csum[2]; float csv = 0.f;
        csum[0] = (f32x4){0.f, 0.f, 0.f, 0.f}; csum[1] = csum[0];
#pragma unroll
        for (int p = 0; p < 4; ++p) {
            csum[0] += lds_ldf4(L_CS1 + ((p * 6 + ch * 3 + 0) * 16 + 4 * g) * 4);
            csum[1] += lds_ldf4(L_CS1 + ((p * 6 + ch * 3 + 1) * 16 + 4 * g) * 4);
            csv += *(LAS float*)(uintptr_t)(L_CS1 + ((p * 6 + ch * 3 + 2) * 16 + t) * 4);
        }
        const float qs = ch ? 1.f : 0.18898223650461362f * 1.4426950408889634f;
        const float m0 = (odd && g == 0) ? 0.f : qs, m1 = (!odd && g == 3) ? 0.f : qs;
        const bool vpad = ch == 0 ? (odd && t < 4) : (!odd && t >= 12);
        const u32 blob = WS_KV + (u32)(RB * 16 + h) * 16384;
        u32x2 vv[2];
#pragma unroll
        for (int tt = 0; tt < 2; ++tt) {
            const int tile = w ^ tt;
            const u32x2 st = *(LAS u32x2*)(uintptr_t)(L_ST1 + (16 * tile + t) * 8);
            const float rstd = __uint_as_float(st[0]), mr = __uint_as_float(st[1]);
            float a[8];
#pragma unroll
            for (int i = 0; i < 4; ++i) { a[i] = fmaf(acc[tt][0][i], rstd, csum[0][i] * mr) * m0; a[4 + i] = fmaf(acc[tt][1][i], rstd, csum[1][i] * mr) * m1; }
            const u32x4 f = {pack2(a[0], a[1]), pack2(a[2], a[3]), pack2(a[4], a[5]), pack2(a[6], a[7])};
            if (ch) { st_sc1(rs, blob + tile * 1024 + l * 16, f); lds_st128(L_K + (16 + tile) * 1024 + l * 16, f); }
            else lds_st128(L_Q + tile * 1024 + l * 16, f);
            const u32x4 sa = lds_ld128(L_ST1 + (16 * tile + 4 * g) * 8), sb = lds_ld128(L_ST1 + (16 * tile + 4 * g + 2) * 8);
            const float v0 = fmaf(acc[tt][2][0], __uint_as_float(sa[0]), csv * __uint_as_float(sa[1]));
            const float v1 = fmaf(acc[tt][2][1], __uint_as_float(sa[2]), csv * __uint_as_float(sa[3]));
            const float v2 = fmaf(acc[tt][2][2], __uint_as_float(sb[0]), csv * __uint_as_float(sb[1]));
            const float v3 = fmaf(acc[tt][2][3], __uint_as_float(sb[2]), csv * __uint_as_float(sb[3]));
            vv[tt] = (u32x2){pack2(v0, v1), pack2(v2, v3)};
            if (vpad) vv[tt] = (u32x2){0x3f803f80u, 0x3f803f80u};
        }
        const u32x4 vfr = ch ? (u32x4){vv[1][0], vv[1][1], vv[0][0], vv[0][1]} : (u32x4){vv[0][0], vv[0][1], vv[1][0], vv[1][1]};
        st_sc1(rs, blob + 8192 + (tp * 2 + ch) * 1024 + l * 16, vfr);
        lds_st128(L_V + ((8 + tp) * 2 + ch) * 1024 + l * 16, vfr);
    }
    {
        WAIT_VM(0);
        set_flag(ws, WS_F1 + (u32)(RB * 16 + h) * FGRP + w * FSTRIDE, tag, l);
        BAR();
        if ((tid & 255) == 0 && pp < 448) __hip_atomic_store((GAS u32*)(ws + WS_FW + pp * 64), tag, __ATOMIC_RELAXED, __HIP_MEMORY_SCOPE_AGENT);
    }
    const u32x4 qf = lds_ld128(L_Q + w * 1024 + l * 16);
    STAMP(5);
    SB();
    const int cg4 = 7 * (xcd & 1) + loc % 7, RB4 = 4 * (xcd >> 1) + loc / 7;
    const int rq = loc / 7, cg = loc % 7;
    const int cw = w & 3, kh = w >> 2;
    const bool worker = loc < 28;
    {
        const int rbl = RB - 2 + (l & 1);
        const int rsafe = rbl >= 0 ? rbl : RB;
        wait_flags(ws, WS_F1 + (u32)(rsafe * 16 + h) * FGRP + ((l & 2) ? (w | 1) : w) * FSTRIDE, tag);
        STAMP(6);
#pragma unroll
        for (int blk = 0; blk < 2; ++blk) {
            const int rb = RB - 2 + blk;
            if (rb >= 0) glds16_run_sc1<2>(ws + WS_KV + (size_t)(rb * 16 + h) * 16384 + w * 1024, l * 16, L_K + blk * 8192 + w * 1024, 8192, L_V - L_K);
            else { lds_st128(L_K + blk * 8192 + tid * 16, (u32x4){0, 0, 0, 0}); lds_st128(L_V + blk * 8192 + tid * 16, (u32x4){0, 0, 0, 0}); }
        }
        WAIT_VM(0);
    }
    BAR();
    STAMP(7);
    u32 fwo;
    {
        const int cgs = worker ? cg : 0, cg4s = worker ? cg4 : 0;
        int pi = 392 + cgs * 8 + 4 * kh + (l & 3);
        if (l >= 4 && l < 11) pi = (2 * cg4s + (w >> 2)) * 7 + (l - 4);
        if (l >= 11 && l < 25) pi = 196 + cgs * 28 + 14 * kh + (l - 11);
        fwo = WS_FW + (u32)pi * 64;
    }
    const u32 fwv = __hip_atomic_load((GAS u32*)(ws + fwo), __ATOMIC_RELAXED, __HIP_MEMORY_SCOPE_AGENT);
    SB();
    u32x4 wof[8];
    u32x4 w1f[14];
    const u32 wofa = WS_WOF + (u32)(((worker ? cg : 0) * 4 + cw) * 16 + 8 * kh) * 1024 + l * 16;
    const u32 w1fa = WS_W1F + (u32)(((worker ? cg4 : 0) * 8 + w) * 14) * 1024 + l * 16;
#define PF2(i) do { if ((i) < 8) wof[(i) < 8 ? (i) : 0] = ld_sc1(rs, wofa + (i) * 1024); else w1f[(i) >= 8 ? (i) - 8 : 0] = ld_sc1(rs, w1fa + ((i) - 8) * 1024); } while (0)
    {
        const int base = w & ~1;
        f32x4 sc[18];
        {
            u32x4 kf[18];
#pragma unroll
            for (int a = 0; a < 18; ++a) kf[a] = lds_ld128(L_K + (base + a) * 1024 + l * 16);
            SB();
#pragma unroll
            for (int a = 0; a < 18; ++a) {
                sc[a] = mfma16(kf[a], qf, (f32x4){0.f, 0.f, 0.f, 0.f});
                SB();
            }
        }
        if (!__all(fwv == tag)) wait_flags(ws, fwo, tag);
        asm volatile("" ::: "memory");
        PF2(0); PF2(1);
        SB();
        u32x4 vf[18];
#pragma unroll
        for (int a = 0; a < 8; ++a) vf[a] = lds_ld128(L_V + (base >> 1) * 2048 + a * 1024 + l * 16);
        SB();
        const float NEG = -1e30f;
        const int qq = 256 + 16 * w + t;
#pragma unroll
        for (int a = 0; a < 2; ++a)
#pragma unroll
            for (int i = 0; i < 4; ++i) { const int kk = 16 * (base + a) + 4 * g + i; if (kk <= qq - 256) sc[a][i] = NEG; }
#pragma unroll
        for (int a = 16; a < 18; ++a)
#pragma unroll
            for (int i = 0; i < 4; ++i) { const int kk = 16 * (base + a) + 4 * g + i; if (kk > qq) sc[a][i] = NEG; }
        if (RB < 2) {
            asm volatile("" ::: "memory");
            const int tmin = 8 * (2 - RB);
#pragma unroll
            for (int a = 0; a < 18; ++a) if (base + a < tmin) sc[a] = (f32x4){NEG, NEG, NEG, NEG};
        }
        PF2(2); PF2(3);
        SB();
        float ma = NEG, mb = NEG;
#pragma unroll
        for (int a = 0; a < 18; a += 2) {
            ma = fmaxf(fmaxf(sc[a][0], sc[a][1]), ma); ma = fmaxf(fmaxf(sc[a][2], sc[a][3]), ma);
            mb = fmaxf(fmaxf(sc[a + 1][0], sc[a + 1][1]), mb); mb = fmaxf(fmaxf(sc[a + 1][2], sc[a + 1][3]), mb);
        }
        PF2(4); PF2(5);
        SB();
        float m = fmaxf(ma, mb);
        m = fmaxf(m, xor16(m)); m = fmaxf(m, xor32(m));
        f32x4 o0 = {0.f, 0.f, 0.f, 0.f}, o1 = {0.f, 0.f, 0.f, 0.f};
#pragma unroll
        for (int a = 0; a < 9; ++a) {
            if (2 * a + 8 < 18) {
                vf[2 * a + 8] = lds_ld128(L_V + (base >> 1) * 2048 + (2 * a + 8) * 1024 + l * 16);
                vf[2 * a + 9] = lds_ld128(L_V + (base >> 1) * 2048 + (2 * a + 9) * 1024 + l * 16);
            }
            if (a < 8) { PF2(6 + 2 * a); PF2(7 + 2 * a); }
            u32x4 pf;
            pf[0] = pack2(__builtin_amdgcn_exp2f(sc[2 * a][0] - m), __builtin_amdgcn_exp2f(sc[2 * a][1] - m));
            pf[1] = pack2(__builtin_amdgcn_exp2f(sc[2 * a][2] - m), __builtin_amdgcn_exp2f(sc[2 * a][3] - m));
            pf[2] = pack2(__builtin_amdgcn_exp2f(sc[2 * a + 1][0] - m), __builtin_amdgcn_exp2f(sc[2 * a + 1][1] - m));
            pf[3] = pack2(__builtin_amdgcn_exp2f(sc[2 * a + 1][2] - m), __builtin_amdgcn_exp2f(sc[2 * a + 1][3] - m));
            o0 = mfma16(vf[2 * a], pf, o0);
            o1 = mfma16(vf[2 * a + 1], pf, o1);
            SB();
        }
        float den = odd ? o0[0] : o1[0];
        den = __shfl(den, odd ? t : (48 + t));
        const float r = 1.f / den;
        const float m0 = (odd && g == 0) ? 0.f : r, m1 = (!odd && g == 3) ? 0.f : r;
        const u32x4 of = {pack2(o0[0] * m0, o0[1] * m0), pack2(o0[2] * m0, o0[3] * m0), pack2(o1[0] * m1, o1[1] * m1), pack2(o1[2] * m1, o1[3] * m1)};
        STAMP(8);
        st_sc1(rs, WS_O + (u32)((RB * 8 + w) * 16 + h) * 1024 + l * 16, of);
        WAIT_VM(0);
        set_flag(ws, WS_F2 + (u32)(RB * 8 + w) * FGRP + h * FSTRIDE, tag, l);
    }
    STAMP(9);
    SB();
    if (!worker) {
        if (b == 255) {
#pragma unroll
            for (int i = 0; i < 4; ++i) wait_flags(ws, WS_F1 + (u32)(w * 32 + i * 8 + (l >> 3)) * FGRP + (l & 7) * FSTRIDE, tag);
            BAR();
            if (tid == 0) __hip_atomic_store((GAS u32*)(ws + WS_GEN), tag, __ATOMIC_RELAXED, __HIP_MEMORY_SCOPE_AGENT);
        }
        STAMP_FLUSH();
        return;
    }
    const int RBW = 2 * xcd + (rq >> 1);
#define TTG(tt) (8 * RBW + 4 * ((tt) >> 1) + 2 * (rq & 1) + ((tt) & 1))
    const int fr = l >> 3, fo8 = l & 7;
    const int TTf = TTG(w >> 1), tl = 8 * (w & 1) + fr;
    const int tok = 16 * TTf + tl;
    const u32x4 xr = *(const u32x4*)((const char*)x + (size_t)tok * 896 + cg * 128 + fo8 * 16);
    SB();
    BAR();
    const int TTc = TTG(cw);
    wait_flags(ws, WS_F2 + (u32)TTc * FGRP + (8 * kh + (l & 7)) * FSTRIDE, tag);
    STAMP(10);
    SB();
    glds16_run_sc1<8>(ws + WS_O + (size_t)(TTc * 16 + 8 * kh) * 1024, l * 16, (cw * 16 + 8 * kh) * 1024, 1024, 1024);
    WAIT_VM(0);
    BAR();
    u32x4 w2f[28];
    const int rot5 = 4 * ((cg + 2 * rq) % 7);
    const u32 w2fa = WS_W2F + (u32)((cg * 4 + cw) * 56 + 28 * kh) * 1024 + l * 16;
#define W2F(i) ld_sc1(rs, w2fa + (u32)((i) + rot5 < 28 ? (i) + rot5 : (i) + rot5 - 28) * 1024)
    u32x4 hres;
    {
        f32x4 acc[4];
        u32x4 af[8];
        const u32 ab = (8 * kh) * 1024 + l * 16;
#pragma unroll
        for (int n = 0; n < 6; ++n) af[n] = lds_ld128(ab + (n & 3) * 16384 + (n >> 2) * 1024);
        SB();
#pragma unroll
        for (int n = 0; n < 32; ++n) {
            if (n < 4) mfma_first(acc[n & 3], wof[n >> 2], af[n & 7]); else mfma_acc(acc[n & 3], wof[n >> 2], af[n & 7]);
            if (n + 6 < 32) af[(n + 6) & 7] = lds_ld128(ab + ((n + 6) & 3) * 16384 + ((n + 6) >> 2) * 1024);
            if ((n & 3) == 1 && (n >> 2) < 7) w2f[n >> 2] = W2F(n >> 2);
            SB();
        }
        MFMA_FENCE4(acc);
        STAMP(11);
#pragma unroll
        for (int tt = 0; tt < 4; ++tt) lds_stf4(L_RED3 + (w * 4 + tt) * 1024 + l * 16, acc[tt]);
        BAR();
        const u32 ra = L_RED3 + (w >> 1) * 1024 + (fo8 >> 1) * 4096 + ((2 * (fo8 & 1)) * 16 + tl) * 16;
        const f32x4 a0 = lds_ldf4(ra) + lds_ldf4(ra + 16384), a1 = lds_ldf4(ra + 256) + lds_ldf4(ra + 16384 + 256);
        hres = (u32x4){pack2(bflo(xr[0]) + a0[0], bfhi(xr[0]) + a0[1]), pack2(bflo(xr[1]) + a0[2], bfhi(xr[1]) + a0[3]),
                       pack2(bflo(xr[2]) + a1[0], bfhi(xr[2]) + a1[1]), pack2(bflo(xr[3]) + a1[2], bfhi(xr[3]) + a1[3])};
        st_sc1(rs, WS_H + (u32)(TTf * 14 + 2 * cg + (fo8 >> 2)) * 1024 + ((fo8 & 3) * 16 + tl) * 16, hres);
        float sum = 0.f, sq = 0.f;
#pragma unroll
        for (int j = 0; j < 4; ++j) { const float a = bflo(hres[j]), c = bfhi(hres[j]); sum += a + c; sq = fmaf(a, a, sq); sq = fmaf(c, c, sq); }
        sum += __shfl_xor(sum, 1); sq += __shfl_xor(sq, 1); sum += __shfl_xor(sum, 2); sq += __shfl_xor(sq, 2);
        sum += __shfl_xor(sum, 4); sq += __shfl_xor(sq, 4);
        if (fo8 == 0) st_sc1_64(rs, WS_ST + (u32)(TTf * 7 + cg) * 128 + tl * 8, (u32x2){__float_as_uint(sum), __float_as_uint(sq)});
        WAIT_VM(0);
        set_flag(ws, WS_F3 + (u32)TTf * FGRP + (2 * cg + (w & 1)) * FSTRIDE, tag, l);
    }
    STAMP(12);
    SB();
#pragma unroll
    for (int s = 7; s < 14; ++s) w2f[s] = W2F(s);
    SB();
    f32x4 cs4, bias4;
    {
        const u32x4 ones = {0x3f803f80u, 0x3f803f80u, 0x3f803f80u, 0x3f803f80u};
        f32x4 c4 = {0.f, 0.f, 0.f, 0.f}, c5 = {0.f, 0.f, 0.f, 0.f};
#pragma unroll
        for (int s = 0; s < 14; s += 2) { c4 = mfma16(w1f[s], ones, c4); c5 = mfma16(w1f[s + 1], ones, c5); }
        cs4 = c4 + c5;
        const u32x2 bb = *(const u32x2*)((const char*)b1 + cg4 * 256 + w * 32 + g * 8);
        bias4 = (f32x4){bflo(bb[0]), bfhi(bb[0]), bflo(bb[1]), bfhi(bb[1])};
    }
    BAR();
    STAMP(13);
    wait_flags(ws, WS_F3 + (u32)(8 * RB4 + (w & 3) + ((l & 16) ? 4 : 0)) * FGRP + ((l & 15) < 14 ? (l & 15) : 0) * FSTRIDE, tag);
    STAMP(14);
    SB();
    {
        glds16_run_sc1<1>(ws + WS_ST + (size_t)((8 * RB4 + w) * 7) * 128, l * 16, L_ST4 + 1024 + w * 1024, 0, 0);
        const char* hsA = ws + WS_H + (size_t)((8 * RB4 + (w & 3)) * 14 + (w >> 2)) * 1024;
        const char* hsB = hsA + 4 * 14 * 1024;
        u32 hv = l * 16 - 2048;
        dma_seek((w >> 2) * 8192 + (w & 3) * 1024 - 16384);
#pragma unroll
        for (int i = 0; i < 4; ++i) dma_next_sc1<16384, 2048>(hsA, hv);
        f32x4 acc[8];
        u32x4 af[8];
        u32 ab0 = l * 16, ab1 = l * 16 + 65536;
        OPAQUE(ab1);
#define H4_BLK(n) ((((n) % 56) >> 2) * 8 + ((n) / 56) * 4 + ((n) & 3))
#define H4_ADDR(n) (H4_BLK(n) < 64 ? ab0 + H4_BLK(n) * 1024 : ab1 + (H4_BLK(n) - 64) * 1024)
#define P4_EPILOGUE(M0) do { _Pragma("unroll") for (int m = (M0); m < (M0) + 2; ++m) { \
            u32 a[2][2]; \
            _Pragma("unroll") for (int u = 0; u < 2; ++u) { \
                const int tt = 2 * m + u; \
                const u32x2 st = *(LAS u32x2*)(uintptr_t)(L_ST4 + (16 * tt + t) * 8); \
                const float rstd = __uint_as_float(st[0]), mr = __uint_as_float(st[1]); \
                float v[4]; \
                _Pragma("unroll") for (int i = 0; i < 4; ++i) { float z = fmaf(acc[tt][i], rstd, fmaf(cs4[i], mr, bias4[i])); z = fmaxf(z, 0.f); v[i] = z * z; } \
                a[u][0] = pack2(v[0], v[1]); a[u][1] = pack2(v[2], v[3]); \
            } \
              \
            const auto r0 = __builtin_amdgcn_permlane16_swap(a[0][0], a[1][0], false, false); \
            const auto r1 = __builtin_amdgcn_permlane16_swap(a[0][1], a[1][1], false, false); \
            const u32x4 o = {r0[0], r1[0], r0[1], r1[1]}; \
            const int TTo = 8 * RB4 + 2 * m + (g & 1); \
            st_sc1(rs, WS_HH + (u32)(TTo * 56 + 4 * cg4 + (w >> 1)) * 1024 + ((2 * (w & 1) + (g >> 1)) * 16 + t) * 16, o); \
        } } while (0)
        WAIT_VM(2);
        STAMP(20);
        BAR();
#pragma unroll
        for (int n = 0; n < 6; ++n) af[n] = lds_ld128(H4_ADDR(n));
        SB();
#pragma unroll
        for (int seg = 0; seg < 7; ++seg) {
            constexpr int SB0[8] = {0, 10, 26, 42, 50, 66, 82, 112};
            if (seg == 1) {
                wait_vm<2>();
                const u32x2 p0 = *(LAS u32x2*)(uintptr_t)(L_ST4 + 1024 + w * 1024 + g * 128 + t * 8);
                const u32x2 p1 = *(LAS u32x2*)(uintptr_t)(L_ST4 + 1024 + w * 1024 + (g + 4) * 128 + t * 8);
                float sum = __uint_as_float(p0[0]), sq = __uint_as_float(p0[1]);
                if (g < 3) { sum += __uint_as_float(p1[0]); sq += __uint_as_float(p1[1]); }
                sum += xor16(sum); sq += xor16(sq); sum += xor32(sum); sq += xor32(sq);
                const float mean = sum * (1.f / 448.f);
                const float rstd = rsqrtf(fmaxf(sq * (1.f / 448.f) - mean * mean, 0.f) + 1e-5f);
                if (g == 0) lds_st64(L_ST4 + (16 * w + t) * 8, (u32x2){__float_as_uint(rstd), __float_as_uint(-mean * rstd)});
                BAR0();
            }
            if (seg == 2) { wait_vm<4>(); BAR(); }
            if (seg == 3) { wait_vm<7>(); BAR0(); }
            if (seg == 4) { wait_vm<5>(); BAR0(); }
            if (seg == 5) { wait_vm<7>(); BAR0(); }
            if (seg == 6) {
                wait_vm<4>(); BAR0();
                if (tid == 0) __hip_atomic_store((GAS u32*)(ws + WS_F4 + (RB4 * 2) * FGRP + cg4 * FSTRIDE), tag, __ATOMIC_RELAXED, __HIP_MEMORY_SCOPE_AGENT);
            }
#pragma unroll
            for (int n = SB0[seg]; n < SB0[seg + 1]; ++n) {
                if (n == 56) {
                    asm volatile("s_nop 7\n\ts_nop 1" : "+v"(acc[0]), "+v"(acc[1]), "+v"(acc[2]), "+v"(acc[3]));
                    P4_EPILOGUE(0);
                    SB();
                }
                const int ai = (n / 56) * 4 + (n & 3), ks = (n % 56) >> 2;
                if (ks == 0) mfma_first(acc[ai], w1f[ks], af[n & 7]); else mfma_acc(acc[ai], w1f[ks], af[n & 7]);
                if (n + 6 < 112) af[(n + 6) & 7] = lds_ld128(H4_ADDR(n + 6));
                if (n == 4 || n == 8 || n == 12) dma_next_sc1<16384, 2048>(hsA, hv);
                if (n == 16) { hv = l * 16 - 2048; dma_seek((w >> 2) * 8192 + (4 + (w & 3)) * 1024 - 16384); }
                if (n >= 16 && n <= 40 && (n & 3) == 0) dma_next_sc1<16384, 2048>(hsB, hv);
                if (n >= 58 && (n - 58) % 6 == 0 && (n - 58) / 6 < 7) w2f[14 + (n - 58) / 6] = W2F(14 + (n - 58) / 6);
                SB();
            }
        }
        asm volatile("s_nop 7\n\ts_nop 1" : "+v"(acc[4]), "+v"(acc[5]), "+v"(acc[6]), "+v"(acc[7]));
        STAMP(15);
        P4_EPILOGUE(2);
        WAIT_VM(0);
        BAR();
        if (tid == 0) __hip_atomic_store((GAS u32*)(ws + WS_F4 + (RB4 * 2 + 1) * FGRP + cg4 * FSTRIDE), tag, __ATOMIC_RELAXED, __HIP_MEMORY_SCOPE_AGENT);
    }
    STAMP(16);
    SB();
#pragma unroll
    for (int s = 21; s < 28; ++s) w2f[s] = W2F(s);
    SB();
    {
        const u32x4 bb = *(const u32x4*)((const char*)b2 + cg * 128 + fo8 * 16);
        const u32 f4o = WS_F4 + (u32)(RBW * 2) * FGRP + (7 * kh + (l < 7 ? l : 0)) * FSTRIDE;
        wait_flags(ws, f4o, tag);
        STAMP(17);
        SB();
        u32 f4b = 0;
        const char* hsA = ws + WS_HH + (size_t)(TTG(cw & 1) * 56 + 28 * kh + (cw >> 1) + rot5) * 1024;
        const char* hsB = ws + WS_HH + (size_t)(TTG(2 + (cw & 1)) * 56 + 28 * kh + (cw >> 1) + rot5) * 1024;
        const int wrap5 = (28 - rot5) >> 1;
#define H5SRC(base, i) ((base) + ((i) < wrap5 ? (i) * 2048 : (i) * 2048 - 28 * 1024))
        const u32 hl = kh * L_H5 + (cw >> 1) * 2048 + (cw & 1) * 1024;
        const u32 hv = l * 16;
        dma_seek(hl - 4096);
#pragma unroll
        for (int i = 0; i < 6; ++i) dma_next_sc1_b<4096>(H5SRC(hsA, i), hv);
        f32x4 acc[4];
        u32x4 af[8];
        u32 ab0 = kh * L_H5 + l * 16, ab1 = kh * L_H5 + l * 16 + 65536;
        OPAQUE(ab0); OPAQUE(ab1);
#define H5_KS(n) (((n) % 56) >> 1)
#define H5_SLOT(n) ((n) < 56 ? H5_KS(n) : H5_KS(n) < 12 ? 28 + H5_KS(n) : H5_KS(n) - 12)
#define H5_BLK(n) (H5_SLOT(n) * 2 + ((n) & 1))
#define H5_ADDR(n) (H5_BLK(n) < 64 ? ab0 + H5_BLK(n) * 1024 : ab1 + (H5_BLK(n) - 64) * 1024)
        wait_vm<4>();
        STAMP(21);
        BAR();
#pragma unroll
        for (int n = 0; n < 6; ++n) af[n] = lds_ld128(H5_ADDR(n));
        SB();
#pragma unroll
        for (int seg = 0; seg < 9; ++seg) {
            constexpr int SB0[10] = {0, 2, 10, 26, 42, 50, 66, 82, 98, 112};
            if (seg == 1) { wait_vm<3>(); BAR0(); }
            if (seg == 2) {
                wait_vm<2>(); BAR0();
                asm volatile("global_load_dword %0, %1, %2 sc1" : "=v"(f4b) : "v"(f4o + FGRP), "s"(ws) : "memory");
            }
            if (seg == 3) {
                wait_vm<2>();
                asm volatile("" : "+v"(f4b));
                if (!__all(f4b == tag)) wait_flags(ws, f4o + FGRP, tag);
                BAR0();
            }
            if (seg == 4) { wait_vm<5>(); BAR0(); }
            if (seg == 5) { wait_vm<4>(); BAR0(); }
            if (seg == 6) { wait_vm<5>(); BAR0(); }
            if (seg == 7) { wait_vm<2>(); BAR0(); }
            if (seg == 8) { wait_vm<0>(); BAR0(); }
#pragma unroll
            for (int n = SB0[seg]; n < SB0[seg + 1]; ++n) {
                const int ai = (n / 56) * 2 + (n & 1);
                if (H5_KS(n) == 0) mfma_first(acc[ai], w2f[H5_KS(n)], af[n & 7]); else mfma_acc(acc[ai], w2f[H5_KS(n)], af[n & 7]);
                if (n + 6 < 112) af[(n + 6) & 7] = lds_ld128(H5_ADDR(n + 6));
                if (n <= 21 && n % 3 == 0) dma_next_sc1_b<4096>(H5SRC(hsA, 6 + n / 3), hv);
                if (n == 28) dma_seek(hl + 28 * 2048 - 4096);
                if (n == 46) dma_seek(hl - 4096);
                if (n >= 28 && n <= 67 && (n - 28) % 3 == 0) dma_next_sc1_b<4096>(H5SRC(hsB, (n - 28) / 3), hv);
                SB();
            }
        }
        MFMA_FENCE4(acc);
        STAMP(18);
        const u32 rw = (w < 6 ? 32768 + w * 4096 : L_H5 + 32768 + (w - 6) * 4096);
#pragma unroll
        for (int tt = 0; tt < 4; ++tt) lds_stf4(rw + tt * 1024 + l * 16, acc[tt]);
        BAR();
        const int c5 = fo8 >> 1;
        const u32 lp = (w >> 1) * 1024 + ((2 * (fo8 & 1)) * 16 + tl) * 16;
        const u32 r0a = 32768 + c5 * 4096 + lp, r1a = (c5 < 2 ? 32768 + (4 + c5) * 4096 : L_H5 + 32768 + (c5 - 2) * 4096) + lp;
        const f32x4 a0 = lds_ldf4(r0a) + lds_ldf4(r1a), a1 = lds_ldf4(r0a + 256) + lds_ldf4(r1a + 256);
        const u32x4 o = {pack2(a0[0] + bflo(bb[0]) + bflo(hres[0]), a0[1] + bfhi(bb[0]) + bfhi(hres[0])),
                         pack2(a0[2] + bflo(bb[1]) + bflo(hres[1]), a0[3] + bfhi(bb[1]) + bfhi(hres[1])),
                         pack2(a1[0] + bflo(bb[2]) + bflo(hres[2]), a1[1] + bfhi(bb[2]) + bfhi(hres[2])),
                         pack2(a1[2] + bflo(bb[3]) + bflo(hres[3]), a1[3] + bfhi(bb[3]) + bfhi(hres[3]))};
        *(u32x4*)((char*)out + (size_t)tok * 896 + cg * 128 + fo8 * 16) = o;
    }
    STAMP(19);
    STAMP_FLUSH();
}

extern "C" void kernel_launch(void* const* d_in, const int* in_sizes, int n_in, void* d_out, int out_size, void* d_ws, size_t ws_size,
                              hipStream_t stream) {
    (void)hipFuncSetAttribute((const void*)fused_kernel, hipFuncAttributeMaxDynamicSharedMemorySize, LDS_BYTES);
    fused_kernel<<<256, 512, LDS_BYTES, stream>>>((const u16*)d_in[0], (const u16*)d_in[1], (const u16*)d_in[2], (const u16*)d_in[4],
                                                  (const u16*)d_in[5], (const u16*)d_in[6], (const u16*)d_in[7], (u16*)d_out, (char*)d_ws);
}
```

```cpp
#include <hip/hip_runtime.h>
#include <stdint.h>

typedef unsigned short u16;
typedef unsigned int u32;
typedef __attribute__((ext_vector_type(8))) __bf16 bf16x8;
typedef __attribute__((ext_vector_type(2))) __bf16 bf16x2;
typedef __attribute__((ext_vector_type(4))) short s16x4;
typedef __attribute__((ext_vector_type(4))) float f32x4;
typedef __attribute__((ext_vector_type(2))) float f32x2;
typedef __attribute__((ext_vector_type(4))) u32 u32x4;
typedef __attribute__((ext_vector_type(2))) u32 u32x2;
#define LAS __attribute__((address_space(3)))
#define GAS __attribute__((address_space(1)))
#define DI __device__ __forceinline__

#define FSTRIDE 64
constexpr u32 FGRP = 16 * FSTRIDE < 128 ? 128 : 16 * FSTRIDE;
constexpr u32 WS_GEN = 0;
constexpr u32 WS_FW = 4096;
constexpr u32 WS_F1 = WS_FW + 448 * 64;
constexpr u32 WS_F2 = WS_F1 + 256 * FGRP;
constexpr u32 WS_F3 = WS_F2 + 128 * FGRP;
constexpr u32 WS_F4 = WS_F3 + 128 * FGRP;
constexpr u32 WS_KV = WS_F4 + 32 * FGRP;
constexpr u32 WS_O = WS_KV + 16 * 16 * 16384;
constexpr u32 WS_H = WS_O + 128 * 16 * 1024;
constexpr u32 WS_ST = WS_H + 128 * 14 * 1024;
constexpr u32 WS_HH = WS_ST + 128 * 7 * 128;
constexpr u32 WS_W1F = WS_HH + 128 * 56 * 1024;
constexpr u32 WS_W2F = WS_W1F + 112 * 14 * 1024;
constexpr u32 WS_WOF = WS_W2F + 28 * 56 * 1024;
constexpr u32 WS_END = WS_WOF + 28 * 16 * 1024;
constexpr u32 L_WA = 114688;
constexpr u32 L_GEN = 157696;
constexpr u32 L_ST1 = 157952;
constexpr u32 L_CS1 = 158976;
constexpr u32 L_Q = 49152;
constexpr u32 L_PP = 98304;
constexpr u32 L_K = 0, L_V = 24576;
constexpr u32 L_RED3 = 65536;
constexpr u32 L_ST4 = 114688;
constexpr u32 L_H5 = 81920;
constexpr u32 L_RED5 = 32768;
constexpr u32 LDS_BYTES = 163840;
constexpr u32 CODE_AHEAD = 30 * 1024;
constexpr int SPIN_MAX = 1 << 14;

DI f32x4 mfma16(u32x4 a, u32x4 b, f32x4 c) {
    return __builtin_amdgcn_mfma_f32_16x16x32_bf16(__builtin_bit_cast(bf16x8, a), __builtin_bit_cast(bf16x8, b), c, 0, 0, 0);
}
DI void mfma_acc(f32x4& acc, const u32x4& a, const u32x4& b) {
    asm volatile("v_mfma_f32_16x16x32_bf16 %0, %1, %2, %0" : "+v"(acc) : "v"(a), "v"(b));
}
DI void mfma_first(f32x4& acc, const u32x4& a, const u32x4& b) {
    asm volatile("v_mfma_f32_16x16x32_bf16 %0, %1, %2, 0" : "=&v"(acc) : "v"(a), "v"(b));
}
#define MFMA_FENCE4(a) asm volatile("s_nop 7\n\ts_nop 1" : "+v"(a[0]), "+v"(a[1]), "+v"(a[2]), "+v"(a[3]))
#define OPAQUE(x) asm volatile("" : "+v"(x))
DI u32 pack2(float lo, float hi) {
    f32x2 v = {lo, hi};
    bf16x2 r = __builtin_convertvector(v, bf16x2);
    return __builtin_bit_cast(u32, r);
}
DI float dot2bf(u32 a, u32 b, float c) {
    return __builtin_amdgcn_fdot2_f32_bf16(__builtin_bit_cast(bf16x2, a), __builtin_bit_cast(bf16x2, b), c, false);
}
DI float bflo(u32 u) { return __uint_as_float(u << 16); }
DI float bfhi(u32 u) { return __uint_as_float(u & 0xffff0000u); }
DI u32x2 trread(u32 off) {
    s16x4 r = __builtin_amdgcn_ds_read_tr16_b64_v4i16((LAS s16x4*)(uintptr_t)off);
    return __builtin_bit_cast(u32x2, r);
}
DI u32x4 trfrag(u32 off0, u32 off1) {
    u32x2 a = trread(off0), b = trread(off1);
    u32x4 r = {a[0], a[1], b[0], b[1]};
    return r;
}
DI u32x4 lds_ld128(u32 off) { return *(LAS u32x4*)(uintptr_t)off; }
DI f32x4 lds_ldf4(u32 off) { return *(LAS f32x4*)(uintptr_t)off; }
DI void lds_st128(u32 off, u32x4 v) { *(LAS u32x4*)(uintptr_t)off = v; }
DI void lds_stf4(u32 off, f32x4 v) { *(LAS f32x4*)(uintptr_t)off = v; }
DI void lds_st64(u32 off, u32x2 v) { *(LAS u32x2*)(uintptr_t)off = v; }
DI void lds_stf(u32 off, float v) { *(LAS float*)(uintptr_t)off = v; }
template <int N>
DI void glds16_run(const void* sbase, u32 voff, u32 lds, u32 gstep, u32 lstep) {
    u32 keep;
    asm volatile("s_mov_b32 %0, m0\n\ts_mov_b32 m0, %3\n\t"
                 ".rept %c6\n\t"
                 "s_nop 0\n\t"
                 "global_load_lds_dwordx4 %1, %2\n\t"
                 "s_add_u32 m0, m0, %5\n\t"
                 "v_add_u32 %1, %4, %1\n\t"
                 ".endr\n\t"
                 "s_mov_b32 m0, %0"
                 : "=&s"(keep), "+v"(voff) : "s"(sbase), "s"(lds), "s"(gstep), "s"(lstep), "i"(N) : "memory", "scc");
}
template <int N>
DI void glds16_run_sc1(const void* sbase, u32 voff, u32 lds, u32 gstep, u32 lstep) {
    u32 keep;
    asm volatile("s_mov_b32 %0, m0\n\ts_mov_b32 m0, %3\n\t"
                 ".rept %c6\n\t"
                 "s_nop 0\n\t"
                 "global_load_lds_dwordx4 %1, %2 sc1\n\t"
                 "s_add_u32 m0, m0, %5\n\t"
                 "v_add_u32 %1, %4, %1\n\t"
                 ".endr\n\t"
                 "s_mov_b32 m0, %0"
                 : "=&s"(keep), "+v"(voff) : "s"(sbase), "s"(lds), "s"(gstep), "s"(lstep), "i"(N) : "memory", "scc");
}
DI void glds16_one(const void* sbase, u32 voff, u32 goff, u32 lds) {
    u32 tmp;
    asm volatile("s_mov_b32 m0, %4\n\tv_add_u32 %0, %3, %1\n\tglobal_load_lds_dwordx4 %0, %2" : "=&v"(tmp) : "v"(voff), "s"(sbase), "s"(goff), "s"(lds) : "memory");
}
DI void dma_seek(u32 lds) { asm volatile("s_mov_b32 m0, %0" :: "s"(lds) : "memory"); }
template <int LSTEP, int GSTEP>
DI void dma_next_sc1(const void* sbase, u32& voff) {
    asm volatile("s_add_u32 m0, m0, %c2\n\tv_add_u32 %0, %c3, %0\n\tglobal_load_lds_dwordx4 %0, %1 sc1" : "+v"(voff) : "s"(sbase), "i"(LSTEP), "i"(GSTEP) : "memory", "scc");
}
template <int LSTEP>
DI void dma_next_sc1_b(const void* sbase, u32 voff) {
    asm volatile("s_add_u32 m0, m0, %c2\n\ts_nop 0\n\tglobal_load_lds_dwordx4 %0, %1 sc1" :: "v"(voff), "s"(sbase), "i"(LSTEP) : "memory", "scc");
}
#define PIN_S(x) asm volatile("" : "+s"(x))
template <int N>
DI void glds4_run(const void* sbase, u32 voff, u32 lds, u32 gstep) {
    u32 keep;
    asm volatile("s_mov_b32 %0, m0\n\ts_mov_b32 m0, %3\n\ts_nop 0\n\t"
                 ".rept %c5\n\t"
                 "global_load_lds_dword %1, %2\n\t"
                 "v_add_u32 %1, %4, %1\n\t"
                 ".endr\n\t"
                 "s_mov_b32 m0, %0"
                 : "=&s"(keep), "+v"(voff) : "s"(sbase), "s"(lds), "s"(gstep), "i"(N) : "memory");
}
#define SB() __builtin_amdgcn_sched_barrier(0)
#define WAIT_VM(n) asm volatile("s_waitcnt vmcnt(" #n ")" ::: "memory")
template <int N> DI void wait_vm() { asm volatile("s_waitcnt vmcnt(%c0)" :: "i"(N) : "memory"); }
#define BAR0() asm volatile("s_barrier" ::: "memory")
#define BAR() asm volatile("s_waitcnt lgkmcnt(0)\n\ts_barrier" ::: "memory")
DI float xor16(float v) { return __shfl_xor(v, 16); }
DI float xor32(float v) { return __shfl_xor(v, 32); }

typedef __amdgpu_buffer_rsrc_t rsrc_t;
DI u32x4 ld_sc1(rsrc_t r, u32 off) { return __builtin_amdgcn_raw_buffer_load_b128(r, off, 0, 16); }
DI u32x2 ld_sc1_64(rsrc_t r, u32 off) { return __builtin_amdgcn_raw_buffer_load_b64(r, off, 0, 16); }
DI void st_sc1(rsrc_t r, u32 off, u32x4 v) { __builtin_amdgcn_raw_buffer_store_b128(v, r, off, 0, 16); }
DI void st_sc1_64(rsrc_t r, u32 off, u32x2 v) { __builtin_amdgcn_raw_buffer_store_b64(v, r, off, 0, 16); }
DI void set_flag(char* ws, u32 off, u32 tag, int lane) {
    if (lane == 0) __hip_atomic_store((GAS u32*)(ws + off), tag, __ATOMIC_RELAXED, __HIP_MEMORY_SCOPE_AGENT);
}
DI void wait_flags(char* ws, u32 off, u32 tag) {
    for (int it = 0; it < SPIN_MAX; ++it) {
        u32 v = __hip_atomic_load((GAS u32*)(ws + off), __ATOMIC_RELAXED, __HIP_MEMORY_SCOPE_AGENT);
        if (__all(v == tag)) break;
#define PSLEEP 1
        if (PSLEEP) __builtin_amdgcn_s_sleep(PSLEEP);
    }
    asm volatile("" ::: "memory");
}

#define STAMP_DECL do {} while (0)
#define STAMP(I) do {} while (0)
#define STAMP_FLUSH() do {} while (0)

__global__ __launch_bounds__(512, 2) __attribute__((amdgpu_waves_per_eu(2, 2)))
void fused_kernel(const u16* __restrict__ x, const u16* __restrict__ wqkv, const u16* __restrict__ wo, const u16* __restrict__ w1,
                  const u16* __restrict__ b1, const u16* __restrict__ w2, const u16* __restrict__ b2, u16* __restrict__ out,
                  char* __restrict__ ws) {
    const int b = blockIdx.x, xcd = b & 7, loc = b >> 3;
    const int tid = threadIdx.x, w = __builtin_amdgcn_readfirstlane(tid >> 6), l = tid & 63, t = l & 15, g = l >> 4;
    const int q4 = t >> 2, p4 = l & 3;
    const rsrc_t rs = __builtin_amdgcn_make_buffer_rsrc(ws, 0, WS_END, 0x00020000);
    STAMP_DECL;
    STAMP(0);
    const int RB = 4 * (xcd >> 1) + (loc >> 3), h = 8 * (xcd & 1) + (loc & 7);
    const bool odd = h & 1;
    const int tp = w >> 1, ch = w & 1;
    const char* wqb = (const char*)wqkv + 56 * h - (h & 1) * 8;
    const u32 wqv = (l >> 2) * 2688 + (((l & 3) ^ ((l >> 5) << 1)) * 16);
    {
        const char* xb = (const char*)x + (size_t)(128 * RB + 16 * w) * 896;
        glds16_run<14>(xb, (l >> 2) * 896 + (((l & 3) ^ ((-(l >> 4)) & 3)) * 16), w * 14336, 64, 1024);
        asm volatile("s_barrier" ::: "memory");
        if (w < 7) {
#pragma unroll
            for (int hf = 0; hf < 2; ++hf) glds16_run<3>(wqb + (32 * w + 16 * hf) * 2688, wqv, L_WA + w * 6144 + hf * 1024, 896, 2048);
        } else {
            asm volatile("s_mov_b32 m0, %2\n\ts_nop 0\n\tglobal_load_lds_dword %0, %1 sc1" :: "v"(0), "s"(ws + WS_GEN), "s"(L_GEN) : "memory");
            {
                unsigned long long pc;
                asm volatile("s_getpc_b64 %0" : "=s"(pc));
                const u32 line = (u32)((loc & 3) * 64 + l);
                asm volatile("s_mov_b32 m0, %2\n\ts_nop 0\n\tglobal_load_lds_dword %0, %1" :: "v"(line * 128u < CODE_AHEAD ? line * 128u : 0u), "s"(pc & ~127ull), "s"(L_GEN + 256) : "memory");
            }
            WAIT_VM(2);
        }
    }
    const int pp = b + 256 * (w >> 2);
    const char* ppsrc; u32 pppitch, ppdst;
    {
        const int pe = pp < 448 ? pp : 447;
        const int c = w & 3;
        if (pe < 196) { ppsrc = (const char*)w1 + (pe / 7) * 128 + (size_t)(pe % 7) * (64 * 3584); pppitch = 3584;
                        ppdst = WS_W1F + (u32)((((pe / 7) >> 1) * 8 + ((pe / 7) & 1) * 4 + c) * 14 + 2 * (pe % 7)) * 1024; }
        else if (pe < 392) { const int i = pe - 196; ppsrc = (const char*)w2 + (i / 28) * 128 + (size_t)(i % 28) * (64 * 896); pppitch = 896;
                             ppdst = WS_W2F + (u32)(((i / 28) * 4 + c) * 56 + 2 * (i % 28)) * 1024; }
        else { const int i = pe - 392; ppsrc = (const char*)wo + (i >> 3) * 128 + (size_t)(i & 7) * (56 * 896); pppitch = 896;
               ppdst = WS_WOF + (u32)(((i >> 3) * 4 + c) * 16 + 2 * (i & 7)) * 1024; }
    }
    const bool ppshort = pp >= 392 && (w & 3) == 3;
    const u32 ppv = (l >> 3) * pppitch + (l & 7) * 16;
    u32 ppg0 = (u32)(16 * (w & 3)) * pppitch, ppg1 = ppshort ? ppg0 : ppg0 + 8 * pppitch;
    u32 ppl0 = L_PP + (w >> 2) * 8192 + (w & 3) * 2048, ppl1 = ppshort ? ppl0 : ppl0 + 1024;
    const u32 ppa = L_PP + (w >> 2) * 8192 + (w & 3) * 32 + 8 * p4 + (pp < 392 ? (8 * g + q4) * 128 : (4 * g + q4) * 128);
    const u32 ppk = pp < 392 ? 4096 : 3072, pph = pp < 392 ? 512 : 2048;
    u32 wg[6], wl[6], wpa[6]; bool wpat[6];
#pragma unroll
    for (int j = 0; j < 6; ++j) {
        const int idx = 7 * j + (w < 7 ? w : 6), ks = idx / 6, r = idx - 6 * ks;
        wg[j] = (u32)((32 * (7 + ks) + 16 * (r & 1)) * 2688 + (r >> 1) * 896);
        wl[j] = (u32)(ks * 14336 + (r >> 1) * 2048 + (r & 1) * 1024);
        wpat[j] = r < 4;
        wpa[j] = wl[j] + (l & 15) * 64 + ((((odd ? 0 : 1) ^ (((r & 1) * 16 + (l & 15)) >> 3 & 1)) * 32) + (odd ? 0 : 24));
        PIN_S(wg[j]); PIN_S(wl[j]);
    }
    PIN_S(ppg0); PIN_S(ppg1); PIN_S(ppl0); PIN_S(ppl1); PIN_S(ppdst);
    WAIT_VM(6);
    BAR();
    STAMP(1);
    u32x4 xf[2][14];
    {
        const u32 xl = t * 64 + ((g ^ ((-(t >> 2)) & 3)) * 16);
        const u32 xa = w * 14336 + xl, xb2 = (w ^ 1) * 14336 + xl;
#pragma unroll
        for (int s = 0; s < 14; ++s) { xf[0][s] = lds_ld128(xa + s * 1024); xf[1][s] = lds_ld128(xb2 + s * 1024); }
    }
    const u32 padoff = (l >> 5) * 2048 + (l & 31) * 64 + ((((odd ? 0 : 1) ^ ((l >> 3) & 1)) * 32) + (odd ? 0 : 24));
    WAIT_VM(0);
    if (w < 7) lds_st64(L_WA + w * 6144 + padoff, (u32x2){0x3f803f80u, 0x3f803f80u});
    BAR();
    const u32 tag = __builtin_amdgcn_readfirstlane(*(LAS u32*)(uintptr_t)L_GEN) + 1u;
    STAMP(2);
    SB();
    {
        f32x4 acc[2][3], cs[3];
        u32 lbA[2], lbB[2], lbC[2];
        {
            const u32 h0 = (8 * g + q4) * 64 + ((g & 1) * 32) + p4 * 8, h1 = (8 * g + q4) * 64 + (((g & 1) ^ 1) * 32) + p4 * 8;
            lbA[0] = h0 + ch * 2048; lbA[1] = h1 + ch * 2048;
            lbB[0] = (ch ? h1 : h0) + 4096;
        }
        u32 b1[3] = {lbA[0] + L_WA, lbA[1] + L_WA, lbB[0] + L_WA};
        u32 b2[3] = {lbA[0], lbA[1], lbB[0]};
        u32 b3[3] = {lbA[0] + 57344, lbA[1] + 57344, lbB[0] + 57344};
        OPAQUE(b1[0]); OPAQUE(b1[1]); OPAQUE(b1[2]); OPAQUE(b2[0]); OPAQUE(b2[1]); OPAQUE(b2[2]); OPAQUE(b3[0]); OPAQUE(b3[1]); OPAQUE(b3[2]);
        (void)lbC;
#define WQ_ADDR(s, c) ((s) < 7 ? b1[c] + (s) * 6144 : (s) < 11 ? b2[c] + ((s) - 7) * 14336 : b3[c] + ((s) - 11) * 14336)
        const u32x4 ones = {0x3f803f80u, 0x3f803f80u, 0x3f803f80u, 0x3f803f80u};
        f32x4 gram;
        u32x4 wf[2][3];
#pragma unroll
        for (int c = 0; c < 3; ++c) { const u32 a = WQ_ADDR(0, c); wf[0][c] = trfrag(a, a + 256); }
#pragma unroll
        for (int c = 0; c < 3; ++c) cs[c] = (f32x4){0.f, 0.f, 0.f, 0.f};
        SB();
#pragma unroll
        for (int s = 0; s < 14; ++s) {
            if (s == 6) glds16_one(ppsrc, ppv, ppg0, ppl0);
            if (s == 7) glds16_one(ppsrc, ppv, ppg1, ppl1);
            if (s == 6 || s == 8 || s == 11) {
                if (s == 6) WAIT_VM(5);
                if (s == 8) WAIT_VM(3);
                if (s == 11) WAIT_VM(0);
#pragma unroll
                for (int j = (s == 6 ? 0 : s == 8 ? 2 : 5); j < (s == 6 ? 2 : s == 8 ? 5 : 6); ++j)
                    if (wpat[j] && l < 16) lds_st64(wpa[j], (u32x2){0x3f803f80u, 0x3f803f80u});
                BAR();
            }
#pragma unroll
            for (int i = 0; i < 6; ++i) {
                const int c = i >> 1, tt = i & 1;
                if (c < 2) { if (s == 0) mfma_first(acc[tt][c], wf[s & 1][c], xf[tt][s]); else mfma_acc(acc[tt][c], wf[s & 1][c], xf[tt][s]); }
                else { if (s == 0) mfma_first(acc[tt][c], xf[tt][s], wf[s & 1][c]); else mfma_acc(acc[tt][c], xf[tt][s], wf[s & 1][c]); }
                if (s + 1 < 14) {
                    const u32x2 r = trread(WQ_ADDR(s + 1, c) + tt * 256);
                    wf[(s + 1) & 1][c][2 * tt] = r[0]; wf[(s + 1) & 1][c][2 * tt + 1] = r[1];
                }
                if (s < 6 && i == 2) glds16_one(wqb, wqv, wg[s < 6 ? s : 0], wl[s < 6 ? s : 0]);
                if (i == 5) { if (s == 0) mfma_first(gram, xf[0][s], xf[0][s]); else mfma_acc(gram, xf[0][s], xf[0][s]); }
                SB();
            }
            if ((s == 12 || s == 13) && pp < 448)
                st_sc1(rs, ppdst + (s - 12) * 1024 + l * 16, trfrag(ppa + (s - 12) * ppk, ppa + (s - 12) * ppk + pph));
            if ((s & 3) == tp) {
                cs[0] = mfma16(wf[s & 1][0], ones, cs[0]); cs[1] = mfma16(wf[s & 1][1], ones, cs[1]); cs[2] = mfma16(ones, wf[s & 1][2], cs[2]);
            }
            SB();
        }
        asm volatile("s_nop 7\n\ts_nop 1" : "+v"(acc[0][0]), "+v"(acc[0][1]), "+v"(acc[0][2]), "+v"(acc[1][0]), "+v"(acc[1][1]), "+v"(acc[1][2]), "+v"(gram));
        STAMP(3);
        {
            const float sum = __shfl(odd ? acc[0][0][0] : acc[0][1][0], odd ? t : 48 + t);
            const float dg = (t & 2) ? ((t & 1) ? gram[3] : gram[2]) : ((t & 1) ? gram[1] : gram[0]);
            const float sq = __shfl(dg, 16 * (t >> 2) + t);
            const float mean = sum * (1.f / 448.f);
            const float rstd = rsqrtf(fmaxf(sq * (1.f / 448.f) - mean * mean, 0.f) + 1e-5f);
            if (g == 0) lds_st64(L_ST1 + (16 * w + t) * 8, (u32x2){__float_as_uint(rstd), __float_as_uint(-mean * rstd)});
            if (t == 0) { lds_stf4(L_CS1 + ((tp * 6 + ch * 3 + 0) * 16 + 4 * g) * 4, cs[0]); lds_stf4(L_CS1 + ((tp * 6 + ch * 3 + 1) * 16 + 4 * g) * 4, cs[1]); }
            if (g == 0) lds_stf(L_CS1 + ((tp * 6 + ch * 3 + 2) * 16 + t) * 4, cs[2][0]);
        }
        BAR();
        STAMP(4);
        f32x4 csum[2]; float csv = 0.f;
        csum[0] = (f32x4){0.f, 0.f, 0.f, 0.f}; csum[1] = csum[0];
#pragma unroll
        for (int p = 0; p < 4; ++p) {
            csum[0] += lds_ldf4(L_CS1 + ((p * 6 + ch * 3 + 0) * 16 + 4 * g) * 4);
            csum[1] += lds_ldf4(L_CS1 + ((p * 6 + ch * 3 + 1) * 16 + 4 * g) * 4);
            csv += *(LAS float*)(uintptr_t)(L_CS1 + ((p * 6 + ch * 3 + 2) * 16 + t) * 4);
        }
        const float qs = ch ? 1.f : 0.18898223650461362f * 1.4426950408889634f;
        const float m0 = (odd && g == 0) ? 0.f : qs, m1 = (!odd && g == 3) ? 0.f : qs;
        const bool vpad = ch == 0 ? (odd && t < 4) : (!odd && t >= 12);
        const u32 blob = WS_KV + (u32)(RB * 16 + h) * 16384;
        u32x2 vv[2];
#pragma unroll
        for (int tt = 0; tt < 2; ++tt) {
            const int tile = w ^ tt;
            const u32x2 st = *(LAS u32x2*)(uintptr_t)(L_ST1 + (16 * tile + t) * 8);
            const float rstd = __uint_as_float(st[0]), mr = __uint_as_float(st[1]);
            float a[8];
#pragma unroll
            for (int i = 0; i < 4; ++i) { a[i] = fmaf(acc[tt][0][i], rstd, csum[0][i] * mr) * m0; a[4 + i] = fmaf(acc[tt][1][i], rstd, csum[1][i] * mr) * m1; }
            const u32x4 f = {pack2(a[0], a[1]), pack2(a[2], a[3]), pack2(a[4], a[5]), pack2(a[6], a[7])};
            if (ch) { st_sc1(rs, blob + tile * 1024 + l * 16, f); lds_st128(L_K + (16 + tile) * 1024 + l * 16, f); }
            else lds_st128(L_Q + tile * 1024 + l * 16, f);
            const u32x4 sa = lds_ld128(L_ST1 + (16 * tile + 4 * g) * 8), sb = lds_ld128(L_ST1 + (16 * tile + 4 * g + 2) * 8);
            const float v0 = fmaf(acc[tt][2][0], __uint_as_float(sa[0]), csv * __uint_as_float(sa[1]));
            const float v1 = fmaf(acc[tt][2][1], __uint_as_float(sa[2]), csv * __uint_as_float(sa[3]));
            const float v2 = fmaf(acc[tt][2][2], __uint_as_float(sb[0]), csv * __uint_as_float(sb[1]));
            const float v3 = fmaf(acc[tt][2][3], __uint_as_float(sb[2]), csv * __uint_as_float(sb[3]));
            vv[tt] = (u32x2){pack2(v0, v1), pack2(v2, v3)};
            if (vpad) vv[tt] = (u32x2){0x3f803f80u, 0x3f803f80u};
        }
        const u32x4 vfr = ch ? (u32x4){vv[1][0], vv[1][1], vv[0][0], vv[0][1]} : (u32x4){vv[0][0], vv[0][1], vv[1][0], vv[1][1]};
        st_sc1(rs, blob + 8192 + (tp * 2 + ch) * 1024 + l * 16, vfr);
        lds_st128(L_V + ((8 + tp) * 2 + ch) * 1024 + l * 16, vfr);
    }
    {
        WAIT_VM(0);
        set_flag(ws, WS_F1 + (u32)(RB * 16 + h) * FGRP + w * FSTRIDE, tag, l);
        BAR();
        if ((tid & 255) == 0 && pp < 448) __hip_atomic_store((GAS u32*)(ws + WS_FW + pp * 64), tag, __ATOMIC_RELAXED, __HIP_MEMORY_SCOPE_AGENT);
    }
    const u32x4 qf = lds_ld128(L_Q + w * 1024 + l * 16);
    STAMP(5);
    SB();
    const int cg4 = 7 * (xcd & 1) + loc % 7, RB4 = 4 * (xcd >> 1) + loc / 7;
    const int rq = loc / 7, cg = loc % 7;
    const int cw = w & 3, kh = w >> 2;
    const bool worker = loc < 28;
    {
        const int rbl = RB - 2 + (l & 1);
        const int rsafe = rbl >= 0 ? rbl : RB;
        wait_flags(ws, WS_F1 + (u32)(rsafe * 16 + h) * FGRP + ((l & 2) ? (w | 1) : w) * FSTRIDE, tag);
        STAMP(6);
#pragma unroll
        for (int blk = 0; blk < 2; ++blk) {
            const int rb = RB - 2 + blk;
            if (rb >= 0) glds16_run_sc1<2>(ws + WS_KV + (size_t)(rb * 16 + h) * 16384 + w * 1024, l * 16, L_K + blk * 8192 + w * 1024, 8192, L_V - L_K);
            else { lds_st128(L_K + blk * 8192 + tid * 16, (u32x4){0, 0, 0, 0}); lds_st128(L_V + blk * 8192 + tid * 16, (u32x4){0, 0, 0, 0}); }
        }
        WAIT_VM(0);
    }
    BAR();
    STAMP(7);
    u32 fwo;
    {
        const int cgs = worker ? cg : 0, cg4s = worker ? cg4 : 0;
        int pi = 392 + cgs * 8 + 4 * kh + (l & 3);
        if (l >= 4 && l < 11) pi = (2 * cg4s + (w >> 2)) * 7 + (l - 4);
        if (l >= 11 && l < 25) pi = 196 + cgs * 28 + 14 * kh + (l - 11);
        fwo = WS_FW + (u32)pi * 64;
    }
    const u32 fwv = __hip_atomic_load((GAS u32*)(ws + fwo), __ATOMIC_RELAXED, __HIP_MEMORY_SCOPE_AGENT);
    SB();
    u32x4 wof[8];
    u32x4 w1f[14];
    const u32 wofa = WS_WOF + (u32)(((worker ? cg : 0) * 4 + cw) * 16 + 8 * kh) * 1024 + l * 16;
    const u32 w1fa = WS_W1F + (u32)(((worker ? cg4 : 0) * 8 + w) * 14) * 1024 + l * 16;
#define PF2(i) do { if ((i) < 8) wof[(i) < 8 ? (i) : 0] = ld_sc1(rs, wofa + (i) * 1024); else w1f[(i) >= 8 ? (i) - 8 : 0] = ld_sc1(rs, w1fa + ((i) - 8) * 1024); } while (0)
    {
        const int base = w & ~1;
        f32x4 sc[18];
        {
            u32x4 kf[18];
#pragma unroll
            for (int a = 0; a < 18; ++a) kf[a] = lds_ld128(L_K + (base + a) * 1024 + l * 16);
            SB();
#pragma unroll
            for (int a = 0; a < 18; ++a) {
                sc[a] = mfma16(kf[a], qf, (f32x4){0.f, 0.f, 0.f, 0.f});
                SB();
            }
        }
        if (!__all(fwv == tag)) wait_flags(ws, fwo, tag);
        asm volatile("" ::: "memory");
        PF2(0); PF2(1);
        SB();
        u32x4 vf[18];
#pragma unroll
        for (int a = 0; a < 8; ++a) vf[a] = lds_ld128(L_V + (base >> 1) * 2048 + a * 1024 + l * 16);
        SB();
        const float NEG = -1e30f;
        const int qq = 256 + 16 * w + t;
#pragma unroll
        for (int a = 0; a < 2; ++a)
#pragma unroll
            for (int i = 0; i < 4; ++i) { const int kk = 16 * (base + a) + 4 * g + i; if (kk <= qq - 256) sc[a][i] = NEG; }
#pragma unroll
        for (int a = 16; a < 18; ++a)
#pragma unroll
            for (int i = 0; i < 4; ++i) { const int kk = 16 * (base + a) + 4 * g + i; if (kk > qq) sc[a][i] = NEG; }
        if (RB < 2) {
            asm volatile("" ::: "memory");
            const int tmin = 8 * (2 - RB);
#pragma unroll
            for (int a = 0; a < 18; ++a) if (base + a < tmin) sc[a] = (f32x4){NEG, NEG, NEG, NEG};
        }
        PF2(2); PF2(3);
        SB();
        float ma = NEG, mb = NEG;
#pragma unroll
        for (int a = 0; a < 18; a += 2) {
            ma = fmaxf(fmaxf(sc[a][0], sc[a][1]), ma); ma = fmaxf(fmaxf(sc[a][2], sc[a][3]), ma);
            mb = fmaxf(fmaxf(sc[a + 1][0], sc[a + 1][1]), mb); mb = fmaxf(fmaxf(sc[a + 1][2], sc[a + 1][3]), mb);
        }
        PF2(4); PF2(5);
        SB();
        float m = fmaxf(ma, mb);
        m = fmaxf(m, xor16(m)); m = fmaxf(m, xor32(m));
        f32x4 o0 = {0.f, 0.f, 0.f, 0.f}, o1 = {0.f, 0.f, 0.f, 0.f};
#pragma unroll
        for (int a = 0; a < 9; ++a) {
            if (2 * a + 8 < 18) {
                vf[2 * a + 8] = lds_ld128(L_V + (base >> 1) * 2048 + (2 * a + 8) * 1024 + l * 16);
                vf[2 * a + 9] = lds_ld128(L_V + (base >> 1) * 2048 + (2 * a + 9) * 1024 + l * 16);
            }
            if (a < 8) { PF2(6 + 2 * a); PF2(7 + 2 * a); }
            u32x4 pf;
            pf[0] = pack2(__builtin_amdgcn_exp2f(sc[2 * a][0] - m), __builtin_amdgcn_exp2f(sc[2 * a][1] - m));
            pf[1] = pack2(__builtin_amdgcn_exp2f(sc[2 * a][2] - m), __builtin_amdgcn_exp2f(sc[2 * a][3] - m));
            pf[2] = pack2(__builtin_amdgcn_exp2f(sc[2 * a + 1][0] - m), __builtin_amdgcn_exp2f(sc[2 * a + 1][1] - m));
            pf[3] = pack2(__builtin_amdgcn_exp2f(sc[2 * a + 1][2] - m), __builtin_amdgcn_exp2f(sc[2 * a + 1][3] - m));
            o0 = mfma16(vf[2 * a], pf, o0);
            o1 = mfma16(vf[2 * a + 1], pf, o1);
            SB();
        }
        float den = odd ? o0[0] : o1[0];
        den = __shfl(den, odd ? t : (48 + t));
        const float r = 1.f / den;
        const float m0 = (odd && g == 0) ? 0.f : r, m1 = (!odd && g == 3) ? 0.f : r;
        const u32x4 of = {pack2(o0[0] * m0, o0[1] * m0), pack2(o0[2] * m0, o0[3] * m0), pack2(o1[0] * m1, o1[1] * m1), pack2(o1[2] * m1, o1[3] * m1)};
        STAMP(8);
        st_sc1(rs, WS_O + (u32)((RB * 8 + w) * 16 + h) * 1024 + l * 16, of);
        WAIT_VM(0);
        set_flag(ws, WS_F2 + (u32)(RB * 8 + w) * FGRP + h * FSTRIDE, tag, l);
    }
    STAMP(9);
    SB();
    if (!worker) {
        if (b == 255) {
#pragma unroll
            for (int i = 0; i < 4; ++i) wait_flags(ws, WS_F1 + (u32)(w * 32 + i * 8 + (l >> 3)) * FGRP + (l & 7) * FSTRIDE, tag);
            BAR();
            if (tid == 0) __hip_atomic_store((GAS u32*)(ws + WS_GEN), tag, __ATOMIC_RELAXED, __HIP_MEMORY_SCOPE_AGENT);
        }
        STAMP_FLUSH();
        return;
    }
    const int RBW = 2 * xcd + (rq >> 1);
#define TTG(tt) (8 * RBW + 4 * ((tt) >> 1) + 2 * (rq & 1) + ((tt) & 1))
    const int fr = l >> 3, fo8 = l & 7;
    const int TTf = TTG(w >> 1), tl = 8 * (w & 1) + fr;
    const int tok = 16 * TTf + tl;
    const u32x4 xr = *(const u32x4*)((const char*)x + (size_t)tok * 896 + cg * 128 + fo8 * 16);
    SB();
    BAR();
    const int TTc = TTG(cw);
    wait_flags(ws, WS_F2 + (u32)TTc * FGRP + (8 * kh + (l & 7)) * FSTRIDE, tag);
    STAMP(10);
    SB();
    glds16_run_sc1<8>(ws + WS_O + (size_t)(TTc * 16 + 8 * kh) * 1024, l * 16, (cw * 16 + 8 * kh) * 1024, 1024, 1024);
    WAIT_VM(0);
    BAR();
    u32x4 w2f[28];
    const int rot5 = 4 * ((cg + 2 * rq) % 7);
    const u32 w2fa = WS_W2F + (u32)((cg * 4 + cw) * 56 + 28 * kh) * 1024 + l * 16;
#define W2F(i) ld_sc1(rs, w2fa + (u32)((i) + rot5 < 28 ? (i) + rot5 : (i) + rot5 - 28) * 1024)
    u32x4 hres;
    {
        f32x4 acc[4];
        u32x4 af[8];
        const u32 ab = (8 * kh) * 1024 + l * 16;
#pragma unroll
        for (int n = 0; n < 6; ++n) af[n] = lds_ld128(ab + (n & 3) * 16384 + (n >> 2) * 1024);
        SB();
#pragma unroll
        for (int n = 0; n < 32; ++n) {
            if (n < 4) mfma_first(acc[n & 3], wof[n >> 2], af[n & 7]); else mfma_acc(acc[n & 3], wof[n >> 2], af[n & 7]);
            if (n + 6 < 32) af[(n + 6) & 7] = lds_ld128(ab + ((n + 6) & 3) * 16384 + ((n + 6) >> 2) * 1024);
            if ((n & 3) == 1 && (n >> 2) < 7) w2f[n >> 2] = W2F(n >> 2);
            SB();
        }
        MFMA_FENCE4(acc);
        STAMP(11);
#pragma unroll
        for (int tt = 0; tt < 4; ++tt) lds_stf4(L_RED3 + (w * 4 + tt) * 1024 + l * 16, acc[tt]);
        BAR();
        const u32 ra = L_RED3 + (w >> 1) * 1024 + (fo8 >> 1) * 4096 + ((2 * (fo8 & 1)) * 16 + tl) * 16;
        const f32x4 a0 = lds_ldf4(ra) + lds_ldf4(ra + 16384), a1 = lds_ldf4(ra + 256) + lds_ldf4(ra + 16384 + 256);
        hres = (u32x4){pack2(bflo(xr[0]) + a0[0], bfhi(xr[0]) + a0[1]), pack2(bflo(xr[1]) + a0[2], bfhi(xr[1]) + a0[3]),
                       pack2(bflo(xr[2]) + a1[0], bfhi(xr[2]) + a1[1]), pack2(bflo(xr[3]) + a1[2], bfhi(xr[3]) + a1[3])};
        st_sc1(rs, WS_H + (u32)(TTf * 14 + 2 * cg + (fo8 >> 2)) * 1024 + ((fo8 & 3) * 16 + tl) * 16, hres);
        float sum = 0.f, sq = 0.f;
#pragma unroll
        for (int j = 0; j < 4; ++j) { const float a = bflo(hres[j]), c = bfhi(hres[j]); sum += a + c; sq = fmaf(a, a, sq); sq = fmaf(c, c, sq); }
        sum += __shfl_xor(sum, 1); sq += __shfl_xor(sq, 1); sum += __shfl_xor(sum, 2); sq += __shfl_xor(sq, 2);
        sum += __shfl_xor(sum, 4); sq += __shfl_xor(sq, 4);
        if (fo8 == 0) st_sc1_64(rs, WS_ST + (u32)(TTf * 7 + cg) * 128 + tl * 8, (u32x2){__float_as_uint(sum), __float_as_uint(sq)});
        WAIT_VM(0);
        set_flag(ws, WS_F3 + (u32)TTf * FGRP + (2 * cg + (w & 1)) * FSTRIDE, tag, l);
    }
    STAMP(12);
    SB();
#pragma unroll
    for (int s = 7; s < 14; ++s) w2f[s] = W2F(s);
    SB();
    f32x4 cs4, bias4;
    {
        const u32x4 ones = {0x3f803f80u, 0x3f803f80u, 0x3f803f80u, 0x3f803f80u};
        f32x4 c4 = {0.f, 0.f, 0.f, 0.f}, c5 = {0.f, 0.f, 0.f, 0.f};
#pragma unroll
        for (int s = 0; s < 14; s += 2) { c4 = mfma16(w1f[s], ones, c4); c5 = mfma16(w1f[s + 1], ones, c5); }
        cs4 = c4 + c5;
        const u32x2 bb = *(const u32x2*)((const char*)b1 + cg4 * 256 + w * 32 + g * 8);
        bias4 = (f32x4){bflo(bb[0]), bfhi(bb[0]), bflo(bb[1]), bfhi(bb[1])};
    }
    BAR();
    STAMP(13);
    wait_flags(ws, WS_F3 + (u32)(8 * RB4 + (w & 3) + ((l & 16) ? 4 : 0)) * FGRP + ((l & 15) < 14 ? (l & 15) : 0) * FSTRIDE, tag);
    STAMP(14);
    SB();
    {
        glds16_run_sc1<1>(ws + WS_ST + (size_t)((8 * RB4 + w) * 7) * 128, l * 16, L_ST4 + 1024 + w * 1024, 0, 0);
        const char* hsA = ws + WS_H + (size_t)((8 * RB4 + (w & 3)) * 14 + (w >> 2)) * 1024;
        const char* hsB = hsA + 4 * 14 * 1024;
        u32 hv = l * 16 - 2048;
        dma_seek((w >> 2) * 8192 + (w & 3) * 1024 - 16384);
#pragma unroll
        for (int i = 0; i < 4; ++i) dma_next_sc1<16384, 2048>(hsA, hv);
        f32x4 acc[8];
        u32x4 af[8];
        u32 ab0 = l * 16, ab1 = l * 16 + 65536;
        OPAQUE(ab1);
#define H4_BLK(n) ((((n) % 56) >> 2) * 8 + ((n) / 56) * 4 + ((n) & 3))
#define H4_ADDR(n) (H4_BLK(n) < 64 ? ab0 + H4_BLK(n) * 1024 : ab1 + (H4_BLK(n) - 64) * 1024)
#define P4_EPILOGUE(M0) do { _Pragma("unroll") for (int m = (M0); m < (M0) + 2; ++m) { \
            u32 a[2][2]; \
            _Pragma("unroll") for (int u = 0; u < 2; ++u) { \
                const int tt = 2 * m + u; \
                const u32x2 st = *(LAS u32x2*)(uintptr_t)(L_ST4 + (16 * tt + t) * 8); \
                const float rstd = __uint_as_float(st[0]), mr = __uint_as_float(st[1]); \
                float v[4]; \
                _Pragma("unroll") for (int i = 0; i < 4; ++i) { float z = fmaf(acc[tt][i], rstd, fmaf(cs4[i], mr, bias4[i])); z = fmaxf(z, 0.f); v[i] = z * z; } \
                a[u][0] = pack2(v[0], v[1]); a[u][1] = pack2(v[2], v[3]); \
            } \
              \
            const auto r0 = __builtin_amdgcn_permlane16_swap(a[0][0], a[1][0], false, false); \
            const auto r1 = __builtin_amdgcn_permlane16_swap(a[0][1], a[1][1], false, false); \
            const u32x4 o = {r0[0], r1[0], r0[1], r1[1]}; \
            const int TTo = 8 * RB4 + 2 * m + (g & 1); \
            st_sc1(rs, WS_HH + (u32)(TTo * 56 + 4 * cg4 + (w >> 1)) * 1024 + ((2 * (w & 1) + (g >> 1)) * 16 + t) * 16, o); \
        } } while (0)
        WAIT_VM(2);
        STAMP(20);
        BAR();
#pragma unroll
        for (int n = 0; n < 6; ++n) af[n] = lds_ld128(H4_ADDR(n));
        SB();
#pragma unroll
        for (int seg = 0; seg < 7; ++seg) {
            constexpr int SB0[8] = {0, 10, 26, 42, 50, 66, 82, 112};
            if (seg == 1) {
                wait_vm<2>();
                const u32x2 p0 = *(LAS u32x2*)(uintptr_t)(L_ST4 + 1024 + w * 1024 + g * 128 + t * 8);
                const u32x2 p1 = *(LAS u32x2*)(uintptr_t)(L_ST4 + 1024 + w * 1024 + (g + 4) * 128 + t * 8);
                float sum = __uint_as_float(p0[0]), sq = __uint_as_float(p0[1]);
                if (g < 3) { sum += __uint_as_float(p1[0]); sq += __uint_as_float(p1[1]); }
                sum += xor16(sum); sq += xor16(sq); sum += xor32(sum); sq += xor32(sq);
                const float mean = sum * (1.f / 448.f);
                const float rstd = rsqrtf(fmaxf(sq * (1.f / 448.f) - mean * mean, 0.f) + 1e-5f);
                if (g == 0) lds_st64(L_ST4 + (16 * w + t) * 8, (u32x2){__float_as_uint(rstd), __float_as_uint(-mean * rstd)});
                BAR0();
            }
            if (seg == 2) { wait_vm<4>(); BAR(); }
            if (seg == 3) { wait_vm<7>(); BAR0(); }
            if (seg == 4) { wait_vm<5>(); BAR0(); }
            if (seg == 5) { wait_vm<7>(); BAR0(); }
            if (seg == 6) {
                wait_vm<4>(); BAR0();
                if (tid == 0) __hip_atomic_store((GAS u32*)(ws + WS_F4 + (RB4 * 2) * FGRP + cg4 * FSTRIDE), tag, __ATOMIC_RELAXED, __HIP_MEMORY_SCOPE_AGENT);
            }
#pragma unroll
            for (int n = SB0[seg]; n < SB0[seg + 1]; ++n) {
                if (n == 56) {
                    asm volatile("s_nop 7\n\ts_nop 1" : "+v"(acc[0]), "+v"(acc[1]), "+v"(acc[2]), "+v"(acc[3]));
                    P4_EPILOGUE(0);
                    SB();
                }
                const int ai = (n / 56) * 4 + (n & 3), ks = (n % 56) >> 2;
                if (ks == 0) mfma_first(acc[ai], w1f[ks], af[n & 7]); else mfma_acc(acc[ai], w1f[ks], af[n & 7]);
                if (n + 6 < 112) af[(n + 6) & 7] = lds_ld128(H4_ADDR(n + 6));
                if (n == 4 || n == 8 || n == 12) dma_next_sc1<16384, 2048>(hsA, hv);
                if (n == 16) { hv = l * 16 - 2048; dma_seek((w >> 2) * 8192 + (4 + (w & 3)) * 1024 - 16384); }
                if (n >= 16 && n <= 40 && (n & 3) == 0) dma_next_sc1<16384, 2048>(hsB, hv);
                if (n >= 58 && (n - 58) % 6 == 0 && (n - 58) / 6 < 7) w2f[14 + (n - 58) / 6] = W2F(14 + (n - 58) / 6);
                SB();
            }
        }
        asm volatile("s_nop 7\n\ts_nop 1" : "+v"(acc[4]), "+v"(acc[5]), "+v"(acc[6]), "+v"(acc[7]));
        STAMP(15);
        P4_EPILOGUE(2);
        WAIT_VM(0);
        BAR();
        if (tid == 0) __hip_atomic_store((GAS u32*)(ws + WS_F4 + (RB4 * 2 + 1) * FGRP + cg4 * FSTRIDE), tag, __ATOMIC_RELAXED, __HIP_MEMORY_SCOPE_AGENT);
    }
    STAMP(16);
    SB();
#pragma unroll
    for (int s = 21; s < 28; ++s) w2f[s] = W2F(s);
    SB();
    {
        const u32x4 bb = *(const u32x4*)((const char*)b2 + cg * 128 + fo8 * 16);
        const u32 f4o = WS_F4 + (u32)(RBW * 2) * FGRP + (7 * kh + (l < 7 ? l : 0)) * FSTRIDE;
        wait_flags(ws, f4o, tag);
        STAMP(17);
        SB();
        u32 f4b = 0;
        const char* hsA = ws + WS_HH + (size_t)(TTG(cw & 1) * 56 + 28 * kh + (cw >> 1) + rot5) * 1024;
        const char* hsB = ws + WS_HH + (size_t)(TTG(2 + (cw & 1)) * 56 + 28 * kh + (cw >> 1) + rot5) * 1024;
        const int wrap5 = (28 - rot5) >> 1;
#define H5SRC(base, i) ((base) + ((i) < wrap5 ? (i) * 2048 : (i) * 2048 - 28 * 1024))
        const u32 hl = kh * L_H5 + (cw >> 1) * 2048 + (cw & 1) * 1024;
        const u32 hv = l * 16;
        dma_seek(hl - 4096);
#pragma unroll
        for (int i = 0; i < 6; ++i) dma_next_sc1_b<4096>(H5SRC(hsA, i), hv);
        f32x4 acc[4];
        u32x4 af[8];
        u32 ab0 = kh * L_H5 + l * 16, ab1 = kh * L_H5 + l * 16 + 65536;
        OPAQUE(ab0); OPAQUE(ab1);
#define H5_KS(n) (((n) % 56) >> 1)
#define H5_SLOT(n) ((n) < 56 ? H5_KS(n) : H5_KS(n) < 12 ? 28 + H5_KS(n) : H5_KS(n) - 12)
#define H5_BLK(n) (H5_SLOT(n) * 2 + ((n) & 1))
#define H5_ADDR(n) (H5_BLK(n) < 64 ? ab0 + H5_BLK(n) * 1024 : ab1 + (H5_BLK(n) - 64) * 1024)
        wait_vm<4>();
        STAMP(21);
        BAR();
#pragma unroll
        for (int n = 0; n < 6; ++n) af[n] = lds_ld128(H5_ADDR(n));
        SB();
#pragma unroll
        for (int seg = 0; seg < 9; ++seg) {
            constexpr int SB0[10] = {0, 2, 10, 26, 42, 50, 66, 82, 98, 112};
            if (seg == 1) { wait_vm<3>(); BAR0(); }
            if (seg == 2) {
                wait_vm<2>(); BAR0();
                asm volatile("global_load_dword %0, %1, %2 sc1" : "=v"(f4b) : "v"(f4o + FGRP), "s"(ws) : "memory");
            }
            if (seg == 3) {
                wait_vm<2>();
                asm volatile("" : "+v"(f4b));
                if (!__all(f4b == tag)) wait_flags(ws, f4o + FGRP, tag);
                BAR0();
            }
            if (seg == 4) { wait_vm<5>(); BAR0(); }
            if (seg == 5) { wait_vm<4>(); BAR0(); }
            if (seg == 6) { wait_vm<5>(); BAR0(); }
            if (seg == 7) { wait_vm<2>(); BAR0(); }
            if (seg == 8) { wait_vm<0>(); BAR0(); }
#pragma unroll
            for (int n = SB0[seg]; n < SB0[seg + 1]; ++n) {
                const int ai = (n / 56) * 2 + (n & 1);
                if (H5_KS(n) == 0) mfma_first(acc[ai], w2f[H5_KS(n)], af[n & 7]); else mfma_acc(acc[ai], w2f[H5_KS(n)], af[n & 7]);
                if (n + 6 < 112) af[(n + 6) & 7] = lds_ld128(H5_ADDR(n + 6));
                if (n <= 21 && n % 3 == 0) dma_next_sc1_b<4096>(H5SRC(hsA, 6 + n / 3), hv);
                if (n == 28) dma_seek(hl + 28 * 2048 - 4096);
                if (n == 46) dma_seek(hl - 4096);
                if (n >= 28 && n <= 67 && (n - 28) % 3 == 0) dma_next_sc1_b<4096>(H5SRC(hsB, (n - 28) / 3), hv);
                SB();
            }
        }
        MFMA_FENCE4(acc);
        STAMP(18);
        const u32 rw = (w < 6 ? 32768 + w * 4096 : L_H5 + 32768 + (w - 6) * 4096);
#pragma unroll
        for (int tt = 0; tt < 4; ++tt) lds_stf4(rw + tt * 1024 + l * 16, acc[tt]);
        BAR();
        const int c5 = fo8 >> 1;
        const u32 lp = (w >> 1) * 1024 + ((2 * (fo8 & 1)) * 16 + tl) * 16;
        const u32 r0a = 32768 + c5 * 4096 + lp, r1a = (c5 < 2 ? 32768 + (4 + c5) * 4096 : L_H5 + 32768 + (c5 - 2) * 4096) + lp;
        const f32x4 a0 = lds_ldf4(r0a) + lds_ldf4(r1a), a1 = lds_ldf4(r0a + 256) + lds_ldf4(r1a + 256);
        const u32x4 o = {pack2(a0[0] + bflo(bb[0]) + bflo(hres[0]), a0[1] + bfhi(bb[0]) + bfhi(hres[0])),
                         pack2(a0[2] + bflo(bb[1]) + bflo(hres[1]), a0[3] + bfhi(bb[1]) + bfhi(hres[1])),
                         pack2(a1[0] + bflo(bb[2]) + bflo(hres[2]), a1[1] + bfhi(bb[2]) + bfhi(hres[2])),
                         pack2(a1[2] + bflo(bb[3]) + bflo(hres[3]), a1[3] + bfhi(bb[3]) + bfhi(hres[3]))};
        *(u32x4*)((char*)out + (size_t)tok * 896 + cg * 128 + fo8 * 16) = o;
    }
    STAMP(19);
    STAMP_FLUSH();
}

extern "C" void kernel_launch(void* const* d_in, const int* in_sizes, int n_in, void* d_out, int out_size, void* d_ws, size_t ws_size,
                              hipStream_t stream) {
    (void)hipFuncSetAttribute((const void*)fused_kernel, hipFuncAttributeMaxDynamicSharedMemorySize, LDS_BYTES);
    fused_kernel<<<256, 512, LDS_BYTES, stream>>>((const u16*)d_in[0], (const u16*)d_in[1], (const u16*)d_in[2], (const u16*)d_in[4],
                                                  (const u16*)d_in[5], (const u16*)d_in[6], (const u16*)d_in[7], (u16*)d_out, (char*)d_ws);
}
```

```cpp
#include <hip/hip_runtime.h>
#include <stdint.h>

typedef unsigned short u16;
typedef unsigned int u32;
typedef __attribute__((ext_vector_type(8))) __bf16 bf16x8;
typedef __attribute__((ext_vector_type(2))) __bf16 bf16x2;
typedef __attribute__((ext_vector_type(4))) short s16x4;
typedef __attribute__((ext_vector_type(4))) float f32x4;
typedef __attribute__((ext_vector_type(2))) float f32x2;
typedef __attribute__((ext_vector_type(4))) u32 u32x4;
typedef __attribute__((ext_vector_type(2))) u32 u32x2;
#define LAS __attribute__((address_space(3)))
#define GAS __attribute__((address_space(1)))
#define DI __device__ __forceinline__

#define FSTRIDE 64
constexpr u32 FGRP = 16 * FSTRIDE < 128 ? 128 : 16 * FSTRIDE;
constexpr u32 WS_GEN = 0;
constexpr u32 WS_FW = 4096;
constexpr u32 WS_FQ = WS_FW + 448 * 64;
constexpr u32 WS_F1 = WS_FQ + 384 * 64;
constexpr u32 WS_F2 = WS_F1 + 256 * FGRP;
constexpr u32 WS_F3 = WS_F2 + 128 * FGRP;
constexpr u32 WS_F4 = WS_F3 + 128 * FGRP;
constexpr u32 WS_KV = WS_F4 + 32 * FGRP;
constexpr u32 WS_O = WS_KV + 16 * 16 * 16384;
constexpr u32 WS_H = WS_O + 128 * 16 * 1024;
constexpr u32 WS_ST = WS_H + 128 * 14 * 1024;
constexpr u32 WS_HH = WS_ST + 128 * 7 * 128;
constexpr u32 WS_W1F = WS_HH + 128 * 56 * 1024;
constexpr u32 WS_W2F = WS_W1F + 112 * 14 * 1024;
constexpr u32 WS_WOF = WS_W2F + 28 * 56 * 1024;
constexpr u32 WS_WQF = WS_WOF + 28 * 16 * 1024;
constexpr u32 WS_END = WS_WQF + 16 * 48 * 1024;
constexpr u32 L_WA = 114688;
constexpr u32 L_RT = 151552;
constexpr u32 L_GEN = 157696;
constexpr u32 L_ST1 = 157952;
constexpr u32 L_CS1 = 158976;
constexpr u32 L_Q = 49152;
constexpr u32 L_PP = 98304;
constexpr u32 L_K = 0, L_V = 24576;
constexpr u32 L_RED3 = 65536;
constexpr u32 L_ST4 = 114688;
constexpr u32 L_H5 = 81920;
constexpr u32 L_RED5 = 32768;
constexpr u32 LDS_BYTES = 163840;
constexpr u32 CODE_AHEAD = 30 * 1024;
constexpr int SPIN_MAX = 1 << 14;

DI f32x4 mfma16(u32x4 a, u32x4 b, f32x4 c) {
    return __builtin_amdgcn_mfma_f32_16x16x32_bf16(__builtin_bit_cast(bf16x8, a), __builtin_bit_cast(bf16x8, b), c, 0, 0, 0);
}
DI void mfma_acc(f32x4& acc, const u32x4& a, const u32x4& b) {
    asm volatile("v_mfma_f32_16x16x32_bf16 %0, %1, %2, %0" : "+v"(acc) : "v"(a), "v"(b));
}
DI void mfma_first(f32x4& acc, const u32x4& a, const u32x4& b) {
    asm volatile("v_mfma_f32_16x16x32_bf16 %0, %1, %2, 0" : "=&v"(acc) : "v"(a), "v"(b));
}
#define MFMA_FENCE4(a) asm volatile("s_nop 7\n\ts_nop 1" : "+v"(a[0]), "+v"(a[1]), "+v"(a[2]), "+v"(a[3]))
#define OPAQUE(x) asm volatile("" : "+v"(x))
DI u32 pack2(float lo, float hi) {
    f32x2 v = {lo, hi};
    bf16x2 r = __builtin_convertvector(v, bf16x2);
    return __builtin_bit_cast(u32, r);
}
DI float dot2bf(u32 a, u32 b, float c) {
    return __builtin_amdgcn_fdot2_f32_bf16(__builtin_bit_cast(bf16x2, a), __builtin_bit_cast(bf16x2, b), c, false);
}
DI float bflo(u32 u) { return __uint_as_float(u << 16); }
DI float bfhi(u32 u) { return __uint_as_float(u & 0xffff0000u); }
DI u32x2 trread(u32 off) {
    s16x4 r = __builtin_amdgcn_ds_read_tr16_b64_v4i16((LAS s16x4*)(uintptr_t)off);
    return __builtin_bit_cast(u32x2, r);
}
DI u32x4 trfrag(u32 off0, u32 off1) {
    u32x2 a = trread(off0), b = trread(off1);
    u32x4 r = {a[0], a[1], b[0], b[1]};
    return r;
}
DI u32x4 lds_ld128(u32 off) { return *(LAS u32x4*)(uintptr_t)off; }
DI f32x4 lds_ldf4(u32 off) { return *(LAS f32x4*)(uintptr_t)off; }
DI void lds_st128(u32 off, u32x4 v) { *(LAS u32x4*)(uintptr_t)off = v; }
DI void lds_stf4(u32 off, f32x4 v) { *(LAS f32x4*)(uintptr_t)off = v; }
DI void lds_st64(u32 off, u32x2 v) { *(LAS u32x2*)(uintptr_t)off = v; }
DI void lds_stf(u32 off, float v) { *(LAS float*)(uintptr_t)off = v; }
template <int N>
DI void glds16_run(const void* sbase, u32 voff, u32 lds, u32 gstep, u32 lstep) {
    u32 keep;
    asm volatile("s_mov_b32 %0, m0\n\ts_mov_b32 m0, %3\n\t"
                 ".rept %c6\n\t"
                 "s_nop 0\n\t"
                 "global_load_lds_dwordx4 %1, %2\n\t"
                 "s_add_u32 m0, m0, %5\n\t"
                 "v_add_u32 %1, %4, %1\n\t"
                 ".endr\n\t"
                 "s_mov_b32 m0, %0"
                 : "=&s"(keep), "+v"(voff) : "s"(sbase), "s"(lds), "s"(gstep), "s"(lstep), "i"(N) : "memory", "scc");
}
template <int N>
DI void glds16_run_sc1(const void* sbase, u32 voff, u32 lds, u32 gstep, u32 lstep) {
    u32 keep;
    asm volatile("s_mov_b32 %0, m0\n\ts_mov_b32 m0, %3\n\t"
                 ".rept %c6\n\t"
                 "s_nop 0\n\t"
                 "global_load_lds_dwordx4 %1, %2 sc1\n\t"
                 "s_add_u32 m0, m0, %5\n\t"
                 "v_add_u32 %1, %4, %1\n\t"
                 ".endr\n\t"
                 "s_mov_b32 m0, %0"
                 : "=&s"(keep), "+v"(voff) : "s"(sbase), "s"(lds), "s"(gstep), "s"(lstep), "i"(N) : "memory", "scc");
}
DI void glds16_one(const void* sbase, u32 voff, u32 goff, u32 lds) {
    u32 tmp;
    asm volatile("s_mov_b32 m0, %4\n\tv_add_u32 %0, %3, %1\n\tglobal_load_lds_dwordx4 %0, %2" : "=&v"(tmp) : "v"(voff), "s"(sbase), "s"(goff), "s"(lds) : "memory");
}
DI void dma_seek(u32 lds) { asm volatile("s_mov_b32 m0, %0" :: "s"(lds) : "memory"); }
template <int LSTEP, int GSTEP>
DI void dma_next_sc1(const void* sbase, u32& voff) {
    asm volatile("s_add_u32 m0, m0, %c2\n\tv_add_u32 %0, %c3, %0\n\tglobal_load_lds_dwordx4 %0, %1 sc1" : "+v"(voff) : "s"(sbase), "i"(LSTEP), "i"(GSTEP) : "memory", "scc");
}
template <int LSTEP>
DI void dma_next_sc1_b(const void* sbase, u32 voff) {
    asm volatile("s_add_u32 m0, m0, %c2\n\ts_nop 0\n\tglobal_load_lds_dwordx4 %0, %1 sc1" :: "v"(voff), "s"(sbase), "i"(LSTEP) : "memory", "scc");
}
#define PIN_S(x) asm volatile("" : "+s"(x))
template <int N>
DI void glds4_run(const void* sbase, u32 voff, u32 lds, u32 gstep) {
    u32 keep;
    asm volatile("s_mov_b32 %0, m0\n\ts_mov_b32 m0, %3\n\ts_nop 0\n\t"
                 ".rept %c5\n\t"
                 "global_load_lds_dword %1, %2\n\t"
                 "v_add_u32 %1, %4, %1\n\t"
                 ".endr\n\t"
                 "s_mov_b32 m0, %0"
                 : "=&s"(keep), "+v"(voff) : "s"(sbase), "s"(lds), "s"(gstep), "i"(N) : "memory");
}
#define SB() __builtin_amdgcn_sched_barrier(0)
#define WAIT_VM(n) asm volatile("s_waitcnt vmcnt(" #n ")" ::: "memory")
template <int N> DI void wait_vm() { asm volatile("s_waitcnt vmcnt(%c0)" :: "i"(N) : "memory"); }
#define BAR0() asm volatile("s_barrier" ::: "memory")
#define BAR() asm volatile("s_waitcnt lgkmcnt(0)\n\ts_barrier" ::: "memory")
DI float xor16(float v) { return __shfl_xor(v, 16); }
DI float xor32(float v) { return __shfl_xor(v, 32); }

typedef __amdgpu_buffer_rsrc_t rsrc_t;
DI u32x4 ld_sc1(rsrc_t r, u32 off) { return __builtin_amdgcn_raw_buffer_load_b128(r, off, 0, 16); }
DI u32x2 ld_sc1_64(rsrc_t r, u32 off) { return __builtin_amdgcn_raw_buffer_load_b64(r, off, 0, 16); }
DI void st_sc1(rsrc_t r, u32 off, u32x4 v) { __builtin_amdgcn_raw_buffer_store_b128(v, r, off, 0, 16); }
DI void st_sc1_64(rsrc_t r, u32 off, u32x2 v) { __builtin_amdgcn_raw_buffer_store_b64(v, r, off, 0, 16); }
DI void set_flag(char* ws, u32 off, u32 tag, int lane) {
    if (lane == 0) __hip_atomic_store((GAS u32*)(ws + off), tag, __ATOMIC_RELAXED, __HIP_MEMORY_SCOPE_AGENT);
}
DI void wait_flags(char* ws, u32 off, u32 tag) {
    for (int it = 0; it < SPIN_MAX; ++it) {
        u32 v = __hip_atomic_load((GAS u32*)(ws + off), __ATOMIC_RELAXED, __HIP_MEMORY_SCOPE_AGENT);
        if (__all(v == tag)) break;
#define PSLEEP 1
        if (PSLEEP) __builtin_amdgcn_s_sleep(PSLEEP);
    }
    asm volatile("" ::: "memory");
}

#define STAMP_DECL do {} while (0)
#define STAMP(I) do {} while (0)
#define STAMP_FLUSH() do {} while (0)

__global__ __launch_bounds__(512, 2) __attribute__((amdgpu_waves_per_eu(2, 2)))
void fused_kernel(const u16* __restrict__ x, const u16* __restrict__ wqkv, const u16* __restrict__ wo, const u16* __restrict__ w1,
                  const u16* __restrict__ b1, const u16* __restrict__ w2, const u16* __restrict__ b2, u16* __restrict__ out,
                  char* __restrict__ ws) {
    const int b = blockIdx.x, xcd = b & 7, loc = b >> 3;
    const int tid = threadIdx.x, w = __builtin_amdgcn_readfirstlane(tid >> 6), l = tid & 63, t = l & 15, g = l >> 4;
    const int q4 = t >> 2, p4 = l & 3;
    const rsrc_t rs = __builtin_amdgcn_make_buffer_rsrc(ws, 0, WS_END, 0x00020000);
    STAMP_DECL;
    STAMP(0);
    const int RB = 4 * (xcd >> 1) + (loc >> 3), h = 8 * (xcd & 1) + (loc & 7);
    const bool odd = h & 1;
    const int tp = w >> 1, ch = w & 1;
    {
        const char* xb = (const char*)x + (size_t)(128 * RB + 16 * w) * 896;
        const u32 xv = (l >> 2) * 896 + (((l & 3) ^ ((-(l >> 4)) & 3)) * 16);
        if (w < 7) {
            glds16_run<14>(xb, xv, w * 14336, 64, 1024);
            glds16_run<2>((const char*)x + (size_t)(128 * RB + 112) * 896 + w * 128, xv, 7 * 14336 + w * 2048, 64, 1024);
            asm volatile("s_barrier" ::: "memory");
            if (w < 6) {
                const char* wqb = (const char*)wqkv + 56 * h - (h & 1) * 8;
                const u32 wqv = (l >> 2) * 2688 + (((l & 3) ^ ((l >> 5) << 1)) * 16);
#pragma unroll
                for (int hf = 0; hf < 2; ++hf) glds16_run<3>(wqb + (32 * w + 16 * hf) * 2688, wqv, L_WA + w * 6144 + hf * 1024, 896, 2048);
            } else {
                WAIT_VM(0);
            }
            WAIT_VM(6);
        } else {
            u32 qd[2];
#pragma unroll
            for (int i = 0; i < 2; ++i) {
                const int q = 2 * b + i < 384 ? 2 * b + i : 383, qh = q / 24, r = q - 24 * qh, qs = r / 3, qm = r - 3 * qs;
                glds16_run<2>((const char*)wqkv + (size_t)(32 * (6 + qs)) * 2688 + qm * 896 + 56 * qh - (qh & 1) * 8, (l >> 2) * 2688 + (l & 3) * 16, L_RT + i * 2048, 16 * 2688, 1024);
                qd[i] = (u32)q;
            }
            asm volatile("s_mov_b32 m0, %2\n\ts_nop 0\n\tglobal_load_lds_dword %0, %1 sc1" :: "v"(0), "s"(ws + WS_GEN), "s"(L_GEN) : "memory");
            {
                unsigned long long pc;
                asm volatile("s_getpc_b64 %0" : "=s"(pc));
                const u32 line = (u32)((loc & 3) * 64 + l);
                asm volatile("s_mov_b32 m0, %2\n\ts_nop 0\n\tglobal_load_lds_dword %0, %1" :: "v"(line * 128u < CODE_AHEAD ? line * 128u : 0u), "s"(pc & ~127ull), "s"(L_GEN + 256) : "memory");
            }
            WAIT_VM(1);
            const u32 tg = __builtin_amdgcn_readfirstlane(*(LAS u32*)(uintptr_t)L_GEN) + 1u;
            if (2 * b < 384) {
#pragma unroll
                for (int i = 0; i < 2; ++i) {
                    const int q = (int)qd[i], qh = q / 24, r = q - 24 * qh, qs = r / 3, qm = r - 3 * qs;
#pragma unroll
                    for (int hf = 0; hf < 2; ++hf) {
                        const u32 a = L_RT + i * 2048 + (8 * g + q4) * 64 + hf * 32 + 8 * p4;
                        u32x4 f = trfrag(a, a + 256);
                        if (qm < 2 && ((qh & 1) ? (hf == 0 && t < 4) : (hf == 1 && t >= 12))) f = (u32x4){0x3f803f80u, 0x3f803f80u, 0x3f803f80u, 0x3f803f80u};
                        st_sc1(rs, WS_WQF + (u32)((qh * 8 + qs) * 6 + qm * 2 + hf) * 1024 + l * 16, f);
                    }
                }
            }
            asm volatile("s_barrier" ::: "memory");
            if (2 * b < 384) {
                WAIT_VM(0);
                if (l < 2) __hip_atomic_store((GAS u32*)(ws + WS_FQ + (2 * b + l) * 64), tg, __ATOMIC_RELAXED, __HIP_MEMORY_SCOPE_AGENT);
            }
        }
    }
    const int pp = b + 256 * (w >> 2);
    const bool ppok = pp < 224 ? (pp & 31) < 28 : pp < 448 ? (pp & 31) < 28 : pp < 504;
    const char* ppsrc; u32 pppitch, ppdst, ppfl;
    {
        const int pe = ppok ? pp : 0, c = w & 3;
        if (pe < 224) { const int rc = pe >> 5, cl = pe & 31; ppsrc = (const char*)w1 + cl * 128 + (size_t)rc * (64 * 3584); pppitch = 3584;
                        ppdst = WS_W1F + (u32)(((cl >> 1) * 8 + (cl & 1) * 4 + c) * 14 + 2 * rc) * 1024; ppfl = (u32)(cl * 7 + rc); }
        else if (pe < 448) { const int g2 = (pe - 224) >> 5, rc = pe & 31; ppsrc = (const char*)w2 + g2 * 128 + (size_t)rc * (64 * 896); pppitch = 896;
                             ppdst = WS_W2F + (u32)((g2 * 4 + c) * 56 + 2 * rc) * 1024; ppfl = (u32)(196 + g2 * 28 + rc); }
        else { const int i = pe - 448; ppsrc = (const char*)wo + (i >> 3) * 128 + (size_t)(i & 7) * (56 * 896); pppitch = 896;
               ppdst = WS_WOF + (u32)(((i >> 3) * 4 + c) * 16 + 2 * (i & 7)) * 1024; ppfl = (u32)(392 + i); }
    }
    const bool ppshort = pp >= 448 && (w & 3) == 3;
    const u32 ppv = (l >> 3) * pppitch + (l & 7) * 16;
    u32 ppg0 = (u32)(16 * (w & 3)) * pppitch, ppg1 = ppshort ? ppg0 : ppg0 + 8 * pppitch;
    u32 ppl0 = L_PP + (w >> 2) * 8192 + (w & 3) * 2048, ppl1 = ppshort ? ppl0 : ppl0 + 1024;
    const u32 ppa = L_PP + (w >> 2) * 8192 + (w & 3) * 32 + 8 * p4 + (pp < 448 ? (8 * g + q4) * 128 : (4 * g + q4) * 128);
    const u32 ppk = pp < 448 ? 4096 : 3072, pph = pp < 448 ? 512 : 2048;
    PIN_S(ppg0); PIN_S(ppg1); PIN_S(ppl0); PIN_S(ppl1); PIN_S(ppdst); PIN_S(ppfl);
    BAR();
    STAMP(1);
    u32x4 xf[2][14];
    {
        const u32 xl = t * 64 + ((g ^ ((-(t >> 2)) & 3)) * 16);
        const u32 xa = w * 14336 + xl, xb2 = (w ^ 1) * 14336 + xl;
#pragma unroll
        for (int s = 0; s < 14; ++s) { xf[0][s] = lds_ld128(xa + s * 1024); xf[1][s] = lds_ld128(xb2 + s * 1024); }
    }
    const u32 tag = __builtin_amdgcn_readfirstlane(*(LAS u32*)(uintptr_t)L_GEN) + 1u;
    WAIT_VM(0);
    if (w < 6) lds_st64(L_WA + w * 6144 + (l >> 5) * 2048 + (l & 31) * 64 + ((((odd ? 0 : 1) ^ ((l >> 3) & 1)) * 32) + (odd ? 0 : 24)), (u32x2){0x3f803f80u, 0x3f803f80u});
    if (w == 7) wait_flags(ws, WS_FQ + (u32)(h * 24 + (l < 24 ? l : 0)) * 64, tag);
    BAR();
    STAMP(2);
    {
        u32 wv = l * 16 + w * 1024 - 8192;
        dma_seek(w * 1024 - 8192);
#pragma unroll
        for (int j = 0; j < 6; ++j) dma_next_sc1<8192, 8192>(ws + WS_WQF + (size_t)(h * 48) * 1024, wv);
    }
    SB();
    {
        f32x4 acc[2][3], cs[3];
        u32 wb1[3] = {(u32)(2 * ch) * 1024 + l * 16, (u32)(2 * ch + 1) * 1024 + l * 16, (u32)(4 + ch) * 1024 + l * 16};
        u32 wb0[3];
        {
            const u32 h0 = (8 * g + q4) * 64 + ((g & 1) * 32) + p4 * 8, h1 = (8 * g + q4) * 64 + (((g & 1) ^ 1) * 32) + p4 * 8;
            wb0[0] = L_WA + h0 + ch * 2048; wb0[1] = L_WA + h1 + ch * 2048; wb0[2] = L_WA + (ch ? h1 : h0) + 4096;
        }
        OPAQUE(wb0[0]); OPAQUE(wb0[1]); OPAQUE(wb0[2]); OPAQUE(wb1[0]); OPAQUE(wb1[1]); OPAQUE(wb1[2]);
#define WQ_FRAG(s, c) ((s) < 6 ? trfrag(wb0[c] + (s) * 6144, wb0[c] + (s) * 6144 + 256) : lds_ld128(wb1[c] + ((s) - 6) * 6144))
        const u32x4 ones = {0x3f803f80u, 0x3f803f80u, 0x3f803f80u, 0x3f803f80u};
        f32x4 gram;
        u32x4 wf[2][3];
#pragma unroll
        for (int c = 0; c < 3; ++c) wf[0][c] = WQ_FRAG(0, c);
#pragma unroll
        for (int c = 0; c < 3; ++c) cs[c] = (f32x4){0.f, 0.f, 0.f, 0.f};
        SB();
#pragma unroll
        for (int s = 0; s < 14; ++s) {
            if (s == 5) { wait_vm<3>(); BAR0(); }
            if (s == 9) { wait_vm<2>(); BAR0(); }
            if (s == 11) { wait_vm<0>(); BAR(); }
            if (s == 6) glds16_one(ppsrc, ppv, ppg0, ppl0);
            if (s == 7) glds16_one(ppsrc, ppv, ppg1, ppl1);
#pragma unroll
            for (int i = 0; i < 6; ++i) {
                const int c = i >> 1, tt = i & 1;
                if (c < 2) { if (s == 0) mfma_first(acc[tt][c], wf[s & 1][c], xf[tt][s]); else mfma_acc(acc[tt][c], wf[s & 1][c], xf[tt][s]); }
                else { if (s == 0) mfma_first(acc[tt][c], xf[tt][s], wf[s & 1][c]); else mfma_acc(acc[tt][c], xf[tt][s], wf[s & 1][c]); }
                if (s + 1 < 14 && tt == 0) wf[(s + 1) & 1][c] = WQ_FRAG(s + 1, c);
                if (i == 5) { if (s == 0) mfma_first(gram, xf[0][s], xf[0][s]); else mfma_acc(gram, xf[0][s], xf[0][s]); }
                SB();
            }
            if ((s == 12 || s == 13) && ppok)
                st_sc1(rs, ppdst + (s - 12) * 1024 + l * 16, trfrag(ppa + (s - 12) * ppk, ppa + (s - 12) * ppk + pph));
            if ((s & 3) == tp) {
                cs[0] = mfma16(wf[s & 1][0], ones, cs[0]); cs[1] = mfma16(wf[s & 1][1], ones, cs[1]); cs[2] = mfma16(ones, wf[s & 1][2], cs[2]);
            }
            SB();
        }
        asm volatile("s_nop 7\n\ts_nop 1" : "+v"(acc[0][0]), "+v"(acc[0][1]), "+v"(acc[0][2]), "+v"(acc[1][0]), "+v"(acc[1][1]), "+v"(acc[1][2]), "+v"(gram));
        STAMP(3);
        {
            const float sum = __shfl(odd ? acc[0][0][0] : acc[0][1][0], odd ? t : 48 + t);
            const float dg = (t & 2) ? ((t & 1) ? gram[3] : gram[2]) : ((t & 1) ? gram[1] : gram[0]);
            const float sq = __shfl(dg, 16 * (t >> 2) + t);
            const float mean = sum * (1.f / 448.f);
            const float rstd = rsqrtf(fmaxf(sq * (1.f / 448.f) - mean * mean, 0.f) + 1e-5f);
            if (g == 0) lds_st64(L_ST1 + (16 * w + t) * 8, (u32x2){__float_as_uint(rstd), __float_as_uint(-mean * rstd)});
            if (t == 0) { lds_stf4(L_CS1 + ((tp * 6 + ch * 3 + 0) * 16 + 4 * g) * 4, cs[0]); lds_stf4(L_CS1 + ((tp * 6 + ch * 3 + 1) * 16 + 4 * g) * 4, cs[1]); }
            if (g == 0) lds_stf(L_CS1 + ((tp * 6 + ch * 3 + 2) * 16 + t) * 4, cs[2][0]);
        }
        BAR();
        STAMP(4);
        f32x4 csum[2]; float csv = 0.f;
        csum[0] = (f32x4){0.f, 0.f, 0.f, 0.f}; csum[1] = csum[0];
#pragma unroll
        for (int p = 0; p < 4; ++p) {
            csum[0] += lds_ldf4(L_CS1 + ((p * 6 + ch * 3 + 0) * 16 + 4 * g) * 4);
            csum[1] += lds_ldf4(L_CS1 + ((p * 6 + ch * 3 + 1) * 16 + 4 * g) * 4);
            csv += *(LAS float*)(uintptr_t)(L_CS1 + ((p * 6 + ch * 3 + 2) * 16 + t) * 4);
        }
        const float qs = ch ? 1.f : 0.18898223650461362f * 1.4426950408889634f;
        const float m0 = (odd && g == 0) ? 0.f : qs, m1 = (!odd && g == 3) ? 0.f : qs;
        const bool vpad = ch == 0 ? (odd && t < 4) : (!odd && t >= 12);
        const u32 blob = WS_KV + (u32)(RB * 16 + h) * 16384;
        u32x2 vv[2];
#pragma unroll
        for (int tt = 0; tt < 2; ++tt) {
            const int tile = w ^ tt;
            const u32x2 st = *(LAS u32x2*)(uintptr_t)(L_ST1 + (16 * tile + t) * 8);
            const float rstd = __uint_as_float(st[0]), mr = __uint_as_float(st[1]);
            float a[8];
#pragma unroll
            for (int i = 0; i < 4; ++i) { a[i] = fmaf(acc[tt][0][i], rstd, csum[0][i] * mr) * m0; a[4 + i] = fmaf(acc[tt][1][i], rstd, csum[1][i] * mr) * m1; }
            const u32x4 f = {pack2(a[0], a[1]), pack2(a[2], a[3]), pack2(a[4], a[5]), pack2(a[6], a[7])};
            if (ch) { st_sc1(rs, blob + tile * 1024 + l * 16, f); lds_st128(L_K + (16 + tile) * 1024 + l * 16, f); }
            else lds_st128(L_Q + tile * 1024 + l * 16, f);
            const u32x4 sa = lds_ld128(L_ST1 + (16 * tile + 4 * g) * 8), sb = lds_ld128(L_ST1 + (16 * tile + 4 * g + 2) * 8);
            const float v0 = fmaf(acc[tt][2][0], __uint_as_float(sa[0]), csv * __uint_as_float(sa[1]));
            const float v1 = fmaf(acc[tt][2][1], __uint_as_float(sa[2]), csv * __uint_as_float(sa[3]));
            const float v2 = fmaf(acc[tt][2][2], __uint_as_float(sb[0]), csv * __uint_as_float(sb[1]));
            const float v3 = fmaf(acc[tt][2][3], __uint_as_float(sb[2]), csv * __uint_as_float(sb[3]));
            vv[tt] = (u32x2){pack2(v0, v1), pack2(v2, v3)};
            if (vpad) vv[tt] = (u32x2){0x3f803f80u, 0x3f803f80u};
        }
        const u32x4 vfr = ch ? (u32x4){vv[1][0], vv[1][1], vv[0][0], vv[0][1]} : (u32x4){vv[0][0], vv[0][1], vv[1][0], vv[1][1]};
        st_sc1(rs, blob + 8192 + (tp * 2 + ch) * 1024 + l * 16, vfr);
        lds_st128(L_V + ((8 + tp) * 2 + ch) * 1024 + l * 16, vfr);
    }
    {
        WAIT_VM(0);
        set_flag(ws, WS_F1 + (u32)(RB * 16 + h) * FGRP + w * FSTRIDE, tag, l);
        BAR();
        if ((tid & 255) == 0 && ppok) __hip_atomic_store((GAS u32*)(ws + WS_FW + ppfl * 64), tag, __ATOMIC_RELAXED, __HIP_MEMORY_SCOPE_AGENT);
    }
    const u32x4 qf = lds_ld128(L_Q + w * 1024 + l * 16);
    STAMP(5);
    SB();
    const int cg4 = 7 * (xcd & 1) + loc % 7, RB4 = 4 * (xcd >> 1) + loc / 7;
    const int rq = loc / 7, cg = loc % 7;
    const int cw = w & 3, kh = w >> 2;
    const bool worker = loc < 28;
    {
        const int rbl = RB - 2 + (l & 1);
        const int rsafe = rbl >= 0 ? rbl : RB;
        wait_flags(ws, WS_F1 + (u32)(rsafe * 16 + h) * FGRP + ((l & 2) ? (w | 1) : w) * FSTRIDE, tag);
        STAMP(6);
#pragma unroll
        for (int blk = 0; blk < 2; ++blk) {
            const int rb = RB - 2 + blk;
            if (rb >= 0) glds16_run_sc1<2>(ws + WS_KV + (size_t)(rb * 16 + h) * 16384 + w * 1024, l * 16, L_K + blk * 8192 + w * 1024, 8192, L_V - L_K);
            else { lds_st128(L_K + blk * 8192 + tid * 16, (u32x4){0, 0, 0, 0}); lds_st128(L_V + blk * 8192 + tid * 16, (u32x4){0, 0, 0, 0}); }
        }
        WAIT_VM(0);
    }
    BAR();
    STAMP(7);
    u32 fwo;
    {
        const int cgs = worker ? cg : 0, cg4s = worker ? cg4 : 0;
        int pi = 392 + cgs * 8 + 4 * kh + (l & 3);
        if (l >= 4 && l < 11) pi = (2 * cg4s + (w >> 2)) * 7 + (l - 4);
        if (l >= 11 && l < 25) pi = 196 + cgs * 28 + 14 * kh + (l - 11);
        fwo = WS_FW + (u32)pi * 64;
    }
    const u32 fwv = __hip_atomic_load((GAS u32*)(ws + fwo), __ATOMIC_RELAXED, __HIP_MEMORY_SCOPE_AGENT);
    SB();
    u32x4 wof[8];
    u32x4 w1f[14];
    const u32 wofa = WS_WOF + (u32)(((worker ? cg : 0) * 4 + cw) * 16 + 8 * kh) * 1024 + l * 16;
    const u32 w1fa = WS_W1F + (u32)(((worker ? cg4 : 0) * 8 + w) * 14) * 1024 + l * 16;
#define PF2(i) do { if ((i) < 8) wof[(i) < 8 ? (i) : 0] = ld_sc1(rs, wofa + (i) * 1024); else w1f[(i) >= 8 ? (i) - 8 : 0] = ld_sc1(rs, w1fa + ((i) - 8) * 1024); } while (0)
    {
        const int base = w & ~1;
        f32x4 sc[18];
        {
            u32x4 kf[18];
#pragma unroll
            for (int a = 0; a < 18; ++a) kf[a] = lds_ld128(L_K + (base + a) * 1024 + l * 16);
            SB();
#pragma unroll
            for (int a = 0; a < 18; ++a) {
                sc[a] = mfma16(kf[a], qf, (f32x4){0.f, 0.f, 0.f, 0.f});
                SB();
            }
        }
        if (!__all(fwv == tag)) wait_flags(ws, fwo, tag);
        asm volatile("" ::: "memory");
        PF2(0); PF2(1);
        SB();
        u32x4 vf[18];
#pragma unroll
        for (int a = 0; a < 8; ++a) vf[a] = lds_ld128(L_V + (base >> 1) * 2048 + a * 1024 + l * 16);
        SB();
        const float NEG = -1e30f;
        const int qq = 256 + 16 * w + t;
#pragma unroll
        for (int a = 0; a < 2; ++a)
#pragma unroll
            for (int i = 0; i < 4; ++i) { const int kk = 16 * (base + a) + 4 * g + i; if (kk <= qq - 256) sc[a][i] = NEG; }
#pragma unroll
        for (int a = 16; a < 18; ++a)
#pragma unroll
            for (int i = 0; i < 4; ++i) { const int kk = 16 * (base + a) + 4 * g + i; if (kk > qq) sc[a][i] = NEG; }
        if (RB < 2) {
            asm volatile("" ::: "memory");
            const int tmin = 8 * (2 - RB);
#pragma unroll
            for (int a = 0; a < 18; ++a) if (base + a < tmin) sc[a] = (f32x4){NEG, NEG, NEG, NEG};
        }
        PF2(2); PF2(3);
        SB();
        float ma = NEG, mb = NEG;
#pragma unroll
        for (int a = 0; a < 18; a += 2) {
            ma = fmaxf(fmaxf(sc[a][0], sc[a][1]), ma); ma = fmaxf(fmaxf(sc[a][2], sc[a][3]), ma);
            mb = fmaxf(fmaxf(sc[a + 1][0], sc[a + 1][1]), mb); mb = fmaxf(fmaxf(sc[a + 1][2], sc[a + 1][3]), mb);
        }
        PF2(4); PF2(5);
        SB();
        float m = fmaxf(ma, mb);
        m = fmaxf(m, xor16(m)); m = fmaxf(m, xor32(m));
        f32x4 o0 = {0.f, 0.f, 0.f, 0.f}, o1 = {0.f, 0.f, 0.f, 0.f};
#pragma unroll
        for (int a = 0; a < 9; ++a) {
            if (2 * a + 8 < 18) {
                vf[2 * a + 8] = lds_ld128(L_V + (base >> 1) * 2048 + (2 * a + 8) * 1024 + l * 16);
                vf[2 * a + 9] = lds_ld128(L_V + (base >> 1) * 2048 + (2 * a + 9) * 1024 + l * 16);
            }
            if (a < 8) { PF2(6 + 2 * a); PF2(7 + 2 * a); }
            u32x4 pf;
            pf[0] = pack2(__builtin_amdgcn_exp2f(sc[2 * a][0] - m), __builtin_amdgcn_exp2f(sc[2 * a][1] - m));
            pf[1] = pack2(__builtin_amdgcn_exp2f(sc[2 * a][2] - m), __builtin_amdgcn_exp2f(sc[2 * a][3] - m));
            pf[2] = pack2(__builtin_amdgcn_exp2f(sc[2 * a + 1][0] - m), __builtin_amdgcn_exp2f(sc[2 * a + 1][1] - m));
            pf[3] = pack2(__builtin_amdgcn_exp2f(sc[2 * a + 1][2] - m), __builtin_amdgcn_exp2f(sc[2 * a + 1][3] - m));
            o0 = mfma16(vf[2 * a], pf, o0);
            o1 = mfma16(vf[2 * a + 1], pf, o1);
            SB();
        }
        float den = odd ? o0[0] : o1[0];
        den = __shfl(den, odd ? t : (48 + t));
        const float r = 1.f / den;
        const float m0 = (odd && g == 0) ? 0.f : r, m1 = (!odd && g == 3) ? 0.f : r;
        const u32x4 of = {pack2(o0[0] * m0, o0[1] * m0), pack2(o0[2] * m0, o0[3] * m0), pack2(o1[0] * m1, o1[1] * m1), pack2(o1[2] * m1, o1[3] * m1)};
        STAMP(8);
        st_sc1(rs, WS_O + (u32)((RB * 8 + w) * 16 + h) * 1024 + l * 16, of);
        WAIT_VM(0);
        set_flag(ws, WS_F2 + (u32)(RB * 8 + w) * FGRP + h * FSTRIDE, tag, l);
    }
    STAMP(9);
    SB();
    if (!worker) {
        if (b == 255) {
#pragma unroll
            for (int i = 0; i < 4; ++i) wait_flags(ws, WS_F1 + (u32)(w * 32 + i * 8 + (l >> 3)) * FGRP + (l & 7) * FSTRIDE, tag);
            BAR();
            if (tid == 0) __hip_atomic_store((GAS u32*)(ws + WS_GEN), tag, __ATOMIC_RELAXED, __HIP_MEMORY_SCOPE_AGENT);
        }
        STAMP_FLUSH();
        return;
    }
    const int RBW = 2 * xcd + (rq >> 1);
#define TTG(tt) (8 * RBW + 4 * ((tt) >> 1) + 2 * (rq & 1) + ((tt) & 1))
    const int fr = l >> 3, fo8 = l & 7;
    const int TTf = TTG(w >> 1), tl = 8 * (w & 1) + fr;
    const int tok = 16 * TTf + tl;
    const u32x4 xr = *(const u32x4*)((const char*)x + (size_t)tok * 896 + cg * 128 + fo8 * 16);
    SB();
    BAR();
    const int TTc = TTG(cw);
    wait_flags(ws, WS_F2 + (u32)TTc * FGRP + (8 * kh + (l & 7)) * FSTRIDE, tag);
    STAMP(10);
    SB();
    glds16_run_sc1<8>(ws + WS_O + (size_t)(TTc * 16 + 8 * kh) * 1024, l * 16, (cw * 16 + 8 * kh) * 1024, 1024, 1024);
    WAIT_VM(0);
    BAR();
    u32x4 w2f[28];
    const int rot5 = 4 * ((cg + 2 * rq) % 7);
    const u32 w2fa = WS_W2F + (u32)((cg * 4 + cw) * 56 + 28 * kh) * 1024 + l * 16;
#define W2F(i) ld_sc1(rs, w2fa + (u32)((i) + rot5 < 28 ? (i) + rot5 : (i) + rot5 - 28) * 1024)
    u32x4 hres;
    {
        f32x4 acc[4];
        u32x4 af[8];
        const u32 ab = (8 * kh) * 1024 + l * 16;
#pragma unroll
        for (int n = 0; n < 6; ++n) af[n] = lds_ld128(ab + (n & 3) * 16384 + (n >> 2) * 1024);
        SB();
#pragma unroll
        for (int n = 0; n < 32; ++n) {
            if (n < 4) mfma_first(acc[n & 3], wof[n >> 2], af[n & 7]); else mfma_acc(acc[n & 3], wof[n >> 2], af[n & 7]);
            if (n + 6 < 32) af[(n + 6) & 7] = lds_ld128(ab + ((n + 6) & 3) * 16384 + ((n + 6) >> 2) * 1024);
            if ((n & 3) == 1 && (n >> 2) < 7) w2f[n >> 2] = W2F(n >> 2);
            SB();
        }
        MFMA_FENCE4(acc);
        STAMP(11);
#pragma unroll
        for (int tt = 0; tt < 4; ++tt) lds_stf4(L_RED3 + (w * 4 + tt) * 1024 + l * 16, acc[tt]);
        BAR();
        const u32 ra = L_RED3 + (w >> 1) * 1024 + (fo8 >> 1) * 4096 + ((2 * (fo8 & 1)) * 16 + tl) * 16;
        const f32x4 a0 = lds_ldf4(ra) + lds_ldf4(ra + 16384), a1 = lds_ldf4(ra + 256) + lds_ldf4(ra + 16384 + 256);
        hres = (u32x4){pack2(bflo(xr[0]) + a0[0], bfhi(xr[0]) + a0[1]), pack2(bflo(xr[1]) + a0[2], bfhi(xr[1]) + a0[3]),
                       pack2(bflo(xr[2]) + a1[0], bfhi(xr[2]) + a1[1]), pack2(bflo(xr[3]) + a1[2], bfhi(xr[3]) + a1[3])};
        st_sc1(rs, WS_H + (u32)(TTf * 14 + 2 * cg + (fo8 >> 2)) * 1024 + ((fo8 & 3) * 16 + tl) * 16, hres);
        float sum = 0.f, sq = 0.f;
#pragma unroll
        for (int j = 0; j < 4; ++j) { const float a = bflo(hres[j]), c = bfhi(hres[j]); sum += a + c; sq = fmaf(a, a, sq); sq = fmaf(c, c, sq); }
        sum += __shfl_xor(sum, 1); sq += __shfl_xor(sq, 1); sum += __shfl_xor(sum, 2); sq += __shfl_xor(sq, 2);
        sum += __shfl_xor(sum, 4); sq += __shfl_xor(sq, 4);
        if (fo8 == 0) st_sc1_64(rs, WS_ST + (u32)(TTf * 7 + cg) * 128 + tl * 8, (u32x2){__float_as_uint(sum), __float_as_uint(sq)});
        WAIT_VM(0);
        set_flag(ws, WS_F3 + (u32)TTf * FGRP + (2 * cg + (w & 1)) * FSTRIDE, tag, l);
    }
    STAMP(12);
    SB();
#pragma unroll
    for (int s = 7; s < 14; ++s) w2f[s] = W2F(s);
    SB();
    f32x4 cs4, bias4;
    {
        const u32x4 ones = {0x3f803f80u, 0x3f803f80u, 0x3f803f80u, 0x3f803f80u};
        f32x4 c4 = {0.f, 0.f, 0.f, 0.f}, c5 = {0.f, 0.f, 0.f, 0.f};
#pragma unroll
        for (int s = 0; s < 14; s += 2) { c4 = mfma16(w1f[s], ones, c4); c5 = mfma16(w1f[s + 1], ones, c5); }
        cs4 = c4 + c5;
        const u32x2 bb = *(const u32x2*)((const char*)b1 + cg4 * 256 + w * 32 + g * 8);
        bias4 = (f32x4){bflo(bb[0]), bfhi(bb[0]), bflo(bb[1]), bfhi(bb[1])};
    }
    BAR();
    STAMP(13);
    wait_flags(ws, WS_F3 + (u32)(8 * RB4 + (w & 3) + ((l & 16) ? 4 : 0)) * FGRP + ((l & 15) < 14 ? (l & 15) : 0) * FSTRIDE, tag);
    STAMP(14);
    SB();
    {
        glds16_run_sc1<1>(ws + WS_ST + (size_t)((8 * RB4 + w) * 7) * 128, l * 16, L_ST4 + 1024 + w * 1024, 0, 0);
        const char* hsA = ws + WS_H + (size_t)((8 * RB4 + (w & 3)) * 14 + (w >> 2)) * 1024;
        const char* hsB = hsA + 4 * 14 * 1024;
        u32 hv = l * 16 - 2048;
        dma_seek((w >> 2) * 8192 + (w & 3) * 1024 - 16384);
#pragma unroll
        for (int i = 0; i < 4; ++i) dma_next_sc1<16384, 2048>(hsA, hv);
        f32x4 acc[8];
        u32x4 af[8];
        u32 ab0 = l * 16, ab1 = l * 16 + 65536;
        OPAQUE(ab1);
#define H4_BLK(n) ((((n) % 56) >> 2) * 8 + ((n) / 56) * 4 + ((n) & 3))
#define H4_ADDR(n) (H4_BLK(n) < 64 ? ab0 + H4_BLK(n) * 1024 : ab1 + (H4_BLK(n) - 64) * 1024)
#define P4_EPILOGUE(M0) do { _Pragma("unroll") for (int m = (M0); m < (M0) + 2; ++m) { \
            u32 a[2][2]; \
            _Pragma("unroll") for (int u = 0; u < 2; ++u) { \
                const int tt = 2 * m + u; \
                const u32x2 st = *(LAS u32x2*)(uintptr_t)(L_ST4 + (16 * tt + t) * 8); \
                const float rstd = __uint_as_float(st[0]), mr = __uint_as_float(st[1]); \
                float v[4]; \
                _Pragma("unroll") for (int i = 0; i < 4; ++i) { float z = fmaf(acc[tt][i], rstd, fmaf(cs4[i], mr, bias4[i])); z = fmaxf(z, 0.f); v[i] = z * z; } \
                a[u][0] = pack2(v[0], v[1]); a[u][1] = pack2(v[2], v[3]); \
            } \
              \
            const auto r0 = __builtin_amdgcn_permlane16_swap(a[0][0], a[1][0], false, false); \
            const auto r1 = __builtin_amdgcn_permlane16_swap(a[0][1], a[1][1], false, false); \
            const u32x4 o = {r0[0], r1[0], r0[1], r1[1]}; \
            const int TTo = 8 * RB4 + 2 * m + (g & 1); \
            st_sc1(rs, WS_HH + (u32)(TTo * 56 + 4 * cg4 + (w >> 1)) * 1024 + ((2 * (w & 1) + (g >> 1)) * 16 + t) * 16, o); \
        } } while (0)
        WAIT_VM(2);
        STAMP(20);
        BAR();
#pragma unroll
        for (int n = 0; n < 6; ++n) af[n] = lds_ld128(H4_ADDR(n));
        SB();
#pragma unroll
        for (int seg = 0; seg < 7; ++seg) {
            constexpr int SB0[8] = {0, 10, 26, 42, 50, 66, 82, 112};
            if (seg == 1) {
                wait_vm<2>();
                const u32x2 p0 = *(LAS u32x2*)(uintptr_t)(L_ST4 + 1024 + w * 1024 + g * 128 + t * 8);
                const u32x2 p1 = *(LAS u32x2*)(uintptr_t)(L_ST4 + 1024 + w * 1024 + (g + 4) * 128 + t * 8);
                float sum = __uint_as_float(p0[0]), sq = __uint_as_float(p0[1]);
                if (g < 3) { sum += __uint_as_float(p1[0]); sq += __uint_as_float(p1[1]); }
                sum += xor16(sum); sq += xor16(sq); sum += xor32(sum); sq += xor32(sq);
                const float mean = sum * (1.f / 448.f);
                const float rstd = rsqrtf(fmaxf(sq * (1.f / 448.f) - mean * mean, 0.f) + 1e-5f);
                if (g == 0) lds_st64(L_ST4 + (16 * w + t) * 8, (u32x2){__float_as_uint(rstd), __float_as_uint(-mean * rstd)});
                BAR0();
            }
            if (seg == 2) { wait_vm<4>(); BAR(); }
            if (seg == 3) { wait_vm<7>(); BAR0(); }
            if (seg == 4) { wait_vm<5>(); BAR0(); }
            if (seg == 5) { wait_vm<7>(); BAR0(); }
            if (seg == 6) {
                wait_vm<4>(); BAR0();
                if (tid == 0) __hip_atomic_store((GAS u32*)(ws + WS_F4 + (RB4 * 2) * FGRP + cg4 * FSTRIDE), tag, __ATOMIC_RELAXED, __HIP_MEMORY_SCOPE_AGENT);
            }
#pragma unroll
            for (int n = SB0[seg]; n < SB0[seg + 1]; ++n) {
                if (n == 56) {
                    asm volatile("s_nop 7\n\ts_nop 1" : "+v"(acc[0]), "+v"(acc[1]), "+v"(acc[2]), "+v"(acc[3]));
                    P4_EPILOGUE(0);
                    SB();
                }
                const int ai = (n / 56) * 4 + (n & 3), ks = (n % 56) >> 2;
                if (ks == 0) mfma_first(acc[ai], w1f[ks], af[n & 7]); else mfma_acc(acc[ai], w1f[ks], af[n & 7]);
                if (n + 6 < 112) af[(n + 6) & 7] = lds_ld128(H4_ADDR(n + 6));
                if (n == 4 || n == 8 || n == 12) dma_next_sc1<16384, 2048>(hsA, hv);
                if (n == 16) { hv = l * 16 - 2048; dma_seek((w >> 2) * 8192 + (4 + (w & 3)) * 1024 - 16384); }
                if (n >= 16 && n <= 40 && (n & 3) == 0) dma_next_sc1<16384, 2048>(hsB, hv);
                if (n >= 58 && (n - 58) % 6 == 0 && (n - 58) / 6 < 7) w2f[14 + (n - 58) / 6] = W2F(14 + (n - 58) / 6);
                SB();
            }
        }
        asm volatile("s_nop 7\n\ts_nop 1" : "+v"(acc[4]), "+v"(acc[5]), "+v"(acc[6]), "+v"(acc[7]));
        STAMP(15);
        P4_EPILOGUE(2);
        WAIT_VM(0);
        BAR();
        if (tid == 0) __hip_atomic_store((GAS u32*)(ws + WS_F4 + (RB4 * 2 + 1) * FGRP + cg4 * FSTRIDE), tag, __ATOMIC_RELAXED, __HIP_MEMORY_SCOPE_AGENT);
    }
    STAMP(16);
    SB();
#pragma unroll
    for (int s = 21; s < 28; ++s) w2f[s] = W2F(s);
    SB();
    {
        const u32x4 bb = *(const u32x4*)((const char*)b2 + cg * 128 + fo8 * 16);
        const u32 f4o = WS_F4 + (u32)(RBW * 2) * FGRP + (7 * kh + (l < 7 ? l : 0)) * FSTRIDE;
        wait_flags(ws, f4o, tag);
        STAMP(17);
        SB();
        u32 f4b = 0;
        const char* hsA = ws + WS_HH + (size_t)(TTG(cw & 1) * 56 + 28 * kh + (cw >> 1) + rot5) * 1024;
        const char* hsB = ws + WS_HH + (size_t)(TTG(2 + (cw & 1)) * 56 + 28 * kh + (cw >> 1) + rot5) * 1024;
        const int wrap5 = (28 - rot5) >> 1;
#define H5SRC(base, i) ((base) + ((i) < wrap5 ? (i) * 2048 : (i) * 2048 - 28 * 1024))
        const u32 hl = kh * L_H5 + (cw >> 1) * 2048 + (cw & 1) * 1024;
        const u32 hv = l * 16;
        dma_seek(hl - 4096);
#pragma unroll
        for (int i = 0; i < 6; ++i) dma_next_sc1_b<4096>(H5SRC(hsA, i), hv);
        f32x4 acc[4];
        u32x4 af[8];
        u32 ab0 = kh * L_H5 + l * 16, ab1 = kh * L_H5 + l * 16 + 65536;
        OPAQUE(ab0); OPAQUE(ab1);
#define H5_KS(n) (((n) % 56) >> 1)
#define H5_SLOT(n) ((n) < 56 ? H5_KS(n) : H5_KS(n) < 12 ? 28 + H5_KS(n) : H5_KS(n) - 12)
#define H5_BLK(n) (H5_SLOT(n) * 2 + ((n) & 1))
#define H5_ADDR(n) (H5_BLK(n) < 64 ? ab0 + H5_BLK(n) * 1024 : ab1 + (H5_BLK(n) - 64) * 1024)
        wait_vm<4>();
        STAMP(21);
        BAR();
#pragma unroll
        for (int n = 0; n < 6; ++n) af[n] = lds_ld128(H5_ADDR(n));
        SB();
#pragma unroll
        for (int seg = 0; seg < 9; ++seg) {
            constexpr int SB0[10] = {0, 2, 10, 26, 42, 50, 66, 82, 98, 112};
            if (seg == 1) { wait_vm<3>(); BAR0(); }
            if (seg == 2) {
                wait_vm<2>(); BAR0();
                asm volatile("global_load_dword %0, %1, %2 sc1" : "=v"(f4b) : "v"(f4o + FGRP), "s"(ws) : "memory");
            }
            if (seg == 3) {
                wait_vm<2>();
                asm volatile("" : "+v"(f4b));
                if (!__all(f4b == tag)) wait_flags(ws, f4o + FGRP, tag);
                BAR0();
            }
            if (seg == 4) { wait_vm<5>(); BAR0(); }
            if (seg == 5) { wait_vm<4>(); BAR0(); }
            if (seg == 6) { wait_vm<5>(); BAR0(); }
            if (seg == 7) { wait_vm<2>(); BAR0(); }
            if (seg == 8) { wait_vm<0>(); BAR0(); }
#pragma unroll
            for (int n = SB0[seg]; n < SB0[seg + 1]; ++n) {
                const int ai = (n / 56) * 2 + (n & 1);
                if (H5_KS(n) == 0) mfma_first(acc[ai], w2f[H5_KS(n)], af[n & 7]); else mfma_acc(acc[ai], w2f[H5_KS(n)], af[n & 7]);
                if (n + 6 < 112) af[(n + 6) & 7] = lds_ld128(H5_ADDR(n + 6));
                if (n <= 21 && n % 3 == 0) dma_next_sc1_b<4096>(H5SRC(hsA, 6 + n / 3), hv);
                if (n == 28) dma_seek(hl + 28 * 2048 - 4096);
                if (n == 46) dma_seek(hl - 4096);
                if (n >= 28 && n <= 67 && (n - 28) % 3 == 0) dma_next_sc1_b<4096>(H5SRC(hsB, (n - 28) / 3), hv);
                SB();
            }
        }
        MFMA_FENCE4(acc);
        STAMP(18);
        const u32 rw = (w < 6 ? 32768 + w * 4096 : L_H5 + 32768 + (w - 6) * 4096);
#pragma unroll
        for (int tt = 0; tt < 4; ++tt) lds_stf4(rw + tt * 1024 + l * 16, acc[tt]);
        BAR();
        const int c5 = fo8 >> 1;
        const u32 lp = (w >> 1) * 1024 + ((2 * (fo8 & 1)) * 16 + tl) * 16;
        const u32 r0a = 32768 + c5 * 4096 + lp, r1a = (c5 < 2 ? 32768 + (4 + c5) * 4096 : L_H5 + 32768 + (c5 - 2) * 4096) + lp;
        const f32x4 a0 = lds_ldf4(r0a) + lds_ldf4(r1a), a1 = lds_ldf4(r0a + 256) + lds_ldf4(r1a + 256);
        const u32x4 o = {pack2(a0[0] + bflo(bb[0]) + bflo(hres[0]), a0[1] + bfhi(bb[0]) + bfhi(hres[0])),
                         pack2(a0[2] + bflo(bb[1]) + bflo(hres[1]), a0[3] + bfhi(bb[1]) + bfhi(hres[1])),
                         pack2(a1[0] + bflo(bb[2]) + bflo(hres[2]), a1[1] + bfhi(bb[2]) + bfhi(hres[2])),
                         pack2(a1[2] + bflo(bb[3]) + bflo(hres[3]), a1[3] + bfhi(bb[3]) + bfhi(hres[3]))};
        *(u32x4*)((char*)out + (size_t)tok * 896 + cg * 128 + fo8 * 16) = o;
    }
    STAMP(19);
    STAMP_FLUSH();
}

extern "C" void kernel_launch(void* const* d_in, const int* in_sizes, int n_in, void* d_out, int out_size, void* d_ws, size_t ws_size,
                              hipStream_t stream) {
    (void)hipFuncSetAttribute((const void*)fused_kernel, hipFuncAttributeMaxDynamicSharedMemorySize, LDS_BYTES);
    fused_kernel<<<256, 512, LDS_BYTES, stream>>>((const u16*)d_in[0], (const u16*)d_in[1], (const u16*)d_in[2], (const u16*)d_in[4],
                                                  (const u16*)d_in[5], (const u16*)d_in[6], (const u16*)d_in[7], (u16*)d_out, (char*)d_ws);
}
```

```cpp
#include <hip/hip_runtime.h>
#include <stdint.h>

typedef unsigned short u16;
typedef unsigned int u32;
typedef __attribute__((ext_vector_type(8))) __bf16 bf16x8;
typedef __attribute__((ext_vector_type(2))) __bf16 bf16x2;
typedef __attribute__((ext_vector_type(4))) short s16x4;
typedef __attribute__((ext_vector_type(4))) float f32x4;
typedef __attribute__((ext_vector_type(2))) float f32x2;
typedef __attribute__((ext_vector_type(4))) u32 u32x4;
typedef __attribute__((ext_vector_type(2))) u32 u32x2;
#define LAS __attribute__((address_space(3)))
#define GAS __attribute__((address_space(1)))
#define DI __device__ __forceinline__

#define FSTRIDE 128
constexpr u32 FGRP = 16 * FSTRIDE < 128 ? 128 : 16 * FSTRIDE;
constexpr u32 WS_GEN = 0;
constexpr u32 WS_FW = 4096;
constexpr u32 WS_FQ = WS_FW + 448 * 64;
constexpr u32 WS_F1 = WS_FQ + 384 * 64;
constexpr u32 WS_F2 = WS_F1 + 256 * FGRP;
constexpr u32 WS_F3 = WS_F2 + 128 * FGRP;
constexpr u32 WS_F4 = WS_F3 + 128 * FGRP;
constexpr u32 WS_KV = WS_F4 + 32 * FGRP;
constexpr u32 WS_O = WS_KV + 16 * 16 * 16384;
constexpr u32 WS_H = WS_O + 128 * 16 * 1024;
constexpr u32 WS_ST = WS_H + 128 * 14 * 1024;
constexpr u32 WS_HH = WS_ST + 128 * 7 * 128;
constexpr u32 WS_W1F = WS_HH + 128 * 56 * 1024;
constexpr u32 WS_W2F = WS_W1F + 112 * 14 * 1024;
constexpr u32 WS_WOF = WS_W2F + 28 * 56 * 1024;
constexpr u32 WS_WQF = WS_WOF + 28 * 16 * 1024;
constexpr u32 WS_END = WS_WQF + 16 * 48 * 1024;
constexpr u32 L_WA = 114688;
constexpr u32 L_RT = 151552;
constexpr u32 L_GEN = 157696;
constexpr u32 L_ST1 = 157952;
constexpr u32 L_CS1 = 158976;
constexpr u32 L_Q = 49152;
constexpr u32 L_PP = 98304;
constexpr u32 L_K = 0, L_V = 24576;
constexpr u32 L_RED3 = 65536;
constexpr u32 L_ST4 = 114688;
constexpr u32 L_H5 = 81920;
constexpr u32 L_RED5 = 32768;
constexpr u32 LDS_BYTES = 163840;
#define P4DEEP 0
#define P5DEEP 0
constexpr int SPIN_MAX = 1 << 14;

DI f32x4 mfma16(u32x4 a, u32x4 b, f32x4 c) {
    return __builtin_amdgcn_mfma_f32_16x16x32_bf16(__builtin_bit_cast(bf16x8, a), __builtin_bit_cast(bf16x8, b), c, 0, 0, 0);
}
DI void mfma_acc(f32x4& acc, const u32x4& a, const u32x4& b) {
    asm volatile("v_mfma_f32_16x16x32_bf16 %0, %1, %2, %0" : "+v"(acc) : "v"(a), "v"(b));
}
DI void mfma_first(f32x4& acc, const u32x4& a, const u32x4& b) {
    asm volatile("v_mfma_f32_16x16x32_bf16 %0, %1, %2, 0" : "=&v"(acc) : "v"(a), "v"(b));
}
#define MFMA_FENCE4(a) asm volatile("s_nop 7\n\ts_nop 1" : "+v"(a[0]), "+v"(a[1]), "+v"(a[2]), "+v"(a[3]))
#define OPAQUE(x) asm volatile("" : "+v"(x))
DI u32 pack2(float lo, float hi) {
    f32x2 v = {lo, hi};
    bf16x2 r = __builtin_convertvector(v, bf16x2);
    return __builtin_bit_cast(u32, r);
}
DI float dot2bf(u32 a, u32 b, float c) {
    return __builtin_amdgcn_fdot2_f32_bf16(__builtin_bit_cast(bf16x2, a), __builtin_bit_cast(bf16x2, b), c, false);
}
DI float relu(float z) { float r; asm("v_max_f32 %0, 0, %1" : "=v"(r) : "v"(z)); return r; }
DI float bflo(u32 u) { return __uint_as_float(u << 16); }
DI float bfhi(u32 u) { return __uint_as_float(u & 0xffff0000u); }
DI u32x2 trread(u32 off) {
    s16x4 r = __builtin_amdgcn_ds_read_tr16_b64_v4i16((LAS s16x4*)(uintptr_t)off);
    return __builtin_bit_cast(u32x2, r);
}
DI u32x4 trfrag(u32 off0, u32 off1) {
    u32x2 a = trread(off0), b = trread(off1);
    u32x4 r = {a[0], a[1], b[0], b[1]};
    return r;
}
DI u32x4 lds_ld128(u32 off) { return *(LAS u32x4*)(uintptr_t)off; }
DI f32x4 lds_ldf4(u32 off) { return *(LAS f32x4*)(uintptr_t)off; }
DI void lds_st128(u32 off, u32x4 v) { *(LAS u32x4*)(uintptr_t)off = v; }
DI void lds_stf4(u32 off, f32x4 v) { *(LAS f32x4*)(uintptr_t)off = v; }
DI void lds_st64(u32 off, u32x2 v) { *(LAS u32x2*)(uintptr_t)off = v; }
DI void lds_stf(u32 off, float v) { *(LAS float*)(uintptr_t)off = v; }
template <int N>
DI void glds16_run(const void* sbase, u32 voff, u32 lds, u32 gstep, u32 lstep) {
    u32 keep;
    asm volatile("s_mov_b32 %0, m0\n\ts_mov_b32 m0, %3\n\t"
                 ".rept %c6\n\t"
                 "s_nop 0\n\t"
                 "global_load_lds_dwordx4 %1, %2\n\t"
                 "s_add_u32 m0, m0, %5\n\t"
                 "v_add_u32 %1, %4, %1\n\t"
                 ".endr\n\t"
                 "s_mov_b32 m0, %0"
                 : "=&s"(keep), "+v"(voff) : "s"(sbase), "s"(lds), "s"(gstep), "s"(lstep), "i"(N) : "memory", "scc");
}
template <int N>
DI void glds16_run_sc1(const void* sbase, u32 voff, u32 lds, u32 gstep, u32 lstep) {
    u32 keep;
    asm volatile("s_mov_b32 %0, m0\n\ts_mov_b32 m0, %3\n\t"
                 ".rept %c6\n\t"
                 "s_nop 0\n\t"
                 "global_load_lds_dwordx4 %1, %2 sc1\n\t"
                 "s_add_u32 m0, m0, %5\n\t"
                 "v_add_u32 %1, %4, %1\n\t"
                 ".endr\n\t"
                 "s_mov_b32 m0, %0"
                 : "=&s"(keep), "+v"(voff) : "s"(sbase), "s"(lds), "s"(gstep), "s"(lstep), "i"(N) : "memory", "scc");
}
DI void glds16_one(const void* sbase, u32 voff, u32 goff, u32 lds) {
    u32 tmp;
    asm volatile("s_mov_b32 m0, %4\n\tv_add_u32 %0, %3, %1\n\tglobal_load_lds_dwordx4 %0, %2" : "=&v"(tmp) : "v"(voff), "s"(sbase), "s"(goff), "s"(lds) : "memory");
}
DI void dma_seek(u32 lds) { asm volatile("s_mov_b32 m0, %0" :: "s"(lds) : "memory"); }
template <int LSTEP, int GSTEP>
DI void dma_next_sc1(const void* sbase, u32& voff) {
    asm volatile("s_add_u32 m0, m0, %c2\n\tv_add_u32 %0, %c3, %0\n\tglobal_load_lds_dwordx4 %0, %1 sc1" : "+v"(voff) : "s"(sbase), "i"(LSTEP), "i"(GSTEP) : "memory", "scc");
}
template <int LSTEP>
DI void dma_next_sc1_b(const void* sbase, u32 voff) {
    asm volatile("s_add_u32 m0, m0, %c2\n\ts_nop 0\n\tglobal_load_lds_dwordx4 %0, %1 sc1" :: "v"(voff), "s"(sbase), "i"(LSTEP) : "memory", "scc");
}
#define PIN_S(x) asm volatile("" : "+s"(x))
template <int N>
DI void glds4_run(const void* sbase, u32 voff, u32 lds, u32 gstep) {
    u32 keep;
    asm volatile("s_mov_b32 %0, m0\n\ts_mov_b32 m0, %3\n\ts_nop 0\n\t"
                 ".rept %c5\n\t"
                 "global_load_lds_dword %1, %2\n\t"
                 "v_add_u32 %1, %4, %1\n\t"
                 ".endr\n\t"
                 "s_mov_b32 m0, %0"
                 : "=&s"(keep), "+v"(voff) : "s"(sbase), "s"(lds), "s"(gstep), "i"(N) : "memory");
}
#define SB() __builtin_amdgcn_sched_barrier(0)
#define WAIT_VM(n) asm volatile("s_waitcnt vmcnt(" #n ")" ::: "memory")
template <int N> DI void wait_vm() { asm volatile("s_waitcnt vmcnt(%c0)" :: "i"(N) : "memory"); }
#define BAR0() asm volatile("s_barrier" ::: "memory")
#define BAR() asm volatile("s_waitcnt lgkmcnt(0)\n\ts_barrier" ::: "memory")
DI float rows_add(float v) {
    const u32 a = __float_as_uint(v);
    const auto r = __builtin_amdgcn_permlane16_swap(a, a, false, false);
    const u32 b2 = __float_as_uint(__uint_as_float(r[0]) + __uint_as_float(r[1]));
    const auto q = __builtin_amdgcn_permlane32_swap(b2, b2, false, false);
    return __uint_as_float(q[0]) + __uint_as_float(q[1]);
}
DI float rows_max(float v) {
    const u32 a = __float_as_uint(v);
    const auto r = __builtin_amdgcn_permlane16_swap(a, a, false, false);
    const u32 b2 = __float_as_uint(fmaxf(__uint_as_float(r[0]), __uint_as_float(r[1])));
    const auto q = __builtin_amdgcn_permlane32_swap(b2, b2, false, false);
    return fmaxf(__uint_as_float(q[0]), __uint_as_float(q[1]));
}
DI float oct_add(float v) {
    v += __uint_as_float(__builtin_amdgcn_update_dpp(0, __float_as_uint(v), 0xB1, 0xf, 0xf, true));
    v += __uint_as_float(__builtin_amdgcn_update_dpp(0, __float_as_uint(v), 0x4E, 0xf, 0xf, true));
    v += __uint_as_float(__builtin_amdgcn_update_dpp(0, __float_as_uint(v), 0x141, 0xf, 0xf, true));
    return v;
}
DI float xor16(float v) { return __shfl_xor(v, 16); }
DI float xor32(float v) { return __shfl_xor(v, 32); }

typedef __amdgpu_buffer_rsrc_t rsrc_t;
DI u32x4 ld_sc1(rsrc_t r, u32 off) { return __builtin_amdgcn_raw_buffer_load_b128(r, off, 0, 16); }
DI u32x2 ld_sc1_64(rsrc_t r, u32 off) { return __builtin_amdgcn_raw_buffer_load_b64(r, off, 0, 16); }
DI void st_sc1(rsrc_t r, u32 off, u32x4 v) { __builtin_amdgcn_raw_buffer_store_b128(v, r, off, 0, 16); }
DI void st_sc1_64(rsrc_t r, u32 off, u32x2 v) { __builtin_amdgcn_raw_buffer_store_b64(v, r, off, 0, 16); }
DI void set_flag(char* ws, u32 off, u32 tag, int lane) {
    if (lane == 0) __hip_atomic_store((GAS u32*)(ws + off), tag, __ATOMIC_RELAXED, __HIP_MEMORY_SCOPE_AGENT);
}
DI void wait_flags(char* ws, u32 off, u32 tag) {
    for (int it = 0; it < SPIN_MAX; ++it) {
        u32 v = __hip_atomic_load((GAS u32*)(ws + off), __ATOMIC_RELAXED, __HIP_MEMORY_SCOPE_AGENT);
        if (__all(v == tag)) break;
#define PSLEEP 1
        if (PSLEEP) __builtin_amdgcn_s_sleep(PSLEEP);
    }
    asm volatile("" ::: "memory");
}

#define STAMP_DECL do {} while (0)
#define STAMP(I) do {} while (0)
#define STAMP_FLUSH() do {} while (0)

__global__ __launch_bounds__(512, 2) __attribute__((amdgpu_waves_per_eu(2, 2)))
void fused_kernel(const u16* __restrict__ x, const u16* __restrict__ wqkv, const u16* __restrict__ wo, const u16* __restrict__ w1,
                  const u16* __restrict__ b1, const u16* __restrict__ w2, const u16* __restrict__ b2, u16* __restrict__ out,
                  char* __restrict__ ws) {
    const int b = blockIdx.x, xcd = b & 7, loc = b >> 3;
    const int tid = threadIdx.x, w = __builtin_amdgcn_readfirstlane(tid >> 6), l = tid & 63, t = l & 15, g = l >> 4;
    const int q4 = t >> 2, p4 = l & 3;
    const rsrc_t rs = __builtin_amdgcn_make_buffer_rsrc(ws, 0, WS_END, 0x00020000);
    STAMP_DECL;
    STAMP(0);
    const int RB = 4 * (xcd >> 1) + (loc >> 3), h = 8 * (xcd & 1) + (loc & 7);
    const bool odd = h & 1;
    const int tp = w >> 1, ch = w & 1;
    {
        const char* xb = (const char*)x + (size_t)(128 * RB + 16 * w) * 896;
        const u32 xv = (l >> 2) * 896 + (((l & 3) ^ ((-(l >> 4)) & 3)) * 16);
        if (w < 7) {
            glds16_run<14>(xb, xv, w * 14336, 64, 1024);
            glds16_run<2>((const char*)x + (size_t)(128 * RB + 112) * 896 + w * 128, xv, 7 * 14336 + w * 2048, 64, 1024);
            asm volatile("s_barrier" ::: "memory");
            if (w < 6) {
                const char* wqb = (const char*)wqkv + 56 * h - (h & 1) * 8;
                const u32 wqv = (l >> 2) * 2688 + (((l & 3) ^ ((l >> 5) << 1)) * 16);
#pragma unroll
                for (int hf = 0; hf < 2; ++hf) glds16_run<3>(wqb + (32 * w + 16 * hf) * 2688, wqv, L_WA + w * 6144 + hf * 1024, 896, 2048);
            } else {
                WAIT_VM(0);
            }
            WAIT_VM(6);
        } else {
            u32 qd[2];
#pragma unroll
            for (int i = 0; i < 2; ++i) {
                const int q = 2 * b + i < 384 ? 2 * b + i : 383, qh = q / 24, r = q - 24 * qh, qs = r / 3, qm = r - 3 * qs;
                glds16_run<2>((const char*)wqkv + (size_t)(32 * (6 + qs)) * 2688 + qm * 896 + 56 * qh - (qh & 1) * 8, (l >> 2) * 2688 + (l & 3) * 16, L_RT + i * 2048, 16 * 2688, 1024);
                qd[i] = (u32)q;
            }
            asm volatile("s_mov_b32 m0, %2\n\ts_nop 0\n\tglobal_load_lds_dword %0, %1 sc1" :: "v"(0), "s"(ws + WS_GEN), "s"(L_GEN) : "memory");
            {
                unsigned long long pc; u32 span;
                asm volatile("s_getpc_b64 %0\n.Lfused_pc_here:\n\ts_mov_b32 %1, fused_code_end_marker-.Lfused_pc_here" : "=s"(pc), "=s"(span));
                const u32 off = (u32)((loc & 3) * 64 + l) * 128u + (u32)(loc >> 2 & 1) * 32768u;
                asm volatile("s_mov_b32 m0, %2\n\ts_nop 0\n\tglobal_load_lds_dword %0, %1" :: "v"(off + 4u <= span ? off : 0u), "s"(pc), "s"(L_GEN + 256) : "memory");
            }
            WAIT_VM(1);
            const u32 tg = __builtin_amdgcn_readfirstlane(*(LAS u32*)(uintptr_t)L_GEN) + 1u;
            if (2 * b < 384) {
#pragma unroll
                for (int i = 0; i < 2; ++i) {
                    const int q = (int)qd[i], qh = q / 24, r = q - 24 * qh, qs = r / 3, qm = r - 3 * qs;
#pragma unroll
                    for (int hf = 0; hf < 2; ++hf) {
                        const u32 a = L_RT + i * 2048 + (8 * g + q4) * 64 + hf * 32 + 8 * p4;
                        u32x4 f = trfrag(a, a + 256);
                        if (qm < 2 && ((qh & 1) ? (hf == 0 && t < 4) : (hf == 1 && t >= 12))) f = (u32x4){0x3f803f80u, 0x3f803f80u, 0x3f803f80u, 0x3f803f80u};
                        st_sc1(rs, WS_WQF + (u32)((qh * 8 + qs) * 6 + qm * 2 + hf) * 1024 + l * 16, f);
                    }
                }
            }
            asm volatile("s_barrier" ::: "memory");
            if (2 * b < 384) {
                WAIT_VM(0);
                if (l < 2) __hip_atomic_store((GAS u32*)(ws + WS_FQ + (2 * b + l) * 64), tg, __ATOMIC_RELAXED, __HIP_MEMORY_SCOPE_AGENT);
            }
        }
    }
    const int pp = b + 256 * (w >> 2);
    const bool ppok = pp < 224 ? (pp & 31) < 28 : pp < 448 ? (pp & 31) < 28 : pp < 504;
    const char* ppsrc; u32 pppitch, ppdst, ppfl;
    {
        const int pe = ppok ? pp : 0, c = w & 3;
        if (pe < 224) { const int rc = pe >> 5, cl = pe & 31; ppsrc = (const char*)w1 + cl * 128 + (size_t)rc * (64 * 3584); pppitch = 3584;
                        ppdst = WS_W1F + (u32)(((cl >> 1) * 8 + (cl & 1) * 4 + c) * 14 + 2 * rc) * 1024; ppfl = (u32)(cl * 7 + rc); }
        else if (pe < 448) { const int g2 = (pe - 224) >> 5, rc = pe & 31; ppsrc = (const char*)w2 + g2 * 128 + (size_t)rc * (64 * 896); pppitch = 896;
                             ppdst = WS_W2F + (u32)((g2 * 4 + c) * 56 + 2 * rc) * 1024; ppfl = (u32)(196 + g2 * 28 + rc); }
        else { const int i = pe - 448; ppsrc = (const char*)wo + (i >> 3) * 128 + (size_t)(i & 7) * (56 * 896); pppitch = 896;
               ppdst = WS_WOF + (u32)(((i >> 3) * 4 + c) * 16 + 2 * (i & 7)) * 1024; ppfl = (u32)(392 + i); }
    }
    const bool ppshort = pp >= 448 && (w & 3) == 3;
    const u32 ppv = (l >> 3) * pppitch + (l & 7) * 16;
    u32 ppg0 = (u32)(16 * (w & 3)) * pppitch, ppg1 = ppshort ? ppg0 : ppg0 + 8 * pppitch;
    u32 ppl0 = L_PP + (w >> 2) * 8192 + (w & 3) * 2048, ppl1 = ppshort ? ppl0 : ppl0 + 1024;
    const u32 ppa = L_PP + (w >> 2) * 8192 + (w & 3) * 32 + 8 * p4 + (pp < 448 ? (8 * g + q4) * 128 : (4 * g + q4) * 128);
    const u32 ppk = pp < 448 ? 4096 : 3072, pph = pp < 448 ? 512 : 2048;
    PIN_S(ppg0); PIN_S(ppg1); PIN_S(ppl0); PIN_S(ppl1); PIN_S(ppdst); PIN_S(ppfl);
    BAR();
    STAMP(1);
    u32x4 xf[2][14];
    {
        const u32 xl = t * 64 + ((g ^ ((-(t >> 2)) & 3)) * 16);
        const u32 xa = w * 14336 + xl, xb2 = (w ^ 1) * 14336 + xl;
#pragma unroll
        for (int s = 0; s < 14; ++s) { xf[0][s] = lds_ld128(xa + s * 1024); xf[1][s] = lds_ld128(xb2 + s * 1024); }
    }
    const u32 tag = __builtin_amdgcn_readfirstlane(*(LAS u32*)(uintptr_t)L_GEN) + 1u;
    WAIT_VM(0);
    if (w < 6) lds_st64(L_WA + w * 6144 + (l >> 5) * 2048 + (l & 31) * 64 + ((((odd ? 0 : 1) ^ ((l >> 3) & 1)) * 32) + (odd ? 0 : 24)), (u32x2){0x3f803f80u, 0x3f803f80u});
    if (w == 7) wait_flags(ws, WS_FQ + (u32)(h * 24 + (l < 24 ? l : 0)) * 64, tag);
    BAR();
    STAMP(2);
    {
        u32 wv = l * 16 + w * 1024 - 8192;
        dma_seek(w * 1024 - 8192);
#pragma unroll
        for (int j = 0; j < 6; ++j) dma_next_sc1<8192, 8192>(ws + WS_WQF + (size_t)(h * 48) * 1024, wv);
    }
    SB();
    {
        f32x4 acc[2][3], cs[3];
        u32 wb1[3] = {(u32)(2 * ch) * 1024 + l * 16, (u32)(2 * ch + 1) * 1024 + l * 16, (u32)(4 + ch) * 1024 + l * 16};
        u32 wb0[3];
        {
            const u32 h0 = (8 * g + q4) * 64 + ((g & 1) * 32) + p4 * 8, h1 = (8 * g + q4) * 64 + (((g & 1) ^ 1) * 32) + p4 * 8;
            wb0[0] = L_WA + h0 + ch * 2048; wb0[1] = L_WA + h1 + ch * 2048; wb0[2] = L_WA + (ch ? h1 : h0) + 4096;
        }
        OPAQUE(wb0[0]); OPAQUE(wb0[1]); OPAQUE(wb0[2]); OPAQUE(wb1[0]); OPAQUE(wb1[1]); OPAQUE(wb1[2]);
#define WQ_FRAG(s, c) ((s) < 6 ? trfrag(wb0[c] + (s) * 6144, wb0[c] + (s) * 6144 + 256) : lds_ld128(wb1[c] + ((s) - 6) * 6144))
        u32x4 ones = {0x3f803f80u, 0x3f803f80u, 0x3f803f80u, 0x3f803f80u};
        OPAQUE(ones);
        f32x4 gram;
        u32x4 wf[2][3];
#pragma unroll
        for (int c = 0; c < 3; ++c) wf[0][c] = WQ_FRAG(0, c);
#pragma unroll
        for (int c = 0; c < 3; ++c) { cs[c] = (f32x4){0.f, 0.f, 0.f, 0.f}; OPAQUE(cs[c]); }
        asm volatile("s_nop 1");
        SB();
#pragma unroll
        for (int s = 0; s < 14; ++s) {
            if (s == 5) { wait_vm<3>(); BAR0(); }
            if (s == 9) { wait_vm<2>(); BAR0(); }
            if (s == 11) { wait_vm<0>(); BAR(); }
            if (s == 6) glds16_one(ppsrc, ppv, ppg0, ppl0);
            if (s == 7) glds16_one(ppsrc, ppv, ppg1, ppl1);
#pragma unroll
            for (int i = 0; i < 6; ++i) {
                const int c = i >> 1, tt = i & 1;
                if (c < 2) { if (s == 0) mfma_first(acc[tt][c], wf[s & 1][c], xf[tt][s]); else mfma_acc(acc[tt][c], wf[s & 1][c], xf[tt][s]); }
                else { if (s == 0) mfma_first(acc[tt][c], xf[tt][s], wf[s & 1][c]); else mfma_acc(acc[tt][c], xf[tt][s], wf[s & 1][c]); }
                if (s + 1 < 14 && tt == 0) wf[(s + 1) & 1][c] = WQ_FRAG(s + 1, c);
                if (i == 5) { if (s == 0) mfma_first(gram, xf[0][s], xf[0][s]); else mfma_acc(gram, xf[0][s], xf[0][s]); }
                SB();
            }
            if ((s == 12 || s == 13) && ppok)
                st_sc1(rs, ppdst + (s - 12) * 1024 + l * 16, trfrag(ppa + (s - 12) * ppk, ppa + (s - 12) * ppk + pph));
            if ((s & 3) == tp) {
                mfma_acc(cs[0], wf[s & 1][0], ones); mfma_acc(cs[1], wf[s & 1][1], ones); mfma_acc(cs[2], ones, wf[s & 1][2]);
            }
            SB();
        }
        asm volatile("s_nop 7\n\ts_nop 1" : "+v"(acc[0][0]), "+v"(acc[0][1]), "+v"(acc[0][2]), "+v"(acc[1][0]), "+v"(acc[1][1]), "+v"(acc[1][2]), "+v"(gram),
                     "+v"(cs[0]), "+v"(cs[1]), "+v"(cs[2]));
        STAMP(3);
        {
            const float sum = rows_add(g == (odd ? 0 : 3) ? (odd ? acc[0][0][0] : acc[0][1][0]) : 0.f);
            const float dg = (t & 2) ? ((t & 1) ? gram[3] : gram[2]) : ((t & 1) ? gram[1] : gram[0]);
            const float sq = rows_add(g == (t >> 2) ? dg : 0.f);
            const float mean = sum * (1.f / 448.f);
            const float rstd = rsqrtf(fmaxf(sq * (1.f / 448.f) - mean * mean, 0.f) + 1e-5f);
            if (g == 0) lds_st64(L_ST1 + (16 * w + t) * 8, (u32x2){__float_as_uint(rstd), __float_as_uint(-mean * rstd)});
            if (t == 0) { lds_stf4(L_CS1 + ((tp * 6 + ch * 3 + 0) * 16 + 4 * g) * 4, cs[0]); lds_stf4(L_CS1 + ((tp * 6 + ch * 3 + 1) * 16 + 4 * g) * 4, cs[1]); }
            if (g == 0) lds_stf(L_CS1 + ((tp * 6 + ch * 3 + 2) * 16 + t) * 4, cs[2][0]);
        }
        BAR();
        STAMP(4);
        f32x4 cp0[4], cp1[4]; float cp2[4];
        u32x2 stq[2]; u32x4 sa[2], sb[2];
#pragma unroll
        for (int p = 0; p < 4; ++p) {
            cp0[p] = lds_ldf4(L_CS1 + ((p * 6 + ch * 3 + 0) * 16 + 4 * g) * 4);
            cp1[p] = lds_ldf4(L_CS1 + ((p * 6 + ch * 3 + 1) * 16 + 4 * g) * 4);
            cp2[p] = *(LAS float*)(uintptr_t)(L_CS1 + ((p * 6 + ch * 3 + 2) * 16 + t) * 4);
        }
#pragma unroll
        for (int tt = 0; tt < 2; ++tt) {
            const int tile = w ^ tt;
            stq[tt] = *(LAS u32x2*)(uintptr_t)(L_ST1 + (16 * tile + t) * 8);
            sa[tt] = lds_ld128(L_ST1 + (16 * tile + 4 * g) * 8); sb[tt] = lds_ld128(L_ST1 + (16 * tile + 4 * g + 2) * 8);
        }
        SB();
        f32x4 csum[2];
        csum[0] = (cp0[0] + cp0[1]) + (cp0[2] + cp0[3]); csum[1] = (cp1[0] + cp1[1]) + (cp1[2] + cp1[3]);
        const float csv = (cp2[0] + cp2[1]) + (cp2[2] + cp2[3]);
        const float qs = ch ? 1.f : 0.18898223650461362f * 1.4426950408889634f;
        const float m0 = (odd && g == 0) ? 0.f : qs, m1 = (!odd && g == 3) ? 0.f : qs;
        const bool vpad = ch == 0 ? (odd && t < 4) : (!odd && t >= 12);
        const u32 blob = WS_KV + (u32)(RB * 16 + h) * 16384;
        u32x2 vv[2];
#pragma unroll
        for (int tt = 0; tt < 2; ++tt) {
            const int tile = w ^ tt;
            const float rstd = __uint_as_float(stq[tt][0]), mr = __uint_as_float(stq[tt][1]);
            float a[8];
#pragma unroll
            for (int i = 0; i < 4; ++i) { a[i] = fmaf(acc[tt][0][i], rstd, csum[0][i] * mr) * m0; a[4 + i] = fmaf(acc[tt][1][i], rstd, csum[1][i] * mr) * m1; }
            const u32x4 f = {pack2(a[0], a[1]), pack2(a[2], a[3]), pack2(a[4], a[5]), pack2(a[6], a[7])};
            if (ch) { st_sc1(rs, blob + tile * 1024 + l * 16, f); lds_st128(L_K + (16 + tile) * 1024 + l * 16, f); }
            else lds_st128(L_Q + tile * 1024 + l * 16, f);
            const float v0 = fmaf(acc[tt][2][0], __uint_as_float(sa[tt][0]), csv * __uint_as_float(sa[tt][1]));
            const float v1 = fmaf(acc[tt][2][1], __uint_as_float(sa[tt][2]), csv * __uint_as_float(sa[tt][3]));
            const float v2 = fmaf(acc[tt][2][2], __uint_as_float(sb[tt][0]), csv * __uint_as_float(sb[tt][1]));
            const float v3 = fmaf(acc[tt][2][3], __uint_as_float(sb[tt][2]), csv * __uint_as_float(sb[tt][3]));
            vv[tt] = (u32x2){pack2(v0, v1), pack2(v2, v3)};
            if (vpad) vv[tt] = (u32x2){0x3f803f80u, 0x3f803f80u};
        }
        const u32x4 vfr = ch ? (u32x4){vv[1][0], vv[1][1], vv[0][0], vv[0][1]} : (u32x4){vv[0][0], vv[0][1], vv[1][0], vv[1][1]};
        st_sc1(rs, blob + 8192 + (tp * 2 + ch) * 1024 + l * 16, vfr);
        lds_st128(L_V + ((8 + tp) * 2 + ch) * 1024 + l * 16, vfr);
    }
    {
        WAIT_VM(0);
        set_flag(ws, WS_F1 + (u32)(RB * 16 + h) * FGRP + w * FSTRIDE, tag, l);
        BAR();
        if ((tid & 255) == 0 && ppok) __hip_atomic_store((GAS u32*)(ws + WS_FW + ppfl * 64), tag, __ATOMIC_RELAXED, __HIP_MEMORY_SCOPE_AGENT);
    }
    const u32x4 qf = lds_ld128(L_Q + w * 1024 + l * 16);
    STAMP(5);
    SB();
    const int cg4 = 7 * (xcd & 1) + loc % 7, RB4 = 4 * (xcd >> 1) + loc / 7;
    const int rq = loc / 7, cg = loc % 7;
    const int cw = w & 3, kh = w >> 2;
    const bool worker = loc < 28;
    {
        const int rbl = RB - 2 + (l & 1);
        const int rsafe = rbl >= 0 ? rbl : RB;
        wait_flags(ws, WS_F1 + (u32)(rsafe * 16 + h) * FGRP + ((l & 2) ? (w | 1) : w) * FSTRIDE, tag);
        STAMP(6);
#pragma unroll
        for (int blk = 0; blk < 2; ++blk) {
            const int rb = RB - 2 + blk;
            if (rb >= 0) glds16_run_sc1<2>(ws + WS_KV + (size_t)(rb * 16 + h) * 16384 + w * 1024, l * 16, L_K + blk * 8192 + w * 1024, 8192, L_V - L_K);
            else { lds_st128(L_K + blk * 8192 + tid * 16, (u32x4){0, 0, 0, 0}); lds_st128(L_V + blk * 8192 + tid * 16, (u32x4){0, 0, 0, 0}); }
        }
        WAIT_VM(0);
    }
    BAR();
    STAMP(7);
    u32 fwo;
    {
        const int cgs = worker ? cg : 0, cg4s = worker ? cg4 : 0;
        int pi = 392 + cgs * 8 + 4 * kh + (l & 3);
        if (l >= 4 && l < 11) pi = (2 * cg4s + (w >> 2)) * 7 + (l - 4);
        if (l >= 11 && l < 25) pi = 196 + cgs * 28 + 14 * kh + (l - 11);
        fwo = WS_FW + (u32)pi * 64;
    }
    const u32 fwv = __hip_atomic_load((GAS u32*)(ws + fwo), __ATOMIC_RELAXED, __HIP_MEMORY_SCOPE_AGENT);
    SB();
    u32x4 wof[8];
    u32x4 w1f[14];
    const u32 wofa = WS_WOF + (u32)(((worker ? cg : 0) * 4 + cw) * 16 + 8 * kh) * 1024 + l * 16;
    const u32 w1fa = WS_W1F + (u32)(((worker ? cg4 : 0) * 8 + w) * 14) * 1024 + l * 16;
#define PF2(i) do { if ((i) < 8) wof[(i) < 8 ? (i) : 0] = ld_sc1(rs, wofa + (i) * 1024); else w1f[(i) >= 8 ? (i) - 8 : 0] = ld_sc1(rs, w1fa + ((i) - 8) * 1024); } while (0)
    {
        const int base = w & ~1;
        f32x4 sc[18];
        {
            u32x4 kf[18];
#pragma unroll
            for (int a = 0; a < 18; ++a) kf[a] = lds_ld128(L_K + (base + a) * 1024 + l * 16);
            SB();
#pragma unroll
            for (int a = 0; a < 18; ++a) {
                sc[a] = mfma16(kf[a], qf, (f32x4){0.f, 0.f, 0.f, 0.f});
                SB();
            }
        }
        if (!__all(fwv == tag)) wait_flags(ws, fwo, tag);
        asm volatile("" ::: "memory");
        PF2(0); PF2(1);
        SB();
        u32x4 vf[18];
#pragma unroll
        for (int a = 0; a < 8; ++a) vf[a] = lds_ld128(L_V + (base >> 1) * 2048 + a * 1024 + l * 16);
        SB();
        const float NEG = -1e30f;
        const int qq = 256 + 16 * w + t;
#pragma unroll
        for (int a = 0; a < 2; ++a)
#pragma unroll
            for (int i = 0; i < 4; ++i) { const int kk = 16 * (base + a) + 4 * g + i; if (kk <= qq - 256) sc[a][i] = NEG; }
#pragma unroll
        for (int a = 16; a < 18; ++a)
#pragma unroll
            for (int i = 0; i < 4; ++i) { const int kk = 16 * (base + a) + 4 * g + i; if (kk > qq) sc[a][i] = NEG; }
        if (RB < 2) {
            asm volatile("" ::: "memory");
            const int tmin = 8 * (2 - RB);
#pragma unroll
            for (int a = 0; a < 18; ++a) if (base + a < tmin) sc[a] = (f32x4){NEG, NEG, NEG, NEG};
        }
        PF2(2); PF2(3);
        SB();
        float ma = NEG, mb = NEG;
#pragma unroll
        for (int a = 0; a < 18; a += 2) {
            ma = fmaxf(fmaxf(sc[a][0], sc[a][1]), ma); ma = fmaxf(fmaxf(sc[a][2], sc[a][3]), ma);
            mb = fmaxf(fmaxf(sc[a + 1][0], sc[a + 1][1]), mb); mb = fmaxf(fmaxf(sc[a + 1][2], sc[a + 1][3]), mb);
        }
        PF2(4); PF2(5);
        SB();
        float m = fmaxf(ma, mb);
        m = rows_max(m);
        f32x4 o0 = {0.f, 0.f, 0.f, 0.f}, o1 = {0.f, 0.f, 0.f, 0.f};
#pragma unroll
        for (int a = 0; a < 9; ++a) {
            if (2 * a + 8 < 18) {
                vf[2 * a + 8] = lds_ld128(L_V + (base >> 1) * 2048 + (2 * a + 8) * 1024 + l * 16);
                vf[2 * a + 9] = lds_ld128(L_V + (base >> 1) * 2048 + (2 * a + 9) * 1024 + l * 16);
            }
            if (a < 8) { PF2(6 + 2 * a); PF2(7 + 2 * a); }
            u32x4 pf;
            pf[0] = pack2(__builtin_amdgcn_exp2f(sc[2 * a][0] - m), __builtin_amdgcn_exp2f(sc[2 * a][1] - m));
            pf[1] = pack2(__builtin_amdgcn_exp2f(sc[2 * a][2] - m), __builtin_amdgcn_exp2f(sc[2 * a][3] - m));
            pf[2] = pack2(__builtin_amdgcn_exp2f(sc[2 * a + 1][0] - m), __builtin_amdgcn_exp2f(sc[2 * a + 1][1] - m));
            pf[3] = pack2(__builtin_amdgcn_exp2f(sc[2 * a + 1][2] - m), __builtin_amdgcn_exp2f(sc[2 * a + 1][3] - m));
            o0 = mfma16(vf[2 * a], pf, o0);
            o1 = mfma16(vf[2 * a + 1], pf, o1);
            SB();
        }
        const float den = rows_add(g == (odd ? 0 : 3) ? (odd ? o0[0] : o1[0]) : 0.f);
        const float r = 1.f / den;
        const float m0 = (odd && g == 0) ? 0.f : r, m1 = (!odd && g == 3) ? 0.f : r;
        const u32x4 of = {pack2(o0[0] * m0, o0[1] * m0), pack2(o0[2] * m0, o0[3] * m0), pack2(o1[0] * m1, o1[1] * m1), pack2(o1[2] * m1, o1[3] * m1)};
        STAMP(8);
        st_sc1(rs, WS_O + (u32)((RB * 8 + w) * 16 + h) * 1024 + l * 16, of);
        WAIT_VM(0);
        set_flag(ws, WS_F2 + (u32)(RB * 8 + w) * FGRP + h * FSTRIDE, tag, l);
    }
    STAMP(9);
    SB();
    if (!worker) {
        if (b == 255) {
#pragma unroll
            for (int i = 0; i < 4; ++i) wait_flags(ws, WS_F1 + (u32)(w * 32 + i * 8 + (l >> 3)) * FGRP + (l & 7) * FSTRIDE, tag);
            BAR();
            if (tid == 0) __hip_atomic_store((GAS u32*)(ws + WS_GEN), tag, __ATOMIC_RELAXED, __HIP_MEMORY_SCOPE_AGENT);
        }
        STAMP_FLUSH();
        return;
    }
    const int RBW = 2 * xcd + (rq >> 1);
#define TTG(tt) (8 * RBW + 4 * ((tt) >> 1) + 2 * (rq & 1) + ((tt) & 1))
    const int fr = l >> 3, fo8 = l & 7;
    const int TTf = TTG(w >> 1), tl = 8 * (w & 1) + fr;
    const int tok = 16 * TTf + tl;
    const u32x4 xr = *(const u32x4*)((const char*)x + (size_t)tok * 896 + cg * 128 + fo8 * 16);
    SB();
    BAR();
    const int TTc = TTG(cw);
    wait_flags(ws, WS_F2 + (u32)TTc * FGRP + (8 * kh + (l & 7)) * FSTRIDE, tag);
    STAMP(10);
    SB();
    glds16_run_sc1<8>(ws + WS_O + (size_t)(TTc * 16 + 8 * kh) * 1024, l * 16, (cw * 16 + 8 * kh) * 1024, 1024, 1024);
    WAIT_VM(0);
    BAR();
    u32x4 w2f[28];
    const int rot5 = 4 * ((cg + 2 * rq) % 7);
    const u32 w2fa = WS_W2F + (u32)((cg * 4 + cw) * 56 + 28 * kh) * 1024 + l * 16;
#define W2F(i) ld_sc1(rs, w2fa + (u32)((i) + rot5 < 28 ? (i) + rot5 : (i) + rot5 - 28) * 1024)
    u32x4 hres;
    {
        f32x4 acc[4];
        u32x4 af[8];
        const u32 ab = (8 * kh) * 1024 + l * 16;
#pragma unroll
        for (int n = 0; n < 6; ++n) af[n] = lds_ld128(ab + (n & 3) * 16384 + (n >> 2) * 1024);
        SB();
#pragma unroll
        for (int n = 0; n < 32; ++n) {
            if (n < 4) mfma_first(acc[n & 3], wof[n >> 2], af[n & 7]); else mfma_acc(acc[n & 3], wof[n >> 2], af[n & 7]);
            if (n + 6 < 32) af[(n + 6) & 7] = lds_ld128(ab + ((n + 6) & 3) * 16384 + ((n + 6) >> 2) * 1024);
            if ((n & 3) == 1 && (n >> 2) < 7) w2f[n >> 2] = W2F(n >> 2);
            SB();
        }
        MFMA_FENCE4(acc);
        STAMP(11);
#pragma unroll
        for (int tt = 0; tt < 4; ++tt) lds_stf4(L_RED3 + (w * 4 + tt) * 1024 + l * 16, acc[tt]);
        BAR();
        const u32 ra = L_RED3 + (w >> 1) * 1024 + (fo8 >> 1) * 4096 + ((2 * (fo8 & 1)) * 16 + tl) * 16;
        const f32x4 a0 = lds_ldf4(ra) + lds_ldf4(ra + 16384), a1 = lds_ldf4(ra + 256) + lds_ldf4(ra + 16384 + 256);
        hres = (u32x4){pack2(bflo(xr[0]) + a0[0], bfhi(xr[0]) + a0[1]), pack2(bflo(xr[1]) + a0[2], bfhi(xr[1]) + a0[3]),
                       pack2(bflo(xr[2]) + a1[0], bfhi(xr[2]) + a1[1]), pack2(bflo(xr[3]) + a1[2], bfhi(xr[3]) + a1[3])};
        st_sc1(rs, WS_H + (u32)(TTf * 14 + 2 * cg + (fo8 >> 2)) * 1024 + ((fo8 & 3) * 16 + tl) * 16, hres);
        float sum = 0.f, sq = 0.f;
#pragma unroll
        for (int j = 0; j < 4; ++j) { const float a = bflo(hres[j]), c = bfhi(hres[j]); sum += a + c; sq = fmaf(a, a, sq); sq = fmaf(c, c, sq); }
        sum = oct_add(sum); sq = oct_add(sq);
        if (fo8 == 0) st_sc1_64(rs, WS_ST + (u32)(TTf * 7 + cg) * 128 + tl * 8, (u32x2){__float_as_uint(sum), __float_as_uint(sq)});
        WAIT_VM(0);
        set_flag(ws, WS_F3 + (u32)TTf * FGRP + (2 * cg + (w & 1)) * FSTRIDE, tag, l);
    }
    STAMP(12);
    SB();
#pragma unroll
    for (int s = 7; s < 14; ++s) w2f[s] = W2F(s);
    SB();
    f32x4 cs4, bias4;
    {
        const u32x4 ones = {0x3f803f80u, 0x3f803f80u, 0x3f803f80u, 0x3f803f80u};
        f32x4 c4 = {0.f, 0.f, 0.f, 0.f}, c5 = {0.f, 0.f, 0.f, 0.f};
#pragma unroll
        for (int s = 0; s < 14; s += 2) { c4 = mfma16(w1f[s], ones, c4); c5 = mfma16(w1f[s + 1], ones, c5); }
        cs4 = c4 + c5;
        const u32x2 bb = *(const u32x2*)((const char*)b1 + cg4 * 256 + w * 32 + g * 8);
        bias4 = (f32x4){bflo(bb[0]), bfhi(bb[0]), bflo(bb[1]), bfhi(bb[1])};
    }
    BAR();
    STAMP(13);
    wait_flags(ws, WS_F3 + (u32)(8 * RB4 + (w & 3) + ((l & 16) ? 4 : 0)) * FGRP + ((l & 15) < 14 ? (l & 15) : 0) * FSTRIDE, tag);
    STAMP(14);
    SB();
    {
        glds16_run_sc1<1>(ws + WS_ST + (size_t)((8 * RB4 + w) * 7) * 128, l * 16, L_ST4 + 1024 + w * 1024, 0, 0);
        const char* hsA = ws + WS_H + (size_t)((8 * RB4 + (w & 3)) * 14 + (w >> 2)) * 1024;
        const char* hsB = hsA + 4 * 14 * 1024;
        u32 hv = l * 16 - 2048;
        dma_seek((w >> 2) * 8192 + (w & 3) * 1024 - 16384);
#pragma unroll
        for (int i = 0; i < (P4DEEP ? 7 : 4); ++i) dma_next_sc1<16384, 2048>(hsA, hv);
        f32x4 acc[8];
        u32x4 af[8];
        u32 ab0 = l * 16, ab1 = l * 16 + 65536;
        OPAQUE(ab1);
#define H4_BLK(n) ((((n) % 56) >> 2) * 8 + ((n) / 56) * 4 + ((n) & 3))
#define H4_ADDR(n) (H4_BLK(n) < 64 ? ab0 + H4_BLK(n) * 1024 : ab1 + (H4_BLK(n) - 64) * 1024)
#define P4_EPILOGUE(M0) do { _Pragma("unroll") for (int m = (M0); m < (M0) + 2; ++m) { \
            u32 a[2][2]; \
            _Pragma("unroll") for (int u = 0; u < 2; ++u) { \
                const int tt = 2 * m + u; \
                const u32x2 st = *(LAS u32x2*)(uintptr_t)(L_ST4 + (16 * tt + t) * 8); \
                const float rstd = __uint_as_float(st[0]), mr = __uint_as_float(st[1]); \
                float v[4]; \
                _Pragma("unroll") for (int i = 0; i < 4; ++i) { float z = fmaf(acc[tt][i], rstd, fmaf(cs4[i], mr, bias4[i])); z = fmaxf(z, 0.f); v[i] = z * z; } \
                a[u][0] = pack2(v[0], v[1]); a[u][1] = pack2(v[2], v[3]); \
            } \
              \
            const auto r0 = __builtin_amdgcn_permlane16_swap(a[0][0], a[1][0], false, false); \
            const auto r1 = __builtin_amdgcn_permlane16_swap(a[0][1], a[1][1], false, false); \
            const u32x4 o = {r0[0], r1[0], r0[1], r1[1]}; \
            const int TTo = 8 * RB4 + 2 * m + (g & 1); \
            st_sc1(rs, WS_HH + (u32)(TTo * 56 + 4 * cg4 + (w >> 1)) * 1024 + ((2 * (w & 1) + (g >> 1)) * 16 + t) * 16, o); \
        } } while (0)
        wait_vm<P4DEEP ? 5 : 2>();
        STAMP(20);
        BAR();
#pragma unroll
        for (int n = 0; n < 6; ++n) af[n] = lds_ld128(H4_ADDR(n));
        SB();
#pragma unroll
        for (int seg = 0; seg < 7; ++seg) {
            constexpr int SB0[8] = {0, 10, 26, 42, 50, 66, 82, 112};
            if (seg == 1) {
                wait_vm<P4DEEP ? 6 : 2>();
                const u32x2 p0 = *(LAS u32x2*)(uintptr_t)(L_ST4 + 1024 + w * 1024 + g * 128 + t * 8);
                const u32x2 p1 = *(LAS u32x2*)(uintptr_t)(L_ST4 + 1024 + w * 1024 + (g + 4) * 128 + t * 8);
                float sum = __uint_as_float(p0[0]), sq = __uint_as_float(p0[1]);
                if (g < 3) { sum += __uint_as_float(p1[0]); sq += __uint_as_float(p1[1]); }
                sum += xor16(sum); sq += xor16(sq); sum += xor32(sum); sq += xor32(sq);
                const float mean = sum * (1.f / 448.f);
                const float rstd = rsqrtf(fmaxf(sq * (1.f / 448.f) - mean * mean, 0.f) + 1e-5f);
                if (g == 0) lds_st64(L_ST4 + (16 * w + t) * 8, (u32x2){__float_as_uint(rstd), __float_as_uint(-mean * rstd)});
                BAR0();
            }
            if (seg == 2) { wait_vm<P4DEEP ? 8 : 4>(); BAR(); }
            if (seg == 3) { wait_vm<7>(); BAR0(); }
            if (seg == 4) { wait_vm<5>(); BAR0(); }
            if (seg == 5) { wait_vm<7>(); BAR0(); }
            if (seg == 6) {
                wait_vm<4>(); BAR0();
                if (tid == 0) __hip_atomic_store((GAS u32*)(ws + WS_F4 + (RB4 * 2) * FGRP + cg4 * FSTRIDE), tag, __ATOMIC_RELAXED, __HIP_MEMORY_SCOPE_AGENT);
            }
#pragma unroll
            for (int n = SB0[seg]; n < SB0[seg + 1]; ++n) {
                if (n == 56) {
                    asm volatile("s_nop 7\n\ts_nop 1" : "+v"(acc[0]), "+v"(acc[1]), "+v"(acc[2]), "+v"(acc[3]));
                    P4_EPILOGUE(0);
                    SB();
                }
                const int ai = (n / 56) * 4 + (n & 3), ks = (n % 56) >> 2;
                if (ks == 0) mfma_first(acc[ai], w1f[ks], af[n & 7]); else mfma_acc(acc[ai], w1f[ks], af[n & 7]);
                if (n + 6 < 112) af[(n + 6) & 7] = lds_ld128(H4_ADDR(n + 6));
                if (!P4DEEP && (n == 4 || n == 8 || n == 12)) dma_next_sc1<16384, 2048>(hsA, hv);
                if (n == (P4DEEP ? 0 : 16)) { hv = l * 16 - 2048; dma_seek((w >> 2) * 8192 + (4 + (w & 3)) * 1024 - 16384); }
                if (n >= (P4DEEP ? 0 : 16) && n <= (P4DEEP ? 24 : 40) && (n & 3) == 0) dma_next_sc1<16384, 2048>(hsB, hv);
                if (n >= 58 && (n - 58) % 6 == 0 && (n - 58) / 6 < 7) w2f[14 + (n - 58) / 6] = W2F(14 + (n - 58) / 6);
                SB();
            }
        }
        asm volatile("s_nop 7\n\ts_nop 1" : "+v"(acc[4]), "+v"(acc[5]), "+v"(acc[6]), "+v"(acc[7]));
        STAMP(15);
        P4_EPILOGUE(2);
        WAIT_VM(0);
        BAR();
        if (tid == 0) __hip_atomic_store((GAS u32*)(ws + WS_F4 + (RB4 * 2 + 1) * FGRP + cg4 * FSTRIDE), tag, __ATOMIC_RELAXED, __HIP_MEMORY_SCOPE_AGENT);
    }
    STAMP(16);
    SB();
#pragma unroll
    for (int s = 21; s < 28; ++s) w2f[s] = W2F(s);
    SB();
    {
        const u32x4 bb = *(const u32x4*)((const char*)b2 + cg * 128 + fo8 * 16);
        const u32 f4o = WS_F4 + (u32)(RBW * 2) * FGRP + (7 * kh + (l < 7 ? l : 0)) * FSTRIDE;
        wait_flags(ws, f4o, tag);
        STAMP(17);
        SB();
        u32 f4b = 0;
        const char* hsA = ws + WS_HH + (size_t)(TTG(cw & 1) * 56 + 28 * kh + (cw >> 1) + rot5) * 1024;
        const char* hsB = ws + WS_HH + (size_t)(TTG(2 + (cw & 1)) * 56 + 28 * kh + (cw >> 1) + rot5) * 1024;
        const int wrap5 = (28 - rot5) >> 1;
#define H5SRC(base, i) ((base) + ((i) < wrap5 ? (i) * 2048 : (i) * 2048 - 28 * 1024))
        const u32 hl = kh * L_H5 + (cw >> 1) * 2048 + (cw & 1) * 1024;
        const u32 hv = l * 16;
        dma_seek(hl - 4096);
#pragma unroll
        for (int i = 0; i < (P5DEEP ? 10 : 6); ++i) dma_next_sc1_b<4096>(H5SRC(hsA, i), hv);
        f32x4 acc[4];
        u32x4 af[8];
        u32 ab0 = kh * L_H5 + l * 16, ab1 = kh * L_H5 + l * 16 + 65536;
        OPAQUE(ab0); OPAQUE(ab1);
#define H5_KS(n) (((n) % 56) >> 1)
#define H5_SLOT(n) ((n) < 56 ? H5_KS(n) : H5_KS(n) < 12 ? 28 + H5_KS(n) : H5_KS(n) - 12)
#define H5_BLK(n) (H5_SLOT(n) * 2 + ((n) & 1))
#define H5_ADDR(n) (H5_BLK(n) < 64 ? ab0 + H5_BLK(n) * 1024 : ab1 + (H5_BLK(n) - 64) * 1024)
        wait_vm<P5DEEP ? 8 : 4>();
        STAMP(21);
        BAR();
#pragma unroll
        for (int n = 0; n < 6; ++n) af[n] = lds_ld128(H5_ADDR(n));
        SB();
#pragma unroll
        for (int seg = 0; seg < 9; ++seg) {
            constexpr int SB0[10] = {0, 2, 10, 26, 42, 50, 66, 82, 98, 112};
            if (P5DEEP) {
                if (seg == 1) {
                    wait_vm<7>(); BAR0();
                    asm volatile("global_load_dword %0, %1, %2 sc1" : "=v"(f4b) : "v"(f4o + FGRP), "s"(ws) : "memory");
                }
                if (seg == 2) {
                    wait_vm<3>();
                    asm volatile("" : "+v"(f4b));
                    if (!__all(f4b == tag)) wait_flags(ws, f4o + FGRP, tag);
                    BAR0();
                }
                if (seg == 3) { wait_vm<7>(); BAR0(); }
                if (seg == 4) { wait_vm<10>(); BAR0(); }
                if (seg == 5) { wait_vm<9>(); BAR0(); }
                if (seg == 6) { wait_vm<6>(); BAR0(); }
                if (seg == 7) { wait_vm<2>(); BAR0(); }
                if (seg == 8) { wait_vm<0>(); BAR0(); }
            } else {
            if (seg == 1) { wait_vm<3>(); BAR0(); }
            if (seg == 2) {
                wait_vm<2>(); BAR0();
                asm volatile("global_load_dword %0, %1, %2 sc1" : "=v"(f4b) : "v"(f4o + FGRP), "s"(ws) : "memory");
            }
            if (seg == 3) {
                wait_vm<2>();
                asm volatile("" : "+v"(f4b));
                if (!__all(f4b == tag)) wait_flags(ws, f4o + FGRP, tag);
                BAR0();
            }
            if (seg == 4) { wait_vm<5>(); BAR0(); }
            if (seg == 5) { wait_vm<4>(); BAR0(); }
            if (seg == 6) { wait_vm<5>(); BAR0(); }
            if (seg == 7) { wait_vm<2>(); BAR0(); }
            if (seg == 8) { wait_vm<0>(); BAR0(); }
            }
#pragma unroll
            for (int n = SB0[seg]; n < SB0[seg + 1]; ++n) {
                const int ai = (n / 56) * 2 + (n & 1);
                if (H5_KS(n) == 0) mfma_first(acc[ai], w2f[H5_KS(n)], af[n & 7]); else mfma_acc(acc[ai], w2f[H5_KS(n)], af[n & 7]);
                if (n + 6 < 112) af[(n + 6) & 7] = lds_ld128(H5_ADDR(n + 6));
                constexpr int NB0 = P5DEEP ? 12 : 28;
                if (n <= (P5DEEP ? 9 : 21) && n % 3 == 0) dma_next_sc1_b<4096>(H5SRC(hsA, (P5DEEP ? 10 : 6) + n / 3), hv);
                if (n == NB0) dma_seek(hl + 28 * 2048 - 4096);
                if (n == NB0 + 18) dma_seek(hl - 4096);
                if (n >= NB0 && n <= NB0 + 39 && (n - NB0) % 3 == 0) dma_next_sc1_b<4096>(H5SRC(hsB, (n - NB0) / 3), hv);
                SB();
            }
        }
        MFMA_FENCE4(acc);
        STAMP(18);
        const u32 rw = (w < 6 ? 32768 + w * 4096 : L_H5 + 32768 + (w - 6) * 4096);
#pragma unroll
        for (int tt = 0; tt < 4; ++tt) lds_stf4(rw + tt * 1024 + l * 16, acc[tt]);
        BAR();
        const int c5 = fo8 >> 1;
        const u32 lp = (w >> 1) * 1024 + ((2 * (fo8 & 1)) * 16 + tl) * 16;
        const u32 r0a = 32768 + c5 * 4096 + lp, r1a = (c5 < 2 ? 32768 + (4 + c5) * 4096 : L_H5 + 32768 + (c5 - 2) * 4096) + lp;
        const f32x4 a0 = lds_ldf4(r0a) + lds_ldf4(r1a), a1 = lds_ldf4(r0a + 256) + lds_ldf4(r1a + 256);
        const u32x4 o = {pack2(a0[0] + bflo(bb[0]) + bflo(hres[0]), a0[1] + bfhi(bb[0]) + bfhi(hres[0])),
                         pack2(a0[2] + bflo(bb[1]) + bflo(hres[1]), a0[3] + bfhi(bb[1]) + bfhi(hres[1])),
                         pack2(a1[0] + bflo(bb[2]) + bflo(hres[2]), a1[1] + bfhi(bb[2]) + bfhi(hres[2])),
                         pack2(a1[2] + bflo(bb[3]) + bflo(hres[3]), a1[3] + bfhi(bb[3]) + bfhi(hres[3]))};
        *(u32x4*)((char*)out + (size_t)tok * 896 + cg * 128 + fo8 * 16) = o;
    }
    STAMP(19);
    STAMP_FLUSH();
    asm volatile("fused_code_end_marker:");
}

extern "C" void kernel_launch(void* const* d_in, const int* in_sizes, int n_in, void* d_out, int out_size, void* d_ws, size_t ws_size,
                              hipStream_t stream) {
    (void)hipFuncSetAttribute((const void*)fused_kernel, hipFuncAttributeMaxDynamicSharedMemorySize, LDS_BYTES);
    fused_kernel<<<256, 512, LDS_BYTES, stream>>>((const u16*)d_in[0], (const u16*)d_in[1], (const u16*)d_in[2], (const u16*)d_in[4],
                                                  (const u16*)d_in[5], (const u16*)d_in[6], (const u16*)d_in[7], (u16*)d_out, (char*)d_ws);
}
```

```cpp
#include <hip/hip_runtime.h>
#include <stdint.h>

typedef unsigned short u16;
typedef unsigned int u32;
typedef __attribute__((ext_vector_type(8))) __bf16 bf16x8;
typedef __attribute__((ext_vector_type(2))) __bf16 bf16x2;
typedef __attribute__((ext_vector_type(4))) short s16x4;
typedef __attribute__((ext_vector_type(4))) float f32x4;
typedef __attribute__((ext_vector_type(2))) float f32x2;
typedef __attribute__((ext_vector_type(4))) u32 u32x4;
typedef __attribute__((ext_vector_type(2))) u32 u32x2;
#define LAS __attribute__((address_space(3)))
#define GAS __attribute__((address_space(1)))
#define DI __device__ __forceinline__

#define FSTRIDE 128
constexpr u32 FGRP = 16 * FSTRIDE < 128 ? 128 : 16 * FSTRIDE;
constexpr u32 WS_GEN = 0;
constexpr u32 WS_FW = 4096;
constexpr u32 WS_FQ = WS_FW + 448 * 128;
constexpr u32 WS_F1 = WS_FQ + 384 * 128;
constexpr u32 WS_F2 = WS_F1 + 256 * FGRP;
constexpr u32 WS_F3 = WS_F2 + 128 * FGRP;
constexpr u32 WS_F4 = WS_F3 + 128 * FGRP;
constexpr u32 WS_KV = WS_F4 + 32 * FGRP;
constexpr u32 WS_O = WS_KV + 16 * 16 * 16384;
constexpr u32 WS_H = WS_O + 128 * 16 * 1024;
constexpr u32 WS_ST = WS_H + 128 * 14 * 1024;
constexpr u32 WS_HH = WS_ST + 128 * 14 * 128;
constexpr u32 WS_W1F = WS_HH + 128 * 56 * 1024;
constexpr u32 WS_W2F = WS_W1F + 112 * 14 * 1024;
constexpr u32 WS_WOF = WS_W2F + 28 * 56 * 1024;
constexpr u32 WS_WQF = WS_WOF + 28 * 16 * 1024;
constexpr u32 WS_END = WS_WQF + 16 * 48 * 1024;
constexpr u32 L_WA = 114688;
constexpr u32 L_RT = 151552;
constexpr u32 L_GEN = 157696;
constexpr u32 L_ST1 = 157952;
constexpr u32 L_CS1 = 158976;
constexpr u32 L_Q = 49152;
constexpr u32 L_PP = 98304;
constexpr u32 L_K = 0, L_V = 24576;
constexpr u32 L_RED3 = 65536;
constexpr u32 L_ST4 = 114688;
constexpr u32 L_H5 = 81920;
constexpr u32 L_RED5 = 32768;
constexpr u32 LDS_BYTES = 163840;
#define P4DEEP 0
#define P5DEEP 0
constexpr int SPIN_MAX = 1 << 14;

DI f32x4 mfma16(u32x4 a, u32x4 b, f32x4 c) {
    return __builtin_amdgcn_mfma_f32_16x16x32_bf16(__builtin_bit_cast(bf16x8, a), __builtin_bit_cast(bf16x8, b), c, 0, 0, 0);
}
DI void mfma_acc(f32x4& acc, const u32x4& a, const u32x4& b) {
    asm volatile("v_mfma_f32_16x16x32_bf16 %0, %1, %2, %0" : "+v"(acc) : "v"(a), "v"(b));
}
DI void mfma_first(f32x4& acc, const u32x4& a, const u32x4& b) {
    asm volatile("v_mfma_f32_16x16x32_bf16 %0, %1, %2, 0" : "=&v"(acc) : "v"(a), "v"(b));
}
#define MFMA_FENCE4(a) asm volatile("s_nop 7\n\ts_nop 1" : "+v"(a[0]), "+v"(a[1]), "+v"(a[2]), "+v"(a[3]))
#define OPAQUE(x) asm volatile("" : "+v"(x))
DI u32 pack2(float lo, float hi) {
    f32x2 v = {lo, hi};
    bf16x2 r = __builtin_convertvector(v, bf16x2);
    return __builtin_bit_cast(u32, r);
}
DI float dot2bf(u32 a, u32 b, float c) {
    return __builtin_amdgcn_fdot2_f32_bf16(__builtin_bit_cast(bf16x2, a), __builtin_bit_cast(bf16x2, b), c, false);
}
DI float relu(float z) { float r; asm("v_max_f32 %0, 0, %1" : "=v"(r) : "v"(z)); return r; }
DI float bflo(u32 u) { return __uint_as_float(u << 16); }
DI float bfhi(u32 u) { return __uint_as_float(u & 0xffff0000u); }
DI u32x2 trread(u32 off) {
    s16x4 r = __builtin_amdgcn_ds_read_tr16_b64_v4i16((LAS s16x4*)(uintptr_t)off);
    return __builtin_bit_cast(u32x2, r);
}
DI u32x4 trfrag(u32 off0, u32 off1) {
    u32x2 a = trread(off0), b = trread(off1);
    u32x4 r = {a[0], a[1], b[0], b[1]};
    return r;
}
DI u32x4 lds_ld128(u32 off) { return *(LAS u32x4*)(uintptr_t)off; }
DI f32x4 lds_ldf4(u32 off) { return *(LAS f32x4*)(uintptr_t)off; }
DI void lds_st128(u32 off, u32x4 v) { *(LAS u32x4*)(uintptr_t)off = v; }
DI void lds_stf4(u32 off, f32x4 v) { *(LAS f32x4*)(uintptr_t)off = v; }
DI void lds_st64(u32 off, u32x2 v) { *(LAS u32x2*)(uintptr_t)off = v; }
DI void lds_stf(u32 off, float v) { *(LAS float*)(uintptr_t)off = v; }
template <int N>
DI void glds16_run(const void* sbase, u32 voff, u32 lds, u32 gstep, u32 lstep) {
    u32 keep;
    asm volatile("s_mov_b32 %0, m0\n\ts_mov_b32 m0, %3\n\t"
                 ".rept %c6\n\t"
                 "s_nop 0\n\t"
                 "global_load_lds_dwordx4 %1, %2\n\t"
                 "s_add_u32 m0, m0, %5\n\t"
                 "v_add_u32 %1, %4, %1\n\t"
                 ".endr\n\t"
                 "s_mov_b32 m0, %0"
                 : "=&s"(keep), "+v"(voff) : "s"(sbase), "s"(lds), "s"(gstep), "s"(lstep), "i"(N) : "memory", "scc");
}
template <int N>
DI void glds16_run_sc1(const void* sbase, u32 voff, u32 lds, u32 gstep, u32 lstep) {
    u32 keep;
    asm volatile("s_mov_b32 %0, m0\n\ts_mov_b32 m0, %3\n\t"
                 ".rept %c6\n\t"
                 "s_nop 0\n\t"
                 "global_load_lds_dwordx4 %1, %2 sc1\n\t"
                 "s_add_u32 m0, m0, %5\n\t"
                 "v_add_u32 %1, %4, %1\n\t"
                 ".endr\n\t"
                 "s_mov_b32 m0, %0"
                 : "=&s"(keep), "+v"(voff) : "s"(sbase), "s"(lds), "s"(gstep), "s"(lstep), "i"(N) : "memory", "scc");
}
DI void glds16_one(const void* sbase, u32 voff, u32 goff, u32 lds) {
    u32 tmp;
    asm volatile("s_mov_b32 m0, %4\n\tv_add_u32 %0, %3, %1\n\tglobal_load_lds_dwordx4 %0, %2" : "=&v"(tmp) : "v"(voff), "s"(sbase), "s"(goff), "s"(lds) : "memory");
}
DI void dma_seek(u32 lds) { asm volatile("s_mov_b32 m0, %0" :: "s"(lds) : "memory"); }
template <int LSTEP, int GSTEP>
DI void dma_next_sc1(const void* sbase, u32& voff) {
    asm volatile("s_add_u32 m0, m0, %c2\n\tv_add_u32 %0, %c3, %0\n\tglobal_load_lds_dwordx4 %0, %1 sc1" : "+v"(voff) : "s"(sbase), "i"(LSTEP), "i"(GSTEP) : "memory", "scc");
}
template <int LSTEP>
DI void dma_next_sc1_b(const void* sbase, u32 voff) {
    asm volatile("s_add_u32 m0, m0, %c2\n\ts_nop 0\n\tglobal_load_lds_dwordx4 %0, %1 sc1" :: "v"(voff), "s"(sbase), "i"(LSTEP) : "memory", "scc");
}
#define PIN_S(x) asm volatile("" : "+s"(x))
template <int N>
DI void glds4_run(const void* sbase, u32 voff, u32 lds, u32 gstep) {
    u32 keep;
    asm volatile("s_mov_b32 %0, m0\n\ts_mov_b32 m0, %3\n\ts_nop 0\n\t"
                 ".rept %c5\n\t"
                 "global_load_lds_dword %1, %2\n\t"
                 "v_add_u32 %1, %4, %1\n\t"
                 ".endr\n\t"
                 "s_mov_b32 m0, %0"
                 : "=&s"(keep), "+v"(voff) : "s"(sbase), "s"(lds), "s"(gstep), "i"(N) : "memory");
}
#define SB() __builtin_amdgcn_sched_barrier(0)
#define WAIT_VM(n) asm volatile("s_waitcnt vmcnt(" #n ")" ::: "memory")
template <int N> DI void wait_vm() { asm volatile("s_waitcnt vmcnt(%c0)" :: "i"(N) : "memory"); }
#define BAR0() asm volatile("s_barrier" ::: "memory")
#define BAR() asm volatile("s_waitcnt lgkmcnt(0)\n\ts_barrier" ::: "memory")
DI float rows_add(float v) {
    const u32 a = __float_as_uint(v);
    const auto r = __builtin_amdgcn_permlane16_swap(a, a, false, false);
    const u32 b2 = __float_as_uint(__uint_as_float(r[0]) + __uint_as_float(r[1]));
    const auto q = __builtin_amdgcn_permlane32_swap(b2, b2, false, false);
    return __uint_as_float(q[0]) + __uint_as_float(q[1]);
}
DI float rows_max(float v) {
    const u32 a = __float_as_uint(v);
    const auto r = __builtin_amdgcn_permlane16_swap(a, a, false, false);
    const u32 b2 = __float_as_uint(fmaxf(__uint_as_float(r[0]), __uint_as_float(r[1])));
    const auto q = __builtin_amdgcn_permlane32_swap(b2, b2, false, false);
    return fmaxf(__uint_as_float(q[0]), __uint_as_float(q[1]));
}
DI float oct_add(float v) {
    v += __uint_as_float(__builtin_amdgcn_update_dpp(0, __float_as_uint(v), 0xB1, 0xf, 0xf, true));
    v += __uint_as_float(__builtin_amdgcn_update_dpp(0, __float_as_uint(v), 0x4E, 0xf, 0xf, true));
    v += __uint_as_float(__builtin_amdgcn_update_dpp(0, __float_as_uint(v), 0x141, 0xf, 0xf, true));
    return v;
}
DI float xor16(float v) { return __shfl_xor(v, 16); }
DI float xor32(float v) { return __shfl_xor(v, 32); }

typedef __amdgpu_buffer_rsrc_t rsrc_t;
DI u32x4 ld_sc1(rsrc_t r, u32 off) { return __builtin_amdgcn_raw_buffer_load_b128(r, off, 0, 16); }
DI u32x2 ld_sc1_64(rsrc_t r, u32 off) { return __builtin_amdgcn_raw_buffer_load_b64(r, off, 0, 16); }
DI void st_sc1(rsrc_t r, u32 off, u32x4 v) { __builtin_amdgcn_raw_buffer_store_b128(v, r, off, 0, 16); }
DI void st_sc1_64(rsrc_t r, u32 off, u32x2 v) { __builtin_amdgcn_raw_buffer_store_b64(v, r, off, 0, 16); }
DI void set_flag(rsrc_t rs, u32 off, u32 tag, int lane) {
    if (lane < 8) st_sc1(rs, off + lane * 16, (u32x4){tag, tag, tag, tag});
}
DI void wait_flags(char* ws, u32 off, u32 tag) {
    for (int it = 0; it < SPIN_MAX; ++it) {
        u32 v = __hip_atomic_load((GAS u32*)(ws + off), __ATOMIC_RELAXED, __HIP_MEMORY_SCOPE_AGENT);
        if (__all(v == tag)) break;
#define PSLEEP 1
        if (PSLEEP) __builtin_amdgcn_s_sleep(PSLEEP);
    }
    asm volatile("" ::: "memory");
}

#define STAMP_DECL do {} while (0)
#define STAMP(I) do {} while (0)
#define STAMP_FLUSH() do {} while (0)

__global__ __launch_bounds__(512, 2) __attribute__((amdgpu_waves_per_eu(2, 2)))
void fused_kernel(const u16* __restrict__ x, const u16* __restrict__ wqkv, const u16* __restrict__ wo, const u16* __restrict__ w1,
                  const u16* __restrict__ b1, const u16* __restrict__ w2, const u16* __restrict__ b2, u16* __restrict__ out,
                  char* __restrict__ ws) {
    const int b = blockIdx.x, xcd = b & 7, loc = b >> 3;
    const int tid = threadIdx.x, w = __builtin_amdgcn_readfirstlane(tid >> 6), l = tid & 63, t = l & 15, g = l >> 4;
    const int q4 = t >> 2, p4 = l & 3;
    const rsrc_t rs = __builtin_amdgcn_make_buffer_rsrc(ws, 0, WS_END, 0x00020000);
    STAMP_DECL;
    STAMP(0);
    const int RB = 4 * (xcd >> 1) + (loc >> 3), h = 8 * (xcd & 1) + (loc & 7);
    const bool odd = h & 1;
    const int tp = w >> 1, ch = w & 1;
    {
        const char* xb = (const char*)x + (size_t)(128 * RB + 16 * w) * 896;
        const u32 xv = (l >> 2) * 896 + (((l & 3) ^ ((-(l >> 4)) & 3)) * 16);
        if (w < 7) {
            glds16_run<14>(xb, xv, w * 14336, 64, 1024);
            glds16_run<2>((const char*)x + (size_t)(128 * RB + 112) * 896 + w * 128, xv, 7 * 14336 + w * 2048, 64, 1024);
            asm volatile("s_barrier" ::: "memory");
            if (w < 6) {
                const char* wqb = (const char*)wqkv + 56 * h - (h & 1) * 8;
                const u32 wqv = (l >> 2) * 2688 + (((l & 3) ^ ((l >> 5) << 1)) * 16);
#pragma unroll
                for (int hf = 0; hf < 2; ++hf) glds16_run<3>(wqb + (32 * w + 16 * hf) * 2688, wqv, L_WA + w * 6144 + hf * 1024, 896, 2048);
            } else {
                WAIT_VM(0);
            }
            WAIT_VM(6);
        } else {
            u32 qd[2];
#pragma unroll
            for (int i = 0; i < 2; ++i) {
                const int q = 2 * b + i < 384 ? 2 * b + i : 383, qh = q / 24, r = q - 24 * qh, qs = r / 3, qm = r - 3 * qs;
                glds16_run<2>((const char*)wqkv + (size_t)(32 * (6 + qs)) * 2688 + qm * 896 + 56 * qh - (qh & 1) * 8, (l >> 2) * 2688 + (l & 3) * 16, L_RT + i * 2048, 16 * 2688, 1024);
                qd[i] = (u32)q;
            }
            asm volatile("s_mov_b32 m0, %2\n\ts_nop 0\n\tglobal_load_lds_dword %0, %1 sc1" :: "v"(0), "s"(ws + WS_GEN), "s"(L_GEN) : "memory");
            {
                unsigned long long pc; u32 span;
                asm volatile("s_getpc_b64 %0\n.Lfused_pc_here:\n\ts_mov_b32 %1, fused_code_end_marker-.Lfused_pc_here" : "=s"(pc), "=s"(span));
                const u32 off = (u32)((loc & 3) * 64 + l) * 128u + (u32)(loc >> 2 & 1) * 32768u;
                asm volatile("s_mov_b32 m0, %2\n\ts_nop 0\n\tglobal_load_lds_dword %0, %1" :: "v"(off + 4u <= span ? off : 0u), "s"(pc), "s"(L_GEN + 256) : "memory");
            }
            WAIT_VM(1);
            const u32 tg = __builtin_amdgcn_readfirstlane(*(LAS u32*)(uintptr_t)L_GEN) + 1u;
            if (2 * b < 384) {
#pragma unroll
                for (int i = 0; i < 2; ++i) {
                    const int q = (int)qd[i], qh = q / 24, r = q - 24 * qh, qs = r / 3, qm = r - 3 * qs;
#pragma unroll
                    for (int hf = 0; hf < 2; ++hf) {
                        const u32 a = L_RT + i * 2048 + (8 * g + q4) * 64 + hf * 32 + 8 * p4;
                        u32x4 f = trfrag(a, a + 256);
                        if (qm < 2 && ((qh & 1) ? (hf == 0 && t < 4) : (hf == 1 && t >= 12))) f = (u32x4){0x3f803f80u, 0x3f803f80u, 0x3f803f80u, 0x3f803f80u};
                        st_sc1(rs, WS_WQF + (u32)((qh * 8 + qs) * 6 + qm * 2 + hf) * 1024 + l * 16, f);
                    }
                }
            }
            asm volatile("s_barrier" ::: "memory");
            if (2 * b < 384) {
                WAIT_VM(0);
                if (l < 16) st_sc1(rs, WS_FQ + (u32)(2 * b) * 128 + l * 16, (u32x4){tg, tg, tg, tg});
            }
        }
    }
    const int pp = b + 256 * (w >> 2);
    const bool ppok = pp < 224 ? (pp & 31) < 28 : pp < 448 ? (pp & 31) < 28 : pp < 504;
    const char* ppsrc; u32 pppitch, ppdst, ppfl;
    {
        const int pe = ppok ? pp : 0, c = w & 3;
        if (pe < 224) { const int rc = pe >> 5, cl = pe & 31; ppsrc = (const char*)w1 + cl * 128 + (size_t)rc * (64 * 3584); pppitch = 3584;
                        ppdst = WS_W1F + (u32)(((cl >> 1) * 8 + (cl & 1) * 4 + c) * 14 + 2 * rc) * 1024; ppfl = (u32)(cl * 7 + rc); }
        else if (pe < 448) { const int g2 = (pe - 224) >> 5, rc = pe & 31; ppsrc = (const char*)w2 + g2 * 128 + (size_t)rc * (64 * 896); pppitch = 896;
                             ppdst = WS_W2F + (u32)((g2 * 4 + c) * 56 + 2 * rc) * 1024; ppfl = (u32)(196 + g2 * 28 + rc); }
        else { const int i = pe - 448; ppsrc = (const char*)wo + (i >> 3) * 128 + (size_t)(i & 7) * (56 * 896); pppitch = 896;
               ppdst = WS_WOF + (u32)(((i >> 3) * 4 + c) * 16 + 2 * (i & 7)) * 1024; ppfl = (u32)(392 + i); }
    }
    const bool ppshort = pp >= 448 && (w & 3) == 3;
    const u32 ppv = (l >> 3) * pppitch + (l & 7) * 16;
    u32 ppg0 = (u32)(16 * (w & 3)) * pppitch, ppg1 = ppshort ? ppg0 : ppg0 + 8 * pppitch;
    u32 ppl0 = L_PP + (w >> 2) * 8192 + (w & 3) * 2048, ppl1 = ppshort ? ppl0 : ppl0 + 1024;
    const u32 ppa = L_PP + (w >> 2) * 8192 + (w & 3) * 32 + 8 * p4 + (pp < 448 ? (8 * g + q4) * 128 : (4 * g + q4) * 128);
    const u32 ppk = pp < 448 ? 4096 : 3072, pph = pp < 448 ? 512 : 2048;
    PIN_S(ppg0); PIN_S(ppg1); PIN_S(ppl0); PIN_S(ppl1); PIN_S(ppdst); PIN_S(ppfl);
    BAR();
    STAMP(1);
    u32x4 xf[2][14];
    {
        const u32 xl = t * 64 + ((g ^ ((-(t >> 2)) & 3)) * 16);
        const u32 xa = w * 14336 + xl, xb2 = (w ^ 1) * 14336 + xl;
#pragma unroll
        for (int s = 0; s < 14; ++s) { xf[0][s] = lds_ld128(xa + s * 1024); xf[1][s] = lds_ld128(xb2 + s * 1024); }
    }
    const u32 tag = __builtin_amdgcn_readfirstlane(*(LAS u32*)(uintptr_t)L_GEN) + 1u;
    WAIT_VM(0);
    if (w < 6) lds_st64(L_WA + w * 6144 + (l >> 5) * 2048 + (l & 31) * 64 + ((((odd ? 0 : 1) ^ ((l >> 3) & 1)) * 32) + (odd ? 0 : 24)), (u32x2){0x3f803f80u, 0x3f803f80u});
    if (w == 7) wait_flags(ws, WS_FQ + (u32)(h * 24 + (l < 24 ? l : 0)) * 128, tag);
    BAR();
    STAMP(2);
    {
        u32 wv = l * 16 + w * 1024 - 8192;
        dma_seek(w * 1024 - 8192);
#pragma unroll
        for (int j = 0; j < 6; ++j) dma_next_sc1<8192, 8192>(ws + WS_WQF + (size_t)(h * 48) * 1024, wv);
    }
    SB();
    {
        f32x4 acc[2][3], cs[3];
        u32 wb1[3] = {(u32)(2 * ch) * 1024 + l * 16, (u32)(2 * ch + 1) * 1024 + l * 16, (u32)(4 + ch) * 1024 + l * 16};
        u32 wb0[3];
        {
            const u32 h0 = (8 * g + q4) * 64 + ((g & 1) * 32) + p4 * 8, h1 = (8 * g + q4) * 64 + (((g & 1) ^ 1) * 32) + p4 * 8;
            wb0[0] = L_WA + h0 + ch * 2048; wb0[1] = L_WA + h1 + ch * 2048; wb0[2] = L_WA + (ch ? h1 : h0) + 4096;
        }
        OPAQUE(wb0[0]); OPAQUE(wb0[1]); OPAQUE(wb0[2]); OPAQUE(wb1[0]); OPAQUE(wb1[1]); OPAQUE(wb1[2]);
#define WQ_FRAG(s, c) ((s) < 6 ? trfrag(wb0[c] + (s) * 6144, wb0[c] + (s) * 6144 + 256) : lds_ld128(wb1[c] + ((s) - 6) * 6144))
        u32x4 ones = {0x3f803f80u, 0x3f803f80u, 0x3f803f80u, 0x3f803f80u};
        OPAQUE(ones);
        f32x4 gram;
        u32x4 wf[2][3];
#pragma unroll
        for (int c = 0; c < 3; ++c) wf[0][c] = WQ_FRAG(0, c);
#pragma unroll
        for (int c = 0; c < 3; ++c) { cs[c] = (f32x4){0.f, 0.f, 0.f, 0.f}; OPAQUE(cs[c]); }
        asm volatile("s_nop 1");
        SB();
#pragma unroll
        for (int s = 0; s < 14; ++s) {
            if (s == 5) { wait_vm<3>(); BAR0(); }
            if (s == 9) { wait_vm<2>(); BAR0(); }
            if (s == 11) { wait_vm<0>(); BAR(); }
            if (s == 6) glds16_one(ppsrc, ppv, ppg0, ppl0);
            if (s == 7) glds16_one(ppsrc, ppv, ppg1, ppl1);
#pragma unroll
            for (int i = 0; i < 6; ++i) {
                const int c = i >> 1, tt = i & 1;
                if (c < 2) { if (s == 0) mfma_first(acc[tt][c], wf[s & 1][c], xf[tt][s]); else mfma_acc(acc[tt][c], wf[s & 1][c], xf[tt][s]); }
                else { if (s == 0) mfma_first(acc[tt][c], xf[tt][s], wf[s & 1][c]); else mfma_acc(acc[tt][c], xf[tt][s], wf[s & 1][c]); }
                if (s + 1 < 14 && tt == 0) wf[(s + 1) & 1][c] = WQ_FRAG(s + 1, c);
                if (i == 5) { if (s == 0) mfma_first(gram, xf[0][s], xf[0][s]); else mfma_acc(gram, xf[0][s], xf[0][s]); }
                SB();
            }
            if ((s == 12 || s == 13) && ppok)
                st_sc1(rs, ppdst + (s - 12) * 1024 + l * 16, trfrag(ppa + (s - 12) * ppk, ppa + (s - 12) * ppk + pph));
            if ((s & 3) == tp) {
                mfma_acc(cs[0], wf[s & 1][0], ones); mfma_acc(cs[1], wf[s & 1][1], ones); mfma_acc(cs[2], ones, wf[s & 1][2]);
            }
            SB();
        }
        asm volatile("s_nop 7\n\ts_nop 1" : "+v"(acc[0][0]), "+v"(acc[0][1]), "+v"(acc[0][2]), "+v"(acc[1][0]), "+v"(acc[1][1]), "+v"(acc[1][2]), "+v"(gram),
                     "+v"(cs[0]), "+v"(cs[1]), "+v"(cs[2]));
        STAMP(3);
        {
            const float sum = rows_add(g == (odd ? 0 : 3) ? (odd ? acc[0][0][0] : acc[0][1][0]) : 0.f);
            const float dg = (t & 2) ? ((t & 1) ? gram[3] : gram[2]) : ((t & 1) ? gram[1] : gram[0]);
            const float sq = rows_add(g == (t >> 2) ? dg : 0.f);
            const float mean = sum * (1.f / 448.f);
            const float rstd = rsqrtf(fmaxf(sq * (1.f / 448.f) - mean * mean, 0.f) + 1e-5f);
            if (g == 0) lds_st64(L_ST1 + (16 * w + t) * 8, (u32x2){__float_as_uint(rstd), __float_as_uint(-mean * rstd)});
            if (t == 0) { lds_stf4(L_CS1 + ((tp * 6 + ch * 3 + 0) * 16 + 4 * g) * 4, cs[0]); lds_stf4(L_CS1 + ((tp * 6 + ch * 3 + 1) * 16 + 4 * g) * 4, cs[1]); }
            if (g == 0) lds_stf(L_CS1 + ((tp * 6 + ch * 3 + 2) * 16 + t) * 4, cs[2][0]);
        }
        BAR();
        STAMP(4);
        f32x4 cp0[4], cp1[4]; float cp2[4];
        u32x2 stq[2]; u32x4 sa[2], sb[2];
#pragma unroll
        for (int p = 0; p < 4; ++p) {
            cp0[p] = lds_ldf4(L_CS1 + ((p * 6 + ch * 3 + 0) * 16 + 4 * g) * 4);
            cp1[p] = lds_ldf4(L_CS1 + ((p * 6 + ch * 3 + 1) * 16 + 4 * g) * 4);
            cp2[p] = *(LAS float*)(uintptr_t)(L_CS1 + ((p * 6 + ch * 3 + 2) * 16 + t) * 4);
        }
#pragma unroll
        for (int tt = 0; tt < 2; ++tt) {
            const int tile = w ^ tt;
            stq[tt] = *(LAS u32x2*)(uintptr_t)(L_ST1 + (16 * tile + t) * 8);
            sa[tt] = lds_ld128(L_ST1 + (16 * tile + 4 * g) * 8); sb[tt] = lds_ld128(L_ST1 + (16 * tile + 4 * g + 2) * 8);
        }
        SB();
        f32x4 csum[2];
        csum[0] = (cp0[0] + cp0[1]) + (cp0[2] + cp0[3]); csum[1] = (cp1[0] + cp1[1]) + (cp1[2] + cp1[3]);
        const float csv = (cp2[0] + cp2[1]) + (cp2[2] + cp2[3]);
        const float qs = ch ? 1.f : 0.18898223650461362f * 1.4426950408889634f;
        const float m0 = (odd && g == 0) ? 0.f : qs, m1 = (!odd && g == 3) ? 0.f : qs;
        const bool vpad = ch == 0 ? (odd && t < 4) : (!odd && t >= 12);
        const u32 blob = WS_KV + (u32)(RB * 16 + h) * 16384;
        u32x2 vv[2];
#pragma unroll
        for (int tt = 0; tt < 2; ++tt) {
            const int tile = w ^ tt;
            const float rstd = __uint_as_float(stq[tt][0]), mr = __uint_as_float(stq[tt][1]);
            float a[8];
#pragma unroll
            for (int i = 0; i < 4; ++i) { a[i] = fmaf(acc[tt][0][i], rstd, csum[0][i] * mr) * m0; a[4 + i] = fmaf(acc[tt][1][i], rstd, csum[1][i] * mr) * m1; }
            const u32x4 f = {pack2(a[0], a[1]), pack2(a[2], a[3]), pack2(a[4], a[5]), pack2(a[6], a[7])};
            if (ch) { st_sc1(rs, blob + tile * 1024 + l * 16, f); lds_st128(L_K + (16 + tile) * 1024 + l * 16, f); }
            else lds_st128(L_Q + tile * 1024 + l * 16, f);
            const float v0 = fmaf(acc[tt][2][0], __uint_as_float(sa[tt][0]), csv * __uint_as_float(sa[tt][1]));
            const float v1 = fmaf(acc[tt][2][1], __uint_as_float(sa[tt][2]), csv * __uint_as_float(sa[tt][3]));
            const float v2 = fmaf(acc[tt][2][2], __uint_as_float(sb[tt][0]), csv * __uint_as_float(sb[tt][1]));
            const float v3 = fmaf(acc[tt][2][3], __uint_as_float(sb[tt][2]), csv * __uint_as_float(sb[tt][3]));
            vv[tt] = (u32x2){pack2(v0, v1), pack2(v2, v3)};
            if (vpad) vv[tt] = (u32x2){0x3f803f80u, 0x3f803f80u};
        }
        const u32x4 vfr = ch ? (u32x4){vv[1][0], vv[1][1], vv[0][0], vv[0][1]} : (u32x4){vv[0][0], vv[0][1], vv[1][0], vv[1][1]};
        st_sc1(rs, blob + 8192 + (tp * 2 + ch) * 1024 + l * 16, vfr);
        lds_st128(L_V + ((8 + tp) * 2 + ch) * 1024 + l * 16, vfr);
    }
    {
        WAIT_VM(0);
        set_flag(rs, WS_F1 + (u32)(RB * 16 + h) * FGRP + w * FSTRIDE, tag, l);
        BAR();
        if ((w & 3) == 0 && ppok) set_flag(rs, WS_FW + ppfl * 128, tag, l);
    }
    const u32x4 qf = lds_ld128(L_Q + w * 1024 + l * 16);
    STAMP(5);
    SB();
    const int cg4 = 7 * (xcd & 1) + loc % 7, RB4 = 4 * (xcd >> 1) + loc / 7;
    const int rq = loc / 7, cg = loc % 7;
    const int cw = w & 3, kh = w >> 2;
    const bool worker = loc < 28;
    {
        const int rbl = RB - 2 + (l & 1);
        const int rsafe = rbl >= 0 ? rbl : RB;
        wait_flags(ws, WS_F1 + (u32)(rsafe * 16 + h) * FGRP + ((l & 2) ? (w | 1) : w) * FSTRIDE, tag);
        STAMP(6);
#pragma unroll
        for (int blk = 0; blk < 2; ++blk) {
            const int rb = RB - 2 + blk;
            if (rb >= 0) glds16_run_sc1<2>(ws + WS_KV + (size_t)(rb * 16 + h) * 16384 + w * 1024, l * 16, L_K + blk * 8192 + w * 1024, 8192, L_V - L_K);
            else { lds_st128(L_K + blk * 8192 + tid * 16, (u32x4){0, 0, 0, 0}); lds_st128(L_V + blk * 8192 + tid * 16, (u32x4){0, 0, 0, 0}); }
        }
        WAIT_VM(0);
    }
    BAR();
    STAMP(7);
    u32 fwo;
    {
        const int cgs = worker ? cg : 0, cg4s = worker ? cg4 : 0;
        int pi = 392 + cgs * 8 + 4 * kh + (l & 3);
        if (l >= 4 && l < 11) pi = (2 * cg4s + (w >> 2)) * 7 + (l - 4);
        if (l >= 11 && l < 25) pi = 196 + cgs * 28 + 14 * kh + (l - 11);
        fwo = WS_FW + (u32)pi * 128;
    }
    const u32 fwv = __hip_atomic_load((GAS u32*)(ws + fwo), __ATOMIC_RELAXED, __HIP_MEMORY_SCOPE_AGENT);
    SB();
    u32x4 wof[8];
    u32x4 w1f[14];
    const u32 wofa = WS_WOF + (u32)(((worker ? cg : 0) * 4 + cw) * 16 + 8 * kh) * 1024 + l * 16;
    const u32 w1fa = WS_W1F + (u32)(((worker ? cg4 : 0) * 8 + w) * 14) * 1024 + l * 16;
#define PF2(i) do { if ((i) < 8) wof[(i) < 8 ? (i) : 0] = ld_sc1(rs, wofa + (i) * 1024); else w1f[(i) >= 8 ? (i) - 8 : 0] = ld_sc1(rs, w1fa + ((i) - 8) * 1024); } while (0)
    {
        const int base = w & ~1;
        f32x4 sc[18];
        {
            u32x4 kf[18];
#pragma unroll
            for (int a = 0; a < 18; ++a) kf[a] = lds_ld128(L_K + (base + a) * 1024 + l * 16);
            SB();
#pragma unroll
            for (int a = 0; a < 18; ++a) {
                sc[a] = mfma16(kf[a], qf, (f32x4){0.f, 0.f, 0.f, 0.f});
                SB();
            }
        }
        if (!__all(fwv == tag)) wait_flags(ws, fwo, tag);
        asm volatile("" ::: "memory");
        PF2(0); PF2(1);
        SB();
        u32x4 vf[18];
#pragma unroll
        for (int a = 0; a < 8; ++a) vf[a] = lds_ld128(L_V + (base >> 1) * 2048 + a * 1024 + l * 16);
        SB();
        const float NEG = -1e30f;
        const int qq = 256 + 16 * w + t;
#pragma unroll
        for (int a = 0; a < 2; ++a)
#pragma unroll
            for (int i = 0; i < 4; ++i) { const int kk = 16 * (base + a) + 4 * g + i; if (kk <= qq - 256) sc[a][i] = NEG; }
#pragma unroll
        for (int a = 16; a < 18; ++a)
#pragma unroll
            for (int i = 0; i < 4; ++i) { const int kk = 16 * (base + a) + 4 * g + i; if (kk > qq) sc[a][i] = NEG; }
        if (RB < 2) {
            asm volatile("" ::: "memory");
            const int tmin = 8 * (2 - RB);
#pragma unroll
            for (int a = 0; a < 18; ++a) if (base + a < tmin) sc[a] = (f32x4){NEG, NEG, NEG, NEG};
        }
        PF2(2); PF2(3);
        SB();
        float ma = NEG, mb = NEG;
#pragma unroll
        for (int a = 0; a < 18; a += 2) {
            ma = fmaxf(fmaxf(sc[a][0], sc[a][1]), ma); ma = fmaxf(fmaxf(sc[a][2], sc[a][3]), ma);
            mb = fmaxf(fmaxf(sc[a + 1][0], sc[a + 1][1]), mb); mb = fmaxf(fmaxf(sc[a + 1][2], sc[a + 1][3]), mb);
        }
        PF2(4); PF2(5);
        SB();
        float m = fmaxf(ma, mb);
        m = rows_max(m);
        f32x4 o0 = {0.f, 0.f, 0.f, 0.f}, o1 = {0.f, 0.f, 0.f, 0.f};
#pragma unroll
        for (int a = 0; a < 9; ++a) {
            if (2 * a + 8 < 18) {
                vf[2 * a + 8] = lds_ld128(L_V + (base >> 1) * 2048 + (2 * a + 8) * 1024 + l * 16);
                vf[2 * a + 9] = lds_ld128(L_V + (base >> 1) * 2048 + (2 * a + 9) * 1024 + l * 16);
            }
            if (a < 8) { PF2(6 + 2 * a); PF2(7 + 2 * a); }
            u32x4 pf;
            pf[0] = pack2(__builtin_amdgcn_exp2f(sc[2 * a][0] - m), __builtin_amdgcn_exp2f(sc[2 * a][1] - m));
            pf[1] = pack2(__builtin_amdgcn_exp2f(sc[2 * a][2] - m), __builtin_amdgcn_exp2f(sc[2 * a][3] - m));
            pf[2] = pack2(__builtin_amdgcn_exp2f(sc[2 * a + 1][0] - m), __builtin_amdgcn_exp2f(sc[2 * a + 1][1] - m));
            pf[3] = pack2(__builtin_amdgcn_exp2f(sc[2 * a + 1][2] - m), __builtin_amdgcn_exp2f(sc[2 * a + 1][3] - m));
            o0 = mfma16(vf[2 * a], pf, o0);
            o1 = mfma16(vf[2 * a + 1], pf, o1);
            SB();
        }
        const float den = rows_add(g == (odd ? 0 : 3) ? (odd ? o0[0] : o1[0]) : 0.f);
        const float r = 1.f / den;
        const float m0 = (odd && g == 0) ? 0.f : r, m1 = (!odd && g == 3) ? 0.f : r;
        const u32x4 of = {pack2(o0[0] * m0, o0[1] * m0), pack2(o0[2] * m0, o0[3] * m0), pack2(o1[0] * m1, o1[1] * m1), pack2(o1[2] * m1, o1[3] * m1)};
        STAMP(8);
        st_sc1(rs, WS_O + (u32)((RB * 8 + w) * 16 + h) * 1024 + l * 16, of);
        WAIT_VM(0);
        set_flag(rs, WS_F2 + (u32)(RB * 8 + w) * FGRP + h * FSTRIDE, tag, l);
    }
    STAMP(9);
    SB();
    if (!worker) {
        if (b == 255) {
#pragma unroll
            for (int i = 0; i < 4; ++i) wait_flags(ws, WS_F1 + (u32)(w * 32 + i * 8 + (l >> 3)) * FGRP + (l & 7) * FSTRIDE, tag);
            BAR();
            if (w == 0) set_flag(rs, WS_GEN, tag, l);
        }
        STAMP_FLUSH();
        return;
    }
    const int RBW = 2 * xcd + (rq >> 1);
#define TTG(tt) (8 * RBW + 4 * ((tt) >> 1) + 2 * (rq & 1) + ((tt) & 1))
    const int fr = l >> 3, fo8 = l & 7;
    const int TTf = TTG(w >> 1), tl = 8 * (w & 1) + fr;
    const int tok = 16 * TTf + tl;
    const u32x4 xr = *(const u32x4*)((const char*)x + (size_t)tok * 896 + cg * 128 + fo8 * 16);
    SB();
    BAR();
    const int TTc = TTG(cw);
    wait_flags(ws, WS_F2 + (u32)TTc * FGRP + (8 * kh + (l & 7)) * FSTRIDE, tag);
    STAMP(10);
    SB();
    glds16_run_sc1<8>(ws + WS_O + (size_t)(TTc * 16 + 8 * kh) * 1024, l * 16, (cw * 16 + 8 * kh) * 1024, 1024, 1024);
    WAIT_VM(0);
    BAR();
    u32x4 w2f[28];
    const int rot5 = 4 * ((cg + 2 * rq) % 7);
    const u32 w2fa = WS_W2F + (u32)((cg * 4 + cw) * 56 + 28 * kh) * 1024 + l * 16;
#define W2F(i) ld_sc1(rs, w2fa + (u32)((i) + rot5 < 28 ? (i) + rot5 : (i) + rot5 - 28) * 1024)
    u32x4 hres;
    {
        f32x4 acc[4];
        u32x4 af[8];
        const u32 ab = (8 * kh) * 1024 + l * 16;
#pragma unroll
        for (int n = 0; n < 6; ++n) af[n] = lds_ld128(ab + (n & 3) * 16384 + (n >> 2) * 1024);
        SB();
#pragma unroll
        for (int n = 0; n < 32; ++n) {
            if (n < 4) mfma_first(acc[n & 3], wof[n >> 2], af[n & 7]); else mfma_acc(acc[n & 3], wof[n >> 2], af[n & 7]);
            if (n + 6 < 32) af[(n + 6) & 7] = lds_ld128(ab + ((n + 6) & 3) * 16384 + ((n + 6) >> 2) * 1024);
            if ((n & 3) == 1 && (n >> 2) < 7) w2f[n >> 2] = W2F(n >> 2);
            SB();
        }
        MFMA_FENCE4(acc);
        STAMP(11);
#pragma unroll
        for (int tt = 0; tt < 4; ++tt) lds_stf4(L_RED3 + (w * 4 + tt) * 1024 + l * 16, acc[tt]);
        BAR();
        const u32 ra = L_RED3 + (w >> 1) * 1024 + (fo8 >> 1) * 4096 + ((2 * (fo8 & 1)) * 16 + tl) * 16;
        const f32x4 a0 = lds_ldf4(ra) + lds_ldf4(ra + 16384), a1 = lds_ldf4(ra + 256) + lds_ldf4(ra + 16384 + 256);
        hres = (u32x4){pack2(bflo(xr[0]) + a0[0], bfhi(xr[0]) + a0[1]), pack2(bflo(xr[1]) + a0[2], bfhi(xr[1]) + a0[3]),
                       pack2(bflo(xr[2]) + a1[0], bfhi(xr[2]) + a1[1]), pack2(bflo(xr[3]) + a1[2], bfhi(xr[3]) + a1[3])};
        st_sc1(rs, WS_H + (u32)(TTf * 14 + 2 * cg + (fo8 >> 2)) * 1024 + ((fo8 & 3) * 16 + tl) * 16, hres);
        float sum = 0.f, sq = 0.f;
#pragma unroll
        for (int j = 0; j < 4; ++j) { const float a = bflo(hres[j]), c = bfhi(hres[j]); sum += a + c; sq = fmaf(a, a, sq); sq = fmaf(c, c, sq); }
        sum = oct_add(sum); sq = oct_add(sq);
        if (fo8 < 2) st_sc1_64(rs, WS_ST + (u32)((TTf * 7 + cg) * 2 + (w & 1)) * 128 + fo8 * 64 + fr * 8, (u32x2){__float_as_uint(sum), __float_as_uint(sq)});
        WAIT_VM(0);
        set_flag(rs, WS_F3 + (u32)TTf * FGRP + (2 * cg + (w & 1)) * FSTRIDE, tag, l);
    }
    STAMP(12);
    SB();
#pragma unroll
    for (int s = 7; s < 14; ++s) w2f[s] = W2F(s);
    SB();
    f32x4 cs4, bias4;
    {
        const u32x4 ones = {0x3f803f80u, 0x3f803f80u, 0x3f803f80u, 0x3f803f80u};
        f32x4 c4 = {0.f, 0.f, 0.f, 0.f}, c5 = {0.f, 0.f, 0.f, 0.f};
#pragma unroll
        for (int s = 0; s < 14; s += 2) { c4 = mfma16(w1f[s], ones, c4); c5 = mfma16(w1f[s + 1], ones, c5); }
        cs4 = c4 + c5;
        const u32x2 bb = *(const u32x2*)((const char*)b1 + cg4 * 256 + w * 32 + g * 8);
        bias4 = (f32x4){bflo(bb[0]), bfhi(bb[0]), bflo(bb[1]), bfhi(bb[1])};
    }
    BAR();
    STAMP(13);
    wait_flags(ws, WS_F3 + (u32)(8 * RB4 + (w & 3) + ((l & 16) ? 4 : 0)) * FGRP + ((l & 15) < 14 ? (l & 15) : 0) * FSTRIDE, tag);
    STAMP(14);
    SB();
    {
        glds16_run_sc1<1>(ws + WS_ST + (size_t)((8 * RB4 + w) * 14) * 128, (l < 56 ? (l >> 2) : 13) * 128 + (l & 3) * 16, L_ST4 + 1024 + w * 1024, 0, 0);
        const char* hsA = ws + WS_H + (size_t)((8 * RB4 + (w & 3)) * 14 + (w >> 2)) * 1024;
        const char* hsB = hsA + 4 * 14 * 1024;
        u32 hv = l * 16 - 2048;
        dma_seek((w >> 2) * 8192 + (w & 3) * 1024 - 16384);
#pragma unroll
        for (int i = 0; i < (P4DEEP ? 7 : 4); ++i) dma_next_sc1<16384, 2048>(hsA, hv);
        f32x4 acc[8];
        u32x4 af[8];
        u32 ab0 = l * 16, ab1 = l * 16 + 65536;
        OPAQUE(ab1);
#define H4_BLK(n) ((((n) % 56) >> 2) * 8 + ((n) / 56) * 4 + ((n) & 3))
#define H4_ADDR(n) (H4_BLK(n) < 64 ? ab0 + H4_BLK(n) * 1024 : ab1 + (H4_BLK(n) - 64) * 1024)
#define P4_EPILOGUE(M0) do { _Pragma("unroll") for (int m = (M0); m < (M0) + 2; ++m) { \
            u32 a[2][2]; \
            _Pragma("unroll") for (int u = 0; u < 2; ++u) { \
                const int tt = 2 * m + u; \
                const u32x2 st = *(LAS u32x2*)(uintptr_t)(L_ST4 + (16 * tt + t) * 8); \
                const float rstd = __uint_as_float(st[0]), mr = __uint_as_float(st[1]); \
                float v[4]; \
                _Pragma("unroll") for (int i = 0; i < 4; ++i) { float z = fmaf(acc[tt][i], rstd, fmaf(cs4[i], mr, bias4[i])); z = fmaxf(z, 0.f); v[i] = z * z; } \
                a[u][0] = pack2(v[0], v[1]); a[u][1] = pack2(v[2], v[3]); \
            } \
              \
            const auto r0 = __builtin_amdgcn_permlane16_swap(a[0][0], a[1][0], false, false); \
            const auto r1 = __builtin_amdgcn_permlane16_swap(a[0][1], a[1][1], false, false); \
            const u32x4 o = {r0[0], r1[0], r0[1], r1[1]}; \
            const int TTo = 8 * RB4 + 2 * m + (g & 1); \
            st_sc1(rs, WS_HH + (u32)(TTo * 56 + 4 * cg4 + (w >> 1)) * 1024 + ((2 * (w & 1) + (g >> 1)) * 16 + t) * 16, o); \
        } } while (0)
        wait_vm<P4DEEP ? 5 : 2>();
        STAMP(20);
        BAR();
#pragma unroll
        for (int n = 0; n < 6; ++n) af[n] = lds_ld128(H4_ADDR(n));
        SB();
#pragma unroll
        for (int seg = 0; seg < 7; ++seg) {
            constexpr int SB0[8] = {0, 10, 26, 42, 50, 66, 82, 112};
            if (seg == 1) {
                wait_vm<P4DEEP ? 6 : 2>();
                const u32x2 p0 = *(LAS u32x2*)(uintptr_t)(L_ST4 + 1024 + w * 1024 + g * 128 + t * 8);
                const u32x2 p1 = *(LAS u32x2*)(uintptr_t)(L_ST4 + 1024 + w * 1024 + (g + 4) * 128 + t * 8);
                float sum = __uint_as_float(p0[0]), sq = __uint_as_float(p0[1]);
                if (g < 3) { sum += __uint_as_float(p1[0]); sq += __uint_as_float(p1[1]); }
                sum += xor16(sum); sq += xor16(sq); sum += xor32(sum); sq += xor32(sq);
                const float mean = sum * (1.f / 448.f);
                const float rstd = rsqrtf(fmaxf(sq * (1.f / 448.f) - mean * mean, 0.f) + 1e-5f);
                if (g == 0) lds_st64(L_ST4 + (16 * w + t) * 8, (u32x2){__float_as_uint(rstd), __float_as_uint(-mean * rstd)});
                BAR0();
            }
            if (seg == 2) { wait_vm<P4DEEP ? 8 : 4>(); BAR(); }
            if (seg == 3) { wait_vm<7>(); BAR0(); }
            if (seg == 4) { wait_vm<5>(); BAR0(); }
            if (seg == 5) { wait_vm<7>(); BAR0(); }
            if (seg == 6) {
                wait_vm<4>(); BAR0();
                if (w == 0) set_flag(rs, WS_F4 + (RB4 * 2) * FGRP + cg4 * FSTRIDE, tag, l);
            }
#pragma unroll
            for (int n = SB0[seg]; n < SB0[seg + 1]; ++n) {
                if (n == 56) {
                    asm volatile("s_nop 7\n\ts_nop 1" : "+v"(acc[0]), "+v"(acc[1]), "+v"(acc[2]), "+v"(acc[3]));
                    P4_EPILOGUE(0);
                    SB();
                }
                const int ai = (n / 56) * 4 + (n & 3), ks = (n % 56) >> 2;
                if (ks == 0) mfma_first(acc[ai], w1f[ks], af[n & 7]); else mfma_acc(acc[ai], w1f[ks], af[n & 7]);
                if (n + 6 < 112) af[(n + 6) & 7] = lds_ld128(H4_ADDR(n + 6));
                if (!P4DEEP && (n == 4 || n == 8 || n == 12)) dma_next_sc1<16384, 2048>(hsA, hv);
                if (n == (P4DEEP ? 0 : 16)) { hv = l * 16 - 2048; dma_seek((w >> 2) * 8192 + (4 + (w & 3)) * 1024 - 16384); }
                if (n >= (P4DEEP ? 0 : 16) && n <= (P4DEEP ? 24 : 40) && (n & 3) == 0) dma_next_sc1<16384, 2048>(hsB, hv);
                if (n >= 58 && (n - 58) % 6 == 0 && (n - 58) / 6 < 7) w2f[14 + (n - 58) / 6] = W2F(14 + (n - 58) / 6);
                SB();
            }
        }
        asm volatile("s_nop 7\n\ts_nop 1" : "+v"(acc[4]), "+v"(acc[5]), "+v"(acc[6]), "+v"(acc[7]));
        STAMP(15);
        P4_EPILOGUE(2);
        WAIT_VM(0);
        BAR();
        if (w == 0) set_flag(rs, WS_F4 + (RB4 * 2 + 1) * FGRP + cg4 * FSTRIDE, tag, l);
    }
    STAMP(16);
    SB();
#pragma unroll
    for (int s = 21; s < 28; ++s) w2f[s] = W2F(s);
    SB();
    {
        const u32x4 bb = *(const u32x4*)((const char*)b2 + cg * 128 + fo8 * 16);
        const u32 f4o = WS_F4 + (u32)(RBW * 2) * FGRP + (7 * kh + (l < 7 ? l : 0)) * FSTRIDE;
        wait_flags(ws, f4o, tag);
        STAMP(17);
        SB();
        u32 f4b = 0;
        const char* hsA = ws + WS_HH + (size_t)(TTG(cw & 1) * 56 + 28 * kh + (cw >> 1) + rot5) * 1024;
        const char* hsB = ws + WS_HH + (size_t)(TTG(2 + (cw & 1)) * 56 + 28 * kh + (cw >> 1) + rot5) * 1024;
        const int wrap5 = (28 - rot5) >> 1;
#define H5SRC(base, i) ((base) + ((i) < wrap5 ? (i) * 2048 : (i) * 2048 - 28 * 1024))
        const u32 hl = kh * L_H5 + (cw >> 1) * 2048 + (cw & 1) * 1024;
        const u32 hv = l * 16;
        dma_seek(hl - 4096);
#pragma unroll
        for (int i = 0; i < (P5DEEP ? 10 : 6); ++i) dma_next_sc1_b<4096>(H5SRC(hsA, i), hv);
        f32x4 acc[4];
        u32x4 af[8];
        u32 ab0 = kh * L_H5 + l * 16, ab1 = kh * L_H5 + l * 16 + 65536;
        OPAQUE(ab0); OPAQUE(ab1);
#define H5_KS(n) (((n) % 56) >> 1)
#define H5_SLOT(n) ((n) < 56 ? H5_KS(n) : H5_KS(n) < 12 ? 28 + H5_KS(n) : H5_KS(n) - 12)
#define H5_BLK(n) (H5_SLOT(n) * 2 + ((n) & 1))
#define H5_ADDR(n) (H5_BLK(n) < 64 ? ab0 + H5_BLK(n) * 1024 : ab1 + (H5_BLK(n) - 64) * 1024)
        wait_vm<P5DEEP ? 8 : 4>();
        STAMP(21);
        BAR();
#pragma unroll
        for (int n = 0; n < 6; ++n) af[n] = lds_ld128(H5_ADDR(n));
        SB();
#pragma unroll
        for (int seg = 0; seg < 9; ++seg) {
            constexpr int SB0[10] = {0, 2, 10, 26, 42, 50, 66, 82, 98, 112};
            if (P5DEEP) {
                if (seg == 1) {
                    wait_vm<7>(); BAR0();
                    asm volatile("global_load_dword %0, %1, %2 sc1" : "=v"(f4b) : "v"(f4o + FGRP), "s"(ws) : "memory");
                }
                if (seg == 2) {
                    wait_vm<3>();
                    asm volatile("" : "+v"(f4b));
                    if (!__all(f4b == tag)) wait_flags(ws, f4o + FGRP, tag);
                    BAR0();
                }
                if (seg == 3) { wait_vm<7>(); BAR0(); }
                if (seg == 4) { wait_vm<10>(); BAR0(); }
                if (seg == 5) { wait_vm<9>(); BAR0(); }
                if (seg == 6) { wait_vm<6>(); BAR0(); }
                if (seg == 7) { wait_vm<2>(); BAR0(); }
                if (seg == 8) { wait_vm<0>(); BAR0(); }
            } else {
            if (seg == 1) { wait_vm<3>(); BAR0(); }
            if (seg == 2) {
                wait_vm<2>(); BAR0();
                asm volatile("global_load_dword %0, %1, %2 sc1" : "=v"(f4b) : "v"(f4o + FGRP), "s"(ws) : "memory");
            }
            if (seg == 3) {
                wait_vm<2>();
                asm volatile("" : "+v"(f4b));
                if (!__all(f4b == tag)) wait_flags(ws, f4o + FGRP, tag);
                BAR0();
            }
            if (seg == 4) { wait_vm<5>(); BAR0(); }
            if (seg == 5) { wait_vm<4>(); BAR0(); }
            if (seg == 6) { wait_vm<5>(); BAR0(); }
            if (seg == 7) { wait_vm<2>(); BAR0(); }
            if (seg == 8) { wait_vm<0>(); BAR0(); }
            }
#pragma unroll
            for (int n = SB0[seg]; n < SB0[seg + 1]; ++n) {
                const int ai = (n / 56) * 2 + (n & 1);
                if (H5_KS(n) == 0) mfma_first(acc[ai], w2f[H5_KS(n)], af[n & 7]); else mfma_acc(acc[ai], w2f[H5_KS(n)], af[n & 7]);
                if (n + 6 < 112) af[(n + 6) & 7] = lds_ld128(H5_ADDR(n + 6));
                constexpr int NB0 = P5DEEP ? 12 : 28;
                if (n <= (P5DEEP ? 9 : 21) && n % 3 == 0) dma_next_sc1_b<4096>(H5SRC(hsA, (P5DEEP ? 10 : 6) + n / 3), hv);
                if (n == NB0) dma_seek(hl + 28 * 2048 - 4096);
                if (n == NB0 + 18) dma_seek(hl - 4096);
                if (n >= NB0 && n <= NB0 + 39 && (n - NB0) % 3 == 0) dma_next_sc1_b<4096>(H5SRC(hsB, (n - NB0) / 3), hv);
                SB();
            }
        }
        MFMA_FENCE4(acc);
        STAMP(18);
        const u32 rw = (w < 6 ? 32768 + w * 4096 : L_H5 + 32768 + (w - 6) * 4096);
#pragma unroll
        for (int tt = 0; tt < 4; ++tt) lds_stf4(rw + tt * 1024 + l * 16, acc[tt]);
        BAR();
        const int c5 = fo8 >> 1;
        const u32 lp = (w >> 1) * 1024 + ((2 * (fo8 & 1)) * 16 + tl) * 16;
        const u32 r0a = 32768 + c5 * 4096 + lp, r1a = (c5 < 2 ? 32768 + (4 + c5) * 4096 : L_H5 + 32768 + (c5 - 2) * 4096) + lp;
        const f32x4 a0 = lds_ldf4(r0a) + lds_ldf4(r1a), a1 = lds_ldf4(r0a + 256) + lds_ldf4(r1a + 256);
        const u32x4 o = {pack2(a0[0] + bflo(bb[0]) + bflo(hres[0]), a0[1] + bfhi(bb[0]) + bfhi(hres[0])),
                         pack2(a0[2] + bflo(bb[1]) + bflo(hres[1]), a0[3] + bfhi(bb[1]) + bfhi(hres[1])),
                         pack2(a1[0] + bflo(bb[2]) + bflo(hres[2]), a1[1] + bfhi(bb[2]) + bfhi(hres[2])),
                         pack2(a1[2] + bflo(bb[3]) + bflo(hres[3]), a1[3] + bfhi(bb[3]) + bfhi(hres[3]))};
        *(u32x4*)((char*)out + (size_t)tok * 896 + cg * 128 + fo8 * 16) = o;
    }
    STAMP(19);
    STAMP_FLUSH();
    asm volatile("fused_code_end_marker:");
}

extern "C" void kernel_launch(void* const* d_in, const int* in_sizes, int n_in, void* d_out, int out_size, void* d_ws, size_t ws_size,
                              hipStream_t stream) {
    (void)hipFuncSetAttribute((const void*)fused_kernel, hipFuncAttributeMaxDynamicSharedMemorySize, LDS_BYTES);
    fused_kernel<<<256, 512, LDS_BYTES, stream>>>((const u16*)d_in[0], (const u16*)d_in[1], (const u16*)d_in[2], (const u16*)d_in[4],
                                                  (const u16*)d_in[5], (const u16*)d_in[6], (const u16*)d_in[7], (u16*)d_out, (char*)d_ws);
}
```

```cpp
#include <hip/hip_runtime.h>
#include <stdint.h>

typedef unsigned short u16;
typedef unsigned int u32;
typedef __attribute__((ext_vector_type(8))) __bf16 bf16x8;
typedef __attribute__((ext_vector_type(2))) __bf16 bf16x2;
typedef __attribute__((ext_vector_type(4))) short s16x4;
typedef __attribute__((ext_vector_type(4))) float f32x4;
typedef __attribute__((ext_vector_type(2))) float f32x2;
typedef __attribute__((ext_vector_type(4))) u32 u32x4;
typedef __attribute__((ext_vector_type(2))) u32 u32x2;
#define LAS __attribute__((address_space(3)))
#define GAS __attribute__((address_space(1)))
#define DI __device__ __forceinline__

constexpr u32 FSTRIDE = 128;
constexpr u32 FGRP = 16 * FSTRIDE;
constexpr u32 WS_GEN = 0;
constexpr u32 WS_FW = 4096;
constexpr u32 WS_FQ = WS_FW + 448 * 128;
constexpr u32 WS_F1 = WS_FQ + 384 * 128;
constexpr u32 WS_F2 = WS_F1 + 256 * FGRP;
constexpr u32 WS_F3 = WS_F2 + 128 * FGRP;
constexpr u32 WS_F4 = WS_F3 + 128 * FGRP;
constexpr u32 WS_KV = WS_F4 + 32 * FGRP;
constexpr u32 WS_O = WS_KV + 16 * 16 * 16384;
constexpr u32 WS_H = WS_O + 128 * 16 * 1024;
constexpr u32 WS_ST = WS_H + 128 * 14 * 1024;
constexpr u32 WS_HH = WS_ST + 128 * 14 * 128;
constexpr u32 WS_W1F = WS_HH + 128 * 56 * 1024;
constexpr u32 WS_W2F = WS_W1F + 112 * 14 * 1024;
constexpr u32 WS_WOF = WS_W2F + 28 * 56 * 1024;
constexpr u32 WS_WQF = WS_WOF + 28 * 16 * 1024;
constexpr u32 WS_END = WS_WQF + 16 * 48 * 1024;
constexpr u32 L_WA = 114688;
constexpr u32 L_RT = 151552;
constexpr u32 L_GEN = 157696;
constexpr u32 L_ST1 = 157952;
constexpr u32 L_CS1 = 158976;
constexpr u32 L_Q = 49152;
constexpr u32 L_PP = 98304;
constexpr u32 L_K = 0, L_V = 24576;
constexpr u32 L_RED3 = 65536;
constexpr u32 L_ST4 = 114688;
constexpr u32 L_H5 = 81920;
constexpr u32 L_RED5 = 32768;
constexpr u32 LDS_BYTES = 163840;
constexpr int SPIN_MAX = 1 << 14;

DI f32x4 mfma16(u32x4 a, u32x4 b, f32x4 c) {
    return __builtin_amdgcn_mfma_f32_16x16x32_bf16(__builtin_bit_cast(bf16x8, a), __builtin_bit_cast(bf16x8, b), c, 0, 0, 0);
}
DI void mfma_acc(f32x4& acc, const u32x4& a, const u32x4& b) {
    asm volatile("v_mfma_f32_16x16x32_bf16 %0, %1, %2, %0" : "+v"(acc) : "v"(a), "v"(b));
}
DI void mfma_first(f32x4& acc, const u32x4& a, const u32x4& b) {
    asm volatile("v_mfma_f32_16x16x32_bf16 %0, %1, %2, 0" : "=&v"(acc) : "v"(a), "v"(b));
}
#define MFMA_FENCE4(a) asm volatile("s_nop 15\n\ts_nop 15" : "+v"(a[0]), "+v"(a[1]), "+v"(a[2]), "+v"(a[3]))
#define OPAQUE(x) asm volatile("" : "+v"(x))
DI u32 pack2(float lo, float hi) {
    f32x2 v = {lo, hi};
    bf16x2 r = __builtin_convertvector(v, bf16x2);
    return __builtin_bit_cast(u32, r);
}
DI float relu(float z) { float r; asm("v_max_f32 %0, 0, %1" : "=v"(r) : "v"(z)); return r; }
DI float bflo(u32 u) { return __uint_as_float(u << 16); }
DI float bfhi(u32 u) { return __uint_as_float(u & 0xffff0000u); }
DI u32x2 trread(u32 off) {
    s16x4 r = __builtin_amdgcn_ds_read_tr16_b64_v4i16((LAS s16x4*)(uintptr_t)off);
    return __builtin_bit_cast(u32x2, r);
}
DI u32x4 trfrag(u32 off0, u32 off1) {
    u32x2 a = trread(off0), b = trread(off1);
    u32x4 r = {a[0], a[1], b[0], b[1]};
    return r;
}
DI u32x4 lds_ld128(u32 off) { return *(LAS u32x4*)(uintptr_t)off; }
DI f32x4 lds_ldf4(u32 off) { return *(LAS f32x4*)(uintptr_t)off; }
DI void lds_st128(u32 off, u32x4 v) { *(LAS u32x4*)(uintptr_t)off = v; }
DI void lds_stf4(u32 off, f32x4 v) { *(LAS f32x4*)(uintptr_t)off = v; }
DI void lds_st64(u32 off, u32x2 v) { *(LAS u32x2*)(uintptr_t)off = v; }
DI void lds_stf(u32 off, float v) { *(LAS float*)(uintptr_t)off = v; }
template <int N>
DI void glds16_run(const void* sbase, u32 voff, u32 lds, u32 gstep, u32 lstep) {
    u32 keep;
    asm volatile("s_mov_b32 %0, m0\n\ts_mov_b32 m0, %3\n\t"
                 ".rept %c6\n\t"
                 "s_nop 0\n\t"
                 "global_load_lds_dwordx4 %1, %2\n\t"
                 "s_add_u32 m0, m0, %5\n\t"
                 "v_add_u32 %1, %4, %1\n\t"
                 ".endr\n\t"
                 "s_mov_b32 m0, %0"
                 : "=&s"(keep), "+v"(voff) : "s"(sbase), "s"(lds), "s"(gstep), "s"(lstep), "i"(N) : "memory", "scc");
}
template <int N>
DI void glds16_run_sc1(const void* sbase, u32 voff, u32 lds, u32 gstep, u32 lstep) {
    u32 keep;
    asm volatile("s_mov_b32 %0, m0\n\ts_mov_b32 m0, %3\n\t"
                 ".rept %c6\n\t"
                 "s_nop 0\n\t"
                 "global_load_lds_dwordx4 %1, %2 sc1\n\t"
                 "s_add_u32 m0, m0, %5\n\t"
                 "v_add_u32 %1, %4, %1\n\t"
                 ".endr\n\t"
                 "s_mov_b32 m0, %0"
                 : "=&s"(keep), "+v"(voff) : "s"(sbase), "s"(lds), "s"(gstep), "s"(lstep), "i"(N) : "memory", "scc");
}
DI void glds16_one(const void* sbase, u32 voff, u32 goff, u32 lds) {
    u32 tmp;
    asm volatile("s_mov_b32 m0, %4\n\tv_add_u32 %0, %3, %1\n\tglobal_load_lds_dwordx4 %0, %2" : "=&v"(tmp) : "v"(voff), "s"(sbase), "s"(goff), "s"(lds) : "memory");
}
DI void dma_seek(u32 lds) { asm volatile("s_mov_b32 m0, %0" :: "s"(lds) : "memory"); }
template <int LSTEP, int GSTEP>
DI void dma_next_sc1(const void* sbase, u32& voff) {
    asm volatile("s_add_u32 m0, m0, %c2\n\tv_add_u32 %0, %c3, %0\n\tglobal_load_lds_dwordx4 %0, %1 sc1" : "+v"(voff) : "s"(sbase), "i"(LSTEP), "i"(GSTEP) : "memory", "scc");
}
template <int LSTEP>
DI void dma_next_sc1_b(const void* sbase, u32 voff) {
    asm volatile("s_add_u32 m0, m0, %c2\n\ts_nop 0\n\tglobal_load_lds_dwordx4 %0, %1 sc1" :: "v"(voff), "s"(sbase), "i"(LSTEP) : "memory", "scc");
}
#define PIN_S(x) asm volatile("" : "+s"(x))
#define SB() __builtin_amdgcn_sched_barrier(0)
#define WAIT_VM(n) asm volatile("s_waitcnt vmcnt(" #n ")" ::: "memory")
template <int N> DI void wait_vm() { asm volatile("s_waitcnt vmcnt(%c0)" :: "i"(N) : "memory"); }
#define BAR0() asm volatile("s_barrier" ::: "memory")
#define BAR() asm volatile("s_waitcnt lgkmcnt(0)\n\ts_barrier" ::: "memory")
DI float rows_add(float v) {
    const u32 a = __float_as_uint(v);
    const auto r = __builtin_amdgcn_permlane16_swap(a, a, false, false);
    const u32 b2 = __float_as_uint(__uint_as_float(r[0]) + __uint_as_float(r[1]));
    const auto q = __builtin_amdgcn_permlane32_swap(b2, b2, false, false);
    return __uint_as_float(q[0]) + __uint_as_float(q[1]);
}
DI float rows_max(float v) {
    const u32 a = __float_as_uint(v);
    const auto r = __builtin_amdgcn_permlane16_swap(a, a, false, false);
    const u32 b2 = __float_as_uint(fmaxf(__uint_as_float(r[0]), __uint_as_float(r[1])));
    const auto q = __builtin_amdgcn_permlane32_swap(b2, b2, false, false);
    return fmaxf(__uint_as_float(q[0]), __uint_as_float(q[1]));
}
DI float oct_add(float v) {
    v += __uint_as_float(__builtin_amdgcn_update_dpp(0, __float_as_uint(v), 0xB1, 0xf, 0xf, true));
    v += __uint_as_float(__builtin_amdgcn_update_dpp(0, __float_as_uint(v), 0x4E, 0xf, 0xf, true));
    v += __uint_as_float(__builtin_amdgcn_update_dpp(0, __float_as_uint(v), 0x141, 0xf, 0xf, true));
    return v;
}

typedef __amdgpu_buffer_rsrc_t rsrc_t;
DI u32x4 ld_sc1(rsrc_t r, u32 off) { return __builtin_amdgcn_raw_buffer_load_b128(r, off, 0, 16); }
DI void st_sc1(rsrc_t r, u32 off, u32x4 v) { __builtin_amdgcn_raw_buffer_store_b128(v, r, off, 0, 16); }
DI void st_sc1_64(rsrc_t r, u32 off, u32x2 v) { __builtin_amdgcn_raw_buffer_store_b64(v, r, off, 0, 16); }
DI void set_flag(rsrc_t rs, u32 off, u32 tag, int lane) {
    if (lane < 8) st_sc1(rs, off + lane * 16, (u32x4){tag, tag, tag, tag});
}
DI void wait_flags(char* ws, u32 off, u32 tag) {
    for (int it = 0; it < SPIN_MAX; ++it) {
        u32 v = __hip_atomic_load((GAS u32*)(ws + off), __ATOMIC_RELAXED, __HIP_MEMORY_SCOPE_AGENT);
        if (__all(v == tag)) break;
        __builtin_amdgcn_s_sleep(1);
    }
    asm volatile("" ::: "memory");
}

__global__ __launch_bounds__(512, 2) __attribute__((amdgpu_waves_per_eu(2, 2)))
void fused_kernel(const u16* __restrict__ x, const u16* __restrict__ wqkv, const u16* __restrict__ wo, const u16* __restrict__ w1,
                  const u16* __restrict__ b1, const u16* __restrict__ w2, const u16* __restrict__ b2, u16* __restrict__ out,
                  char* __restrict__ ws) {
    const int b = blockIdx.x, xcd = b & 7, loc = b >> 3;
    const int tid = threadIdx.x, w = __builtin_amdgcn_readfirstlane(tid >> 6), l = tid & 63, t = l & 15, g = l >> 4;
    const int q4 = t >> 2, p4 = l & 3;
    const rsrc_t rs = __builtin_amdgcn_make_buffer_rsrc(ws, 0, WS_END, 0x00020000);
    const int RB = 4 * (xcd >> 1) + (loc >> 3), h = 8 * (xcd & 1) + (loc & 7);
    const bool odd = h & 1;
    const int tp = w >> 1, ch = w & 1;
    {
        const char* xb = (const char*)x + (size_t)(128 * RB + 16 * w) * 896;
        const u32 xv = (l >> 2) * 896 + (((l & 3) ^ ((-(l >> 4)) & 3)) * 16);
        if (w < 7) {
            glds16_run<14>(xb, xv, w * 14336, 64, 1024);
            glds16_run<2>((const char*)x + (size_t)(128 * RB + 112) * 896 + w * 128, xv, 7 * 14336 + w * 2048, 64, 1024);
            asm volatile("s_barrier" ::: "memory");
            if (w < 6) {
                const char* wqb = (const char*)wqkv + 56 * h - (h & 1) * 8;
                const u32 wqv = (l >> 2) * 2688 + (((l & 3) ^ ((l >> 5) << 1)) * 16);
#pragma unroll
                for (int hf = 0; hf < 2; ++hf) glds16_run<3>(wqb + (32 * w + 16 * hf) * 2688, wqv, L_WA + w * 6144 + hf * 1024, 896, 2048);
            } else {
                WAIT_VM(0);
            }
            WAIT_VM(6);
        } else {
            u32 qd[2];
#pragma unroll
            for (int i = 0; i < 2; ++i) {
                const int q = 2 * b + i < 384 ? 2 * b + i : 383, qh = q / 24, r = q - 24 * qh, qs = r / 3, qm = r - 3 * qs;
                glds16_run<2>((const char*)wqkv + (size_t)(32 * (6 + qs)) * 2688 + qm * 896 + 56 * qh - (qh & 1) * 8, (l >> 2) * 2688 + (l & 3) * 16, L_RT + i * 2048, 16 * 2688, 1024);
                qd[i] = (u32)q;
            }
            asm volatile("s_mov_b32 m0, %2\n\ts_nop 0\n\tglobal_load_lds_dword %0, %1 sc1" :: "v"(0), "s"(ws + WS_GEN), "s"(L_GEN) : "memory");
            {
                unsigned long long pc; u32 span;
                asm volatile("s_getpc_b64 %0\n.Lfused_pc_here:\n\ts_mov_b32 %1, fused_code_end_marker-.Lfused_pc_here" : "=s"(pc), "=s"(span));
                const u32 off = (u32)((loc & 3) * 64 + l) * 128u + (u32)(loc >> 2 & 1) * 32768u;
                asm volatile("s_mov_b32 m0, %2\n\ts_nop 0\n\tglobal_load_lds_dword %0, %1" :: "v"(off + 4u <= span ? off : 0u), "s"(pc), "s"(L_GEN + 256) : "memory");
            }
            WAIT_VM(1);
            const u32 tg = __builtin_amdgcn_readfirstlane(*(LAS u32*)(uintptr_t)L_GEN) + 1u;
            if (2 * b < 384) {
#pragma unroll
                for (int i = 0; i < 2; ++i) {
                    const int q = (int)qd[i], qh = q / 24, r = q - 24 * qh, qs = r / 3, qm = r - 3 * qs;
#pragma unroll
                    for (int hf = 0; hf < 2; ++hf) {
                        const u32 a = L_RT + i * 2048 + (8 * g + q4) * 64 + hf * 32 + 8 * p4;
                        u32x4 f = trfrag(a, a + 256);
                        if (qm < 2 && ((qh & 1) ? (hf == 0 && t < 4) : (hf == 1 && t >= 12))) f = (u32x4){0x3f803f80u, 0x3f803f80u, 0x3f803f80u, 0x3f803f80u};
                        st_sc1(rs, WS_WQF + (u32)((qh * 8 + qs) * 6 + qm * 2 + hf) * 1024 + l * 16, f);
                    }
                }
            }
            asm volatile("s_barrier" ::: "memory");
            if (2 * b < 384) {
                WAIT_VM(0);
                if (l < 16) st_sc1(rs, WS_FQ + (u32)(2 * b) * 128 + l * 16, (u32x4){tg, tg, tg, tg});
            }
        }
    }
    const int pp = b + 256 * (w >> 2);
    const bool ppok = pp < 224 ? (pp & 31) < 28 : pp < 448 ? (pp & 31) < 28 : pp < 504;
    const char* ppsrc; u32 pppitch, ppdst, ppfl;
    {
        const int pe = ppok ? pp : 0, c = w & 3;
        if (pe < 224) { const int rc = pe >> 5, cl = pe & 31; ppsrc = (const char*)w1 + cl * 128 + (size_t)rc * (64 * 3584); pppitch = 3584;
                        ppdst = WS_W1F + (u32)(((cl >> 1) * 8 + (cl & 1) * 4 + c) * 14 + 2 * rc) * 1024; ppfl = (u32)(cl * 7 + rc); }
        else if (pe < 448) { const int g2 = (pe - 224) >> 5, rc = pe & 31; ppsrc = (const char*)w2 + g2 * 128 + (size_t)rc * (64 * 896); pppitch = 896;
                             ppdst = WS_W2F + (u32)((g2 * 4 + c) * 56 + 2 * rc) * 1024; ppfl = (u32)(196 + g2 * 28 + rc); }
        else { const int i = pe - 448; ppsrc = (const char*)wo + (i >> 3) * 128 + (size_t)(i & 7) * (56 * 896); pppitch = 896;
               ppdst = WS_WOF + (u32)(((i >> 3) * 4 + c) * 16 + 2 * (i & 7)) * 1024; ppfl = (u32)(392 + i); }
    }
    const bool ppshort = pp >= 448 && (w & 3) == 3;
    const u32 ppv = (l >> 3) * pppitch + (l & 7) * 16;
    u32 ppg0 = (u32)(16 * (w & 3)) * pppitch, ppg1 = ppshort ? ppg0 : ppg0 + 8 * pppitch;
    u32 ppl0 = L_PP + (w >> 2) * 8192 + (w & 3) * 2048, ppl1 = ppshort ? ppl0 : ppl0 + 1024;
    const u32 ppa = L_PP + (w >> 2) * 8192 + (w & 3) * 32 + 8 * p4 + (pp < 448 ? (8 * g + q4) * 128 : (4 * g + q4) * 128);
    const u32 ppk = pp < 448 ? 4096 : 3072, pph = pp < 448 ? 512 : 2048;
    PIN_S(ppg0); PIN_S(ppg1); PIN_S(ppl0); PIN_S(ppl1); PIN_S(ppdst); PIN_S(ppfl);
    BAR();
    u32x4 xf[2][14];
    {
        const u32 xl = t * 64 + ((g ^ ((-(t >> 2)) & 3)) * 16);
        const u32 xa = w * 14336 + xl, xb2 = (w ^ 1) * 14336 + xl;
#pragma unroll
        for (int s = 0; s < 14; ++s) { xf[0][s] = lds_ld128(xa + s * 1024); xf[1][s] = lds_ld128(xb2 + s * 1024); }
    }
    const u32 tag = __builtin_amdgcn_readfirstlane(*(LAS u32*)(uintptr_t)L_GEN) + 1u;
    WAIT_VM(0);
    if (w < 6) lds_st64(L_WA + w * 6144 + (l >> 5) * 2048 + (l & 31) * 64 + ((((odd ? 0 : 1) ^ ((l >> 3) & 1)) * 32) + (odd ? 0 : 24)), (u32x2){0x3f803f80u, 0x3f803f80u});
    if (w == 7) wait_flags(ws, WS_FQ + (u32)(h * 24 + (l < 24 ? l : 0)) * 128, tag);
    BAR();
    {
        u32 wv = l * 16 + w * 1024 - 8192;
        dma_seek(w * 1024 - 8192);
#pragma unroll
        for (int j = 0; j < 6; ++j) dma_next_sc1<8192, 8192>(ws + WS_WQF + (size_t)(h * 48) * 1024, wv);
    }
    SB();
    {
        f32x4 acc[2][3], cs[3];
        u32 wb1[3] = {(u32)(2 * ch) * 1024 + l * 16, (u32)(2 * ch + 1) * 1024 + l * 16, (u32)(4 + ch) * 1024 + l * 16};
        u32 wb0[3];
        {
            const u32 h0 = (8 * g + q4) * 64 + ((g & 1) * 32) + p4 * 8, h1 = (8 * g + q4) * 64 + (((g & 1) ^ 1) * 32) + p4 * 8;
            wb0[0] = L_WA + h0 + ch * 2048; wb0[1] = L_WA + h1 + ch * 2048; wb0[2] = L_WA + (ch ? h1 : h0) + 4096;
        }
        OPAQUE(wb0[0]); OPAQUE(wb0[1]); OPAQUE(wb0[2]); OPAQUE(wb1[0]); OPAQUE(wb1[1]); OPAQUE(wb1[2]);
#define WQ_FRAG(s, c) ((s) < 6 ? trfrag(wb0[c] + (s) * 6144, wb0[c] + (s) * 6144 + 256) : lds_ld128(wb1[c] + ((s) - 6) * 6144))
        u32x4 ones = {0x3f803f80u, 0x3f803f80u, 0x3f803f80u, 0x3f803f80u};
        OPAQUE(ones);
        f32x4 gram;
        u32x4 wf[2][3];
#pragma unroll
        for (int c = 0; c < 3; ++c) wf[0][c] = WQ_FRAG(0, c);
#pragma unroll
        for (int c = 0; c < 3; ++c) { cs[c] = (f32x4){0.f, 0.f, 0.f, 0.f}; OPAQUE(cs[c]); }
        asm volatile("s_nop 1");
        SB();
#pragma unroll
        for (int s = 0; s < 14; ++s) {
            if (s == 5) { wait_vm<3>(); BAR0(); }
            if (s == 9) { wait_vm<2>(); BAR0(); }
            if (s == 11) { wait_vm<0>(); BAR(); }
            if (s == 6) glds16_one(ppsrc, ppv, ppg0, ppl0);
            if (s == 7) glds16_one(ppsrc, ppv, ppg1, ppl1);
#pragma unroll
            for (int i = 0; i < 6; ++i) {
                const int c = i >> 1, tt = i & 1;
                if (c < 2) { if (s == 0) mfma_first(acc[tt][c], wf[s & 1][c], xf[tt][s]); else mfma_acc(acc[tt][c], wf[s & 1][c], xf[tt][s]); }
                else { if (s == 0) mfma_first(acc[tt][c], xf[tt][s], wf[s & 1][c]); else mfma_acc(acc[tt][c], xf[tt][s], wf[s & 1][c]); }
                if (s + 1 < 14 && tt == 0) wf[(s + 1) & 1][c] = WQ_FRAG(s + 1, c);
                if (i == 5) { if (s == 0) mfma_first(gram, xf[0][s], xf[0][s]); else mfma_acc(gram, xf[0][s], xf[0][s]); }
                SB();
            }
            if ((s == 12 || s == 13) && ppok)
                st_sc1(rs, ppdst + (s - 12) * 1024 + l * 16, trfrag(ppa + (s - 12) * ppk, ppa + (s - 12) * ppk + pph));
            if ((s & 3) == tp) {
                mfma_acc(cs[0], wf[s & 1][0], ones); mfma_acc(cs[1], wf[s & 1][1], ones); mfma_acc(cs[2], ones, wf[s & 1][2]);
            }
            SB();
        }
        asm volatile("s_nop 15\n\ts_nop 15" : "+v"(acc[0][0]), "+v"(acc[0][1]), "+v"(acc[0][2]), "+v"(acc[1][0]), "+v"(acc[1][1]), "+v"(acc[1][2]), "+v"(gram),
                     "+v"(cs[0]), "+v"(cs[1]), "+v"(cs[2]));
        {
            const float sum = rows_add(g == (odd ? 0 : 3) ? (odd ? acc[0][0][0] : acc[0][1][0]) : 0.f);
            const float dg = (t & 2) ? ((t & 1) ? gram[3] : gram[2]) : ((t & 1) ? gram[1] : gram[0]);
            const float sq = rows_add(g == (t >> 2) ? dg : 0.f);
            const float mean = sum * (1.f / 448.f);
            const float rstd = rsqrtf(fmaxf(sq * (1.f / 448.f) - mean * mean, 0.f) + 1e-5f);
            if (g == 0) lds_st64(L_ST1 + (16 * w + t) * 8, (u32x2){__float_as_uint(rstd), __float_as_uint(-mean * rstd)});
            if (t == 0) { lds_stf4(L_CS1 + ((tp * 6 + ch * 3 + 0) * 16 + 4 * g) * 4, cs[0]); lds_stf4(L_CS1 + ((tp * 6 + ch * 3 + 1) * 16 + 4 * g) * 4, cs[1]); }
            if (g == 0) lds_stf(L_CS1 + ((tp * 6 + ch * 3 + 2) * 16 + t) * 4, cs[2][0]);
        }
        BAR();
        f32x4 cp0[4], cp1[4]; float cp2[4];
        u32x2 stq[2]; u32x4 sa[2], sb[2];
#pragma unroll
        for (int p = 0; p < 4; ++p) {
            cp0[p] = lds_ldf4(L_CS1 + ((p * 6 + ch * 3 + 0) * 16 + 4 * g) * 4);
            cp1[p] = lds_ldf4(L_CS1 + ((p * 6 + ch * 3 + 1) * 16 + 4 * g) * 4);
            cp2[p] = *(LAS float*)(uintptr_t)(L_CS1 + ((p * 6 + ch * 3 + 2) * 16 + t) * 4);
        }
#pragma unroll
        for (int tt = 0; tt < 2; ++tt) {
            const int tile = w ^ tt;
            stq[tt] = *(LAS u32x2*)(uintptr_t)(L_ST1 + (16 * tile + t) * 8);
            sa[tt] = lds_ld128(L_ST1 + (16 * tile + 4 * g) * 8); sb[tt] = lds_ld128(L_ST1 + (16 * tile + 4 * g + 2) * 8);
        }
        SB();
        f32x4 csum[2];
        csum[0] = (cp0[0] + cp0[1]) + (cp0[2] + cp0[3]); csum[1] = (cp1[0] + cp1[1]) + (cp1[2] + cp1[3]);
        const float csv = (cp2[0] + cp2[1]) + (cp2[2] + cp2[3]);
        const float qs = ch ? 1.f : 0.18898223650461362f * 1.4426950408889634f;
        const float m0 = (odd && g == 0) ? 0.f : qs, m1 = (!odd && g == 3) ? 0.f : qs;
        const bool vpad = ch == 0 ? (odd && t < 4) : (!odd && t >= 12);
        const u32 blob = WS_KV + (u32)(RB * 16 + h) * 16384;
        u32x2 vv[2];
#pragma unroll
        for (int tt = 0; tt < 2; ++tt) {
            const int tile = w ^ tt;
            const float rstd = __uint_as_float(stq[tt][0]), mr = __uint_as_float(stq[tt][1]);
            float a[8];
#pragma unroll
            for (int i = 0; i < 4; ++i) { a[i] = fmaf(acc[tt][0][i], rstd, csum[0][i] * mr) * m0; a[4 + i] = fmaf(acc[tt][1][i], rstd, csum[1][i] * mr) * m1; }
            const u32x4 f = {pack2(a[0], a[1]), pack2(a[2], a[3]), pack2(a[4], a[5]), pack2(a[6], a[7])};
            if (ch) { st_sc1(rs, blob + tile * 1024 + l * 16, f); lds_st128(L_K + (16 + tile) * 1024 + l * 16, f); }
            else lds_st128(L_Q + tile * 1024 + l * 16, f);
            const float v0 = fmaf(acc[tt][2][0], __uint_as_float(sa[tt][0]), csv * __uint_as_float(sa[tt][1]));
            const float v1 = fmaf(acc[tt][2][1], __uint_as_float(sa[tt][2]), csv * __uint_as_float(sa[tt][3]));
            const float v2 = fmaf(acc[tt][2][2], __uint_as_float(sb[tt][0]), csv * __uint_as_float(sb[tt][1]));
            const float v3 = fmaf(acc[tt][2][3], __uint_as_float(sb[tt][2]), csv * __uint_as_float(sb[tt][3]));
            vv[tt] = (u32x2){pack2(v0, v1), pack2(v2, v3)};
            if (vpad) vv[tt] = (u32x2){0x3f803f80u, 0x3f803f80u};
        }
        const u32x4 vfr = ch ? (u32x4){vv[1][0], vv[1][1], vv[0][0], vv[0][1]} : (u32x4){vv[0][0], vv[0][1], vv[1][0], vv[1][1]};
        st_sc1(rs, blob + 8192 + (tp * 2 + ch) * 1024 + l * 16, vfr);
        lds_st128(L_V + ((8 + tp) * 2 + ch) * 1024 + l * 16, vfr);
    }
    {
        WAIT_VM(0);
        set_flag(rs, WS_F1 + (u32)(RB * 16 + h) * FGRP + w * FSTRIDE, tag, l);
        BAR();
        if ((w & 3) == 0 && ppok) set_flag(rs, WS_FW + ppfl * 128, tag, l);
    }
    const u32x4 qf = lds_ld128(L_Q + w * 1024 + l * 16);
    SB();
    const int cg4 = 7 * (xcd & 1) + loc % 7, RB4 = 4 * (xcd >> 1) + loc / 7;
    const int rq = loc / 7, cg = loc % 7;
    const int cw = w & 3, kh = w >> 2;
    const bool worker = loc < 28;
    {
        const int rbl = RB - 2 + (l & 1);
        const int rsafe = rbl >= 0 ? rbl : RB;
        wait_flags(ws, WS_F1 + (u32)(rsafe * 16 + h) * FGRP + ((l & 2) ? (w | 1) : w) * FSTRIDE, tag);
#pragma unroll
        for (int blk = 0; blk < 2; ++blk) {
            const int rb = RB - 2 + blk;
            if (rb >= 0) glds16_run_sc1<2>(ws + WS_KV + (size_t)(rb * 16 + h) * 16384 + w * 1024, l * 16, L_K + blk * 8192 + w * 1024, 8192, L_V - L_K);
            else { lds_st128(L_K + blk * 8192 + tid * 16, (u32x4){0, 0, 0, 0}); lds_st128(L_V + blk * 8192 + tid * 16, (u32x4){0, 0, 0, 0}); }
        }
        WAIT_VM(0);
    }
    BAR();
    u32 fwo;
    {
        const int cgs = worker ? cg : 0, cg4s = worker ? cg4 : 0;
        int pi = 392 + cgs * 8 + 4 * kh + (l & 3);
        if (l >= 4 && l < 11) pi = (2 * cg4s + (w >> 2)) * 7 + (l - 4);
        if (l >= 11 && l < 25) pi = 196 + cgs * 28 + 14 * kh + (l - 11);
        fwo = WS_FW + (u32)pi * 128;
    }
    const u32 fwv = __hip_atomic_load((GAS u32*)(ws + fwo), __ATOMIC_RELAXED, __HIP_MEMORY_SCOPE_AGENT);
    SB();
    u32x4 wof[8];
    u32x4 w1f[14];
    const u32 wofa = WS_WOF + (u32)(((worker ? cg : 0) * 4 + cw) * 16 + 8 * kh) * 1024 + l * 16;
    const u32 w1fa = WS_W1F + (u32)(((worker ? cg4 : 0) * 8 + w) * 14) * 1024 + l * 16;
#define PF2(i) do { if ((i) < 8) wof[(i) < 8 ? (i) : 0] = ld_sc1(rs, wofa + (i) * 1024); else w1f[(i) >= 8 ? (i) - 8 : 0] = ld_sc1(rs, w1fa + ((i) - 8) * 1024); } while (0)
    {
        const int base = w & ~1;
        f32x4 sc[18];
        {
            u32x4 kf[18];
#pragma unroll
            for (int a = 0; a < 18; ++a) kf[a] = lds_ld128(L_K + (base + a) * 1024 + l * 16);
            SB();
#pragma unroll
            for (int a = 0; a < 18; ++a) {
                sc[a] = mfma16(kf[a], qf, (f32x4){0.f, 0.f, 0.f, 0.f});
                SB();
            }
        }
        if (!__all(fwv == tag)) wait_flags(ws, fwo, tag);
        asm volatile("" ::: "memory");
        PF2(0); PF2(1);
        SB();
        u32x4 vf[18];
#pragma unroll
        for (int a = 0; a < 8; ++a) vf[a] = lds_ld128(L_V + (base >> 1) * 2048 + a * 1024 + l * 16);
        SB();
        const float NEG = -1e30f;
        const int qq = 256 + 16 * w + t;
#pragma unroll
        for (int a = 0; a < 2; ++a)
#pragma unroll
            for (int i = 0; i < 4; ++i) { const int kk = 16 * (base + a) + 4 * g + i; if (kk <= qq - 256) sc[a][i] = NEG; }
#pragma unroll
        for (int a = 16; a < 18; ++a)
#pragma unroll
            for (int i = 0; i < 4; ++i) { const int kk = 16 * (base + a) + 4 * g + i; if (kk > qq) sc[a][i] = NEG; }
        if (RB < 2) {
            asm volatile("" ::: "memory");
            const int tmin = 8 * (2 - RB);
#pragma unroll
            for (int a = 0; a < 18; ++a) if (base + a < tmin) sc[a] = (f32x4){NEG, NEG, NEG, NEG};
        }
        PF2(2); PF2(3);
        SB();
        float ma = NEG, mb = NEG;
#pragma unroll
        for (int a = 0; a < 18; a += 2) {
            ma = fmaxf(fmaxf(sc[a][0], sc[a][1]), ma); ma = fmaxf(fmaxf(sc[a][2], sc[a][3]), ma);
            mb = fmaxf(fmaxf(sc[a + 1][0], sc[a + 1][1]), mb); mb = fmaxf(fmaxf(sc[a + 1][2], sc[a + 1][3]), mb);
        }
        PF2(4); PF2(5);
        SB();
        float m = fmaxf(ma, mb);
        m = rows_max(m);
        f32x4 o0 = {0.f, 0.f, 0.f, 0.f}, o1 = {0.f, 0.f, 0.f, 0.f};
#pragma unroll
        for (int a = 0; a < 9; ++a) {
            if (2 * a + 8 < 18) {
                vf[2 * a + 8] = lds_ld128(L_V + (base >> 1) * 2048 + (2 * a + 8) * 1024 + l * 16);
                vf[2 * a + 9] = lds_ld128(L_V + (base >> 1) * 2048 + (2 * a + 9) * 1024 + l * 16);
            }
            if (a < 8) { PF2(6 + 2 * a); PF2(7 + 2 * a); }
            u32x4 pf;
            pf[0] = pack2(__builtin_amdgcn_exp2f(sc[2 * a][0] - m), __builtin_amdgcn_exp2f(sc[2 * a][1] - m));
            pf[1] = pack2(__builtin_amdgcn_exp2f(sc[2 * a][2] - m), __builtin_amdgcn_exp2f(sc[2 * a][3] - m));
            pf[2] = pack2(__builtin_amdgcn_exp2f(sc[2 * a + 1][0] - m), __builtin_amdgcn_exp2f(sc[2 * a + 1][1] - m));
            pf[3] = pack2(__builtin_amdgcn_exp2f(sc[2 * a + 1][2] - m), __builtin_amdgcn_exp2f(sc[2 * a + 1][3] - m));
            o0 = mfma16(vf[2 * a], pf, o0);
            o1 = mfma16(vf[2 * a + 1], pf, o1);
            SB();
        }
        const float den = rows_add(g == (odd ? 0 : 3) ? (odd ? o0[0] : o1[0]) : 0.f);
        const float r = 1.f / den;
        const float m0 = (odd && g == 0) ? 0.f : r, m1 = (!odd && g == 3) ? 0.f : r;
        const u32x4 of = {pack2(o0[0] * m0, o0[1] * m0), pack2(o0[2] * m0, o0[3] * m0), pack2(o1[0] * m1, o1[1] * m1), pack2(o1[2] * m1, o1[3] * m1)};
        st_sc1(rs, WS_O + (u32)((RB * 8 + w) * 16 + h) * 1024 + l * 16, of);
        WAIT_VM(0);
        set_flag(rs, WS_F2 + (u32)(RB * 8 + w) * FGRP + h * FSTRIDE, tag, l);
    }
    SB();
    if (!worker) {
        if (b == 255) {
#pragma unroll
            for (int i = 0; i < 4; ++i) wait_flags(ws, WS_F1 + (u32)(w * 32 + i * 8 + (l >> 3)) * FGRP + (l & 7) * FSTRIDE, tag);
            BAR();
            if (w == 0) set_flag(rs, WS_GEN, tag, l);
        }
        return;
    }
    const int RBW = 2 * xcd + (rq >> 1);
#define TTG(tt) (8 * RBW + 4 * ((tt) >> 1) + 2 * (rq & 1) + ((tt) & 1))
    const int fr = l >> 3, fo8 = l & 7;
    const int TTf = TTG(w >> 1), tl = 8 * (w & 1) + fr;
    const int tok = 16 * TTf + tl;
    const u32x4 xr = *(const u32x4*)((const char*)x + (size_t)tok * 896 + cg * 128 + fo8 * 16);
    SB();
    BAR();
    const int TTc = TTG(cw);
    wait_flags(ws, WS_F2 + (u32)TTc * FGRP + (8 * kh + (l & 7)) * FSTRIDE, tag);
    SB();
    glds16_run_sc1<8>(ws + WS_O + (size_t)(TTc * 16 + 8 * kh) * 1024, l * 16, (cw * 16 + 8 * kh) * 1024, 1024, 1024);
    WAIT_VM(0);
    BAR();
    u32x4 w2f[28];
    const int rot5 = 4 * ((cg + 2 * rq) % 7);
    const u32 w2fa = WS_W2F + (u32)((cg * 4 + cw) * 56 + 28 * kh) * 1024 + l * 16;
#define W2F(i) ld_sc1(rs, w2fa + (u32)((i) + rot5 < 28 ? (i) + rot5 : (i) + rot5 - 28) * 1024)
    u32x4 hres;
    {
        f32x4 acc[4];
        u32x4 af[8];
        const u32 ab = (8 * kh) * 1024 + l * 16;
#pragma unroll
        for (int n = 0; n < 6; ++n) af[n] = lds_ld128(ab + (n & 3) * 16384 + (n >> 2) * 1024);
        SB();
#pragma unroll
        for (int n = 0; n < 32; ++n) {
            if (n < 4) mfma_first(acc[n & 3], wof[n >> 2], af[n & 7]); else mfma_acc(acc[n & 3], wof[n >> 2], af[n & 7]);
            if (n + 6 < 32) af[(n + 6) & 7] = lds_ld128(ab + ((n + 6) & 3) * 16384 + ((n + 6) >> 2) * 1024);
            if ((n & 3) == 1 && (n >> 2) < 7) w2f[n >> 2] = W2F(n >> 2);
            SB();
        }
        MFMA_FENCE4(acc);
#pragma unroll
        for (int tt = 0; tt < 4; ++tt) lds_stf4(L_RED3 + (w * 4 + tt) * 1024 + l * 16, acc[tt]);
        BAR();
        const u32 ra = L_RED3 + (w >> 1) * 1024 + (fo8 >> 1) * 4096 + ((2 * (fo8 & 1)) * 16 + tl) * 16;
        const f32x4 a0 = lds_ldf4(ra) + lds_ldf4(ra + 16384), a1 = lds_ldf4(ra + 256) + lds_ldf4(ra + 16384 + 256);
        hres = (u32x4){pack2(bflo(xr[0]) + a0[0], bfhi(xr[0]) + a0[1]), pack2(bflo(xr[1]) + a0[2], bfhi(xr[1]) + a0[3]),
                       pack2(bflo(xr[2]) + a1[0], bfhi(xr[2]) + a1[1]), pack2(bflo(xr[3]) + a1[2], bfhi(xr[3]) + a1[3])};
        st_sc1(rs, WS_H + (u32)(TTf * 14 + 2 * cg + (fo8 >> 2)) * 1024 + ((fo8 & 3) * 16 + tl) * 16, hres);
        float sum = 0.f, sq = 0.f;
#pragma unroll
        for (int j = 0; j < 4; ++j) { const float a = bflo(hres[j]), c = bfhi(hres[j]); sum += a + c; sq = fmaf(a, a, sq); sq = fmaf(c, c, sq); }
        sum = oct_add(sum); sq = oct_add(sq);
        if (fo8 < 2) st_sc1_64(rs, WS_ST + (u32)((TTf * 7 + cg) * 2 + (w & 1)) * 128 + fo8 * 64 + fr * 8, (u32x2){__float_as_uint(sum), __float_as_uint(sq)});
        WAIT_VM(0);
        set_flag(rs, WS_F3 + (u32)TTf * FGRP + (2 * cg + (w & 1)) * FSTRIDE, tag, l);
    }
    SB();
#pragma unroll
    for (int s = 7; s < 14; ++s) w2f[s] = W2F(s);
    SB();
    f32x4 cs4, bias4;
    {
        const u32x4 ones = {0x3f803f80u, 0x3f803f80u, 0x3f803f80u, 0x3f803f80u};
        f32x4 c4 = {0.f, 0.f, 0.f, 0.f}, c5 = {0.f, 0.f, 0.f, 0.f};
#pragma unroll
        for (int s = 0; s < 14; s += 2) { c4 = mfma16(w1f[s], ones, c4); c5 = mfma16(w1f[s + 1], ones, c5); }
        cs4 = c4 + c5;
        const u32x2 bb = *(const u32x2*)((const char*)b1 + cg4 * 256 + w * 32 + g * 8);
        bias4 = (f32x4){bflo(bb[0]), bfhi(bb[0]), bflo(bb[1]), bfhi(bb[1])};
    }
    BAR();
    wait_flags(ws, WS_F3 + (u32)(8 * RB4 + (w & 3) + ((l & 16) ? 4 : 0)) * FGRP + ((l & 15) < 14 ? (l & 15) : 0) * FSTRIDE, tag);
    SB();
    {
        glds16_run_sc1<1>(ws + WS_ST + (size_t)((8 * RB4 + w) * 14) * 128, (l < 56 ? (l >> 2) : 13) * 128 + (l & 3) * 16, L_ST4 + 1024 + w * 1024, 0, 0);
        const char* hsA = ws + WS_H + (size_t)((8 * RB4 + (w & 3)) * 14 + (w >> 2)) * 1024;
        const char* hsB = hsA + 4 * 14 * 1024;
        u32 hv = l * 16 - 2048;
        dma_seek((w >> 2) * 8192 + (w & 3) * 1024 - 16384);
#pragma unroll
        for (int i = 0; i < 4; ++i) dma_next_sc1<16384, 2048>(hsA, hv);
        f32x4 acc[8];
        u32x4 af[8];
        u32 ab0 = l * 16, ab1 = l * 16 + 65536;
        OPAQUE(ab1);
#define H4_BLK(n) ((((n) % 56) >> 2) * 8 + ((n) / 56) * 4 + ((n) & 3))
#define H4_ADDR(n) (H4_BLK(n) < 64 ? ab0 + H4_BLK(n) * 1024 : ab1 + (H4_BLK(n) - 64) * 1024)
#define P4_EPILOGUE(M0) do { _Pragma("unroll") for (int m = (M0); m < (M0) + 2; ++m) { \
            u32 a[2][2]; \
            _Pragma("unroll") for (int u = 0; u < 2; ++u) { \
                const int tt = 2 * m + u; \
                const u32x2 st = *(LAS u32x2*)(uintptr_t)(L_ST4 + (16 * tt + t) * 8); \
                const float rstd = __uint_as_float(st[0]), mr = __uint_as_float(st[1]); \
                float v[4]; \
                _Pragma("unroll") for (int i = 0; i < 4; ++i) { float z = fmaf(acc[tt][i], rstd, fmaf(cs4[i], mr, bias4[i])); z = fmaxf(z, 0.f); v[i] = z * z; } \
                a[u][0] = pack2(v[0], v[1]); a[u][1] = pack2(v[2], v[3]); \
            } \
              \
            const auto r0 = __builtin_amdgcn_permlane16_swap(a[0][0], a[1][0], false, false); \
            const auto r1 = __builtin_amdgcn_permlane16_swap(a[0][1], a[1][1], false, false); \
            const u32x4 o = {r0[0], r1[0], r0[1], r1[1]}; \
            const int TTo = 8 * RB4 + 2 * m + (g & 1); \
            st_sc1(rs, WS_HH + (u32)(TTo * 56 + 4 * cg4 + (w >> 1)) * 1024 + ((2 * (w & 1) + (g >> 1)) * 16 + t) * 16, o); \
        } } while (0)
        wait_vm<2>();
        BAR();
#pragma unroll
        for (int n = 0; n < 6; ++n) af[n] = lds_ld128(H4_ADDR(n));
        SB();
#pragma unroll
        for (int seg = 0; seg < 7; ++seg) {
            constexpr int SB0[8] = {0, 10, 26, 42, 50, 66, 82, 112};
            if (seg == 1) {
                wait_vm<2>();
                const u32x2 p0 = *(LAS u32x2*)(uintptr_t)(L_ST4 + 1024 + w * 1024 + g * 128 + t * 8);
                const u32x2 p1 = *(LAS u32x2*)(uintptr_t)(L_ST4 + 1024 + w * 1024 + (g + 4) * 128 + t * 8);
                float sum = __uint_as_float(p0[0]), sq = __uint_as_float(p0[1]);
                if (g < 3) { sum += __uint_as_float(p1[0]); sq += __uint_as_float(p1[1]); }
                sum = rows_add(sum); sq = rows_add(sq);
                const float mean = sum * (1.f / 448.f);
                const float rstd = rsqrtf(fmaxf(sq * (1.f / 448.f) - mean * mean, 0.f) + 1e-5f);
                if (g == 0) lds_st64(L_ST4 + (16 * w + t) * 8, (u32x2){__float_as_uint(rstd), __float_as_uint(-mean * rstd)});
                BAR0();
            }
            if (seg == 2) { wait_vm<4>(); BAR(); }
            if (seg == 3) { wait_vm<7>(); BAR0(); }
            if (seg == 4) { wait_vm<5>(); BAR0(); }
            if (seg == 5) { wait_vm<7>(); BAR0(); }
            if (seg == 6) {
                wait_vm<4>(); BAR0();
                if (w == 0) set_flag(rs, WS_F4 + (RB4 * 2) * FGRP + cg4 * FSTRIDE, tag, l);
            }
#pragma unroll
            for (int n = SB0[seg]; n < SB0[seg + 1]; ++n) {
                if (n == 56) {
                    asm volatile("s_nop 15\n\ts_nop 15" : "+v"(acc[0]), "+v"(acc[1]), "+v"(acc[2]), "+v"(acc[3]));
                    P4_EPILOGUE(0);
                    SB();
                }
                const int ai = (n / 56) * 4 + (n & 3), ks = (n % 56) >> 2;
                if (ks == 0) mfma_first(acc[ai], w1f[ks], af[n & 7]); else mfma_acc(acc[ai], w1f[ks], af[n & 7]);
                if (n + 6 < 112) af[(n + 6) & 7] = lds_ld128(H4_ADDR(n + 6));
                if (n == 4 || n == 8 || n == 12) dma_next_sc1<16384, 2048>(hsA, hv);
                if (n == 16) { hv = l * 16 - 2048; dma_seek((w >> 2) * 8192 + (4 + (w & 3)) * 1024 - 16384); }
                if (n >= 16 && n <= 40 && (n & 3) == 0) dma_next_sc1<16384, 2048>(hsB, hv);
                if (n >= 58 && (n - 58) % 6 == 0 && (n - 58) / 6 < 7) w2f[14 + (n - 58) / 6] = W2F(14 + (n - 58) / 6);
                SB();
            }
        }
        asm volatile("s_nop 15\n\ts_nop 15" : "+v"(acc[4]), "+v"(acc[5]), "+v"(acc[6]), "+v"(acc[7]));
        P4_EPILOGUE(2);
        WAIT_VM(0);
        BAR();
        if (w == 0) set_flag(rs, WS_F4 + (RB4 * 2 + 1) * FGRP + cg4 * FSTRIDE, tag, l);
    }
    SB();
#pragma unroll
    for (int s = 21; s < 28; ++s) w2f[s] = W2F(s);
    SB();
    {
        const u32x4 bb = *(const u32x4*)((const char*)b2 + cg * 128 + fo8 * 16);
        const u32 f4o = WS_F4 + (u32)(RBW * 2) * FGRP + (7 * kh + (l < 7 ? l : 0)) * FSTRIDE;
        wait_flags(ws, f4o, tag);
        SB();
        u32 f4b = 0;
        const char* hsA = ws + WS_HH + (size_t)(TTG(cw & 1) * 56 + 28 * kh + (cw >> 1) + rot5) * 1024;
        const char* hsB = ws + WS_HH + (size_t)(TTG(2 + (cw & 1)) * 56 + 28 * kh + (cw >> 1) + rot5) * 1024;
        const int wrap5 = (28 - rot5) >> 1;
#define H5SRC(base, i) ((base) + ((i) < wrap5 ? (i) * 2048 : (i) * 2048 - 28 * 1024))
        const u32 hl = kh * L_H5 + (cw >> 1) * 2048 + (cw & 1) * 1024;
        const u32 hv = l * 16;
        dma_seek(hl - 4096);
#pragma unroll
        for (int i = 0; i < 6; ++i) dma_next_sc1_b<4096>(H5SRC(hsA, i), hv);
        f32x4 acc[4];
        u32x4 af[8];
        u32 ab0 = kh * L_H5 + l * 16, ab1 = kh * L_H5 + l * 16 + 65536;
        OPAQUE(ab0); OPAQUE(ab1);
#define H5_KS(n) (((n) % 56) >> 1)
#define H5_SLOT(n) ((n) < 56 ? H5_KS(n) : H5_KS(n) < 12 ? 28 + H5_KS(n) : H5_KS(n) - 12)
#define H5_BLK(n) (H5_SLOT(n) * 2 + ((n) & 1))
#define H5_ADDR(n) (H5_BLK(n) < 64 ? ab0 + H5_BLK(n) * 1024 : ab1 + (H5_BLK(n) - 64) * 1024)
        wait_vm<4>();
        BAR();
#pragma unroll
        for (int n = 0; n < 6; ++n) af[n] = lds_ld128(H5_ADDR(n));
        SB();
#pragma unroll
        for (int seg = 0; seg < 9; ++seg) {
            constexpr int SB0[10] = {0, 2, 10, 26, 42, 50, 66, 82, 98, 112};
            if (seg == 1) { wait_vm<3>(); BAR0(); }
            if (seg == 2) {
                wait_vm<2>(); BAR0();
                asm volatile("global_load_dword %0, %1, %2 sc1" : "=v"(f4b) : "v"(f4o + FGRP), "s"(ws) : "memory");
            }
            if (seg == 3) {
                wait_vm<2>();
                asm volatile("" : "+v"(f4b));
                if (!__all(f4b == tag)) wait_flags(ws, f4o + FGRP, tag);
                BAR0();
            }
            if (seg == 4) { wait_vm<5>(); BAR0(); }
            if (seg == 5) { wait_vm<4>(); BAR0(); }
            if (seg == 6) { wait_vm<5>(); BAR0(); }
            if (seg == 7) { wait_vm<2>(); BAR0(); }
            if (seg == 8) { wait_vm<0>(); BAR0(); }
#pragma unroll
            for (int n = SB0[seg]; n < SB0[seg + 1]; ++n) {
                const int ai = (n / 56) * 2 + (n & 1);
                if (H5_KS(n) == 0) mfma_first(acc[ai], w2f[H5_KS(n)], af[n & 7]); else mfma_acc(acc[ai], w2f[H5_KS(n)], af[n & 7]);
                if (n + 6 < 112) af[(n + 6) & 7] = lds_ld128(H5_ADDR(n + 6));
                constexpr int NB0 = 28;
                if (n <= 21 && n % 3 == 0) dma_next_sc1_b<4096>(H5SRC(hsA, 6 + n / 3), hv);
                if (n == NB0) dma_seek(hl + 28 * 2048 - 4096);
                if (n == NB0 + 18) dma_seek(hl - 4096);
                if (n >= NB0 && n <= NB0 + 39 && (n - NB0) % 3 == 0) dma_next_sc1_b<4096>(H5SRC(hsB, (n - NB0) / 3), hv);
                SB();
            }
        }
        MFMA_FENCE4(acc);
        const u32 rw = (w < 6 ? 32768 + w * 4096 : L_H5 + 32768 + (w - 6) * 4096);
#pragma unroll
        for (int tt = 0; tt < 4; ++tt) lds_stf4(rw + tt * 1024 + l * 16, acc[tt]);
        BAR();
        const int c5 = fo8 >> 1;
        const u32 lp = (w >> 1) * 1024 + ((2 * (fo8 & 1)) * 16 + tl) * 16;
        const u32 r0a = 32768 + c5 * 4096 + lp, r1a = (c5 < 2 ? 32768 + (4 + c5) * 4096 : L_H5 + 32768 + (c5 - 2) * 4096) + lp;
        const f32x4 a0 = lds_ldf4(r0a) + lds_ldf4(r1a), a1 = lds_ldf4(r0a + 256) + lds_ldf4(r1a + 256);
        const u32x4 o = {pack2(a0[0] + bflo(bb[0]) + bflo(hres[0]), a0[1] + bfhi(bb[0]) + bfhi(hres[0])),
                         pack2(a0[2] + bflo(bb[1]) + bflo(hres[1]), a0[3] + bfhi(bb[1]) + bfhi(hres[1])),
                         pack2(a1[0] + bflo(bb[2]) + bflo(hres[2]), a1[1] + bfhi(bb[2]) + bfhi(hres[2])),
                         pack2(a1[2] + bflo(bb[3]) + bflo(hres[3]), a1[3] + bfhi(bb[3]) + bfhi(hres[3]))};
        __builtin_amdgcn_raw_buffer_store_b128(o, __builtin_amdgcn_make_buffer_rsrc(out, 0, 2048 * 896, 0x00020000), (u32)tok * 896 + cg * 128 + fo8 * 16, 0, 16);
    }
    asm volatile("fused_code_end_marker:");
}

extern "C" void kernel_launch(void* const* d_in, const int* in_sizes, int n_in, void* d_out, int out_size, void* d_ws, size_t ws_size,
                              hipStream_t stream) {
    (void)hipFuncSetAttribute((const void*)fused_kernel, hipFuncAttributeMaxDynamicSharedMemorySize, LDS_BYTES);
    fused_kernel<<<256, 512, LDS_BYTES, stream>>>((const u16*)d_in[0], (const u16*)d_in[1], (const u16*)d_in[2], (const u16*)d_in[4],
                                                  (const u16*)d_in[5], (const u16*)d_in[6], (const u16*)d_in[7], (u16*)d_out, (char*)d_ws);
}
```

```cpp
#include <hip/hip_runtime.h>
#include <stdint.h>

typedef unsigned short u16;
typedef unsigned int u32;
typedef __attribute__((ext_vector_type(8))) __bf16 bf16x8;
typedef __attribute__((ext_vector_type(2))) __bf16 bf16x2;
typedef __attribute__((ext_vector_type(4))) short s16x4;
typedef __attribute__((ext_vector_type(4))) float f32x4;
typedef __attribute__((ext_vector_type(2))) float f32x2;
typedef __attribute__((ext_vector_type(4))) u32 u32x4;
typedef __attribute__((ext_vector_type(2))) u32 u32x2;
#define LAS __attribute__((address_space(3)))
#define GAS __attribute__((address_space(1)))
#define DI __device__ __forceinline__

constexpr u32 FSTRIDE = 128;
constexpr u32 FGRP = 16 * FSTRIDE;
constexpr u32 WS_GEN = 0;
constexpr u32 WS_FW = 4096;
constexpr u32 WS_FQ = WS_FW + 448 * 128;
constexpr u32 WS_F1 = WS_FQ + 384 * 128;
constexpr u32 WS_F2 = WS_F1 + 256 * FGRP;
constexpr u32 WS_F3 = WS_F2 + 128 * FGRP;
constexpr u32 WS_F4 = WS_F3 + 128 * FGRP;
constexpr u32 WS_KV = WS_F4 + 32 * FGRP;
constexpr u32 WS_O = WS_KV + 16 * 16 * 16384;
constexpr u32 WS_H = WS_O + 128 * 16 * 1024;
constexpr u32 WS_ST = WS_H + 128 * 14 * 1024;
constexpr u32 WS_HH = WS_ST + 128 * 14 * 128;
constexpr u32 WS_W1F = WS_HH + 128 * 56 * 1024;
constexpr u32 WS_W2F = WS_W1F + 112 * 14 * 1024;
constexpr u32 WS_WOF = WS_W2F + 28 * 56 * 1024;
constexpr u32 WS_WQF = WS_WOF + 28 * 16 * 1024;
constexpr u32 WS_END = WS_WQF + 16 * 48 * 1024;
constexpr u32 L_WA = 114688;
constexpr u32 L_RT = 151552;
constexpr u32 L_GEN = 157696;
constexpr u32 L_ST1 = 157952;
constexpr u32 L_CS1 = 158976;
constexpr u32 L_Q = 49152;
constexpr u32 L_PP = 98304;
constexpr u32 L_K = 0, L_V = 24576;
constexpr u32 L_RED3 = 65536;
constexpr u32 L_ST4 = 114688;
constexpr u32 L_H5 = 81920;
constexpr u32 L_RED5 = 32768;
constexpr u32 LDS_BYTES = 163840;
constexpr int SPIN_MAX = 1 << 14;

DI f32x4 mfma16(u32x4 a, u32x4 b, f32x4 c) {
    return __builtin_amdgcn_mfma_f32_16x16x32_bf16(__builtin_bit_cast(bf16x8, a), __builtin_bit_cast(bf16x8, b), c, 0, 0, 0);
}
DI void mfma_acc(f32x4& acc, const u32x4& a, const u32x4& b) {
    asm volatile("v_mfma_f32_16x16x32_bf16 %0, %1, %2, %0" : "+v"(acc) : "v"(a), "v"(b));
}
DI void mfma_first(f32x4& acc, const u32x4& a, const u32x4& b) {
    asm volatile("v_mfma_f32_16x16x32_bf16 %0, %1, %2, 0" : "=&v"(acc) : "v"(a), "v"(b));
}
#define MFMA_FENCE4(a) asm volatile("s_nop 15\n\ts_nop 15" : "+v"(a[0]), "+v"(a[1]), "+v"(a[2]), "+v"(a[3]))
#define OPAQUE(x) asm volatile("" : "+v"(x))
DI u32 pack2(float lo, float hi) {
    f32x2 v = {lo, hi};
    bf16x2 r = __builtin_convertvector(v, bf16x2);
    return __builtin_bit_cast(u32, r);
}
DI float relu(float z) { float r; asm("v_max_f32 %0, 0, %1" : "=v"(r) : "v"(z)); return r; }
DI float bflo(u32 u) { return __uint_as_float(u << 16); }
DI float bfhi(u32 u) { return __uint_as_float(u & 0xffff0000u); }
DI u32x2 trread(u32 off) {
    s16x4 r = __builtin_amdgcn_ds_read_tr16_b64_v4i16((LAS s16x4*)(uintptr_t)off);
    return __builtin_bit_cast(u32x2, r);
}
DI u32x4 trfrag(u32 off0, u32 off1) {
    u32x2 a = trread(off0), b = trread(off1);
    u32x4 r = {a[0], a[1], b[0], b[1]};
    return r;
}
DI u32x4 lds_ld128(u32 off) { return *(LAS u32x4*)(uintptr_t)off; }
DI f32x4 lds_ldf4(u32 off) { return *(LAS f32x4*)(uintptr_t)off; }
DI void lds_st128(u32 off, u32x4 v) { *(LAS u32x4*)(uintptr_t)off = v; }
DI void lds_stf4(u32 off, f32x4 v) { *(LAS f32x4*)(uintptr_t)off = v; }
DI void lds_st64(u32 off, u32x2 v) { *(LAS u32x2*)(uintptr_t)off = v; }
DI void lds_stf(u32 off, float v) { *(LAS float*)(uintptr_t)off = v; }
template <int N>
DI void glds16_run(const void* sbase, u32 voff, u32 lds, u32 gstep, u32 lstep) {
    u32 keep;
    asm volatile("s_mov_b32 %0, m0\n\ts_mov_b32 m0, %3\n\t"
                 ".rept %c6\n\t"
                 "s_nop 0\n\t"
                 "global_load_lds_dwordx4 %1, %2\n\t"
                 "s_add_u32 m0, m0, %5\n\t"
                 "v_add_u32 %1, %4, %1\n\t"
                 ".endr\n\t"
                 "s_mov_b32 m0, %0"
                 : "=&s"(keep), "+v"(voff) : "s"(sbase), "s"(lds), "s"(gstep), "s"(lstep), "i"(N) : "memory", "scc");
}
template <int N>
DI void glds16_run_sc1(const void* sbase, u32 voff, u32 lds, u32 gstep, u32 lstep) {
    u32 keep;
    asm volatile("s_mov_b32 %0, m0\n\ts_mov_b32 m0, %3\n\t"
                 ".rept %c6\n\t"
                 "s_nop 0\n\t"
                 "global_load_lds_dwordx4 %1, %2 sc1\n\t"
                 "s_add_u32 m0, m0, %5\n\t"
                 "v_add_u32 %1, %4, %1\n\t"
                 ".endr\n\t"
                 "s_mov_b32 m0, %0"
                 : "=&s"(keep), "+v"(voff) : "s"(sbase), "s"(lds), "s"(gstep), "s"(lstep), "i"(N) : "memory", "scc");
}
DI void glds16_one(const void* sbase, u32 voff, u32 goff, u32 lds) {
    u32 tmp;
    asm volatile("s_mov_b32 m0, %4\n\tv_add_u32 %0, %3, %1\n\tglobal_load_lds_dwordx4 %0, %2" : "=&v"(tmp) : "v"(voff), "s"(sbase), "s"(goff), "s"(lds) : "memory");
}
DI void dma_seek(u32 lds) { asm volatile("s_mov_b32 m0, %0" :: "s"(lds) : "memory"); }
template <int LSTEP, int GSTEP>
DI void dma_next_sc1(const void* sbase, u32& voff) {
    asm volatile("s_add_u32 m0, m0, %c2\n\tv_add_u32 %0, %c3, %0\n\tglobal_load_lds_dwordx4 %0, %1 sc1" : "+v"(voff) : "s"(sbase), "i"(LSTEP), "i"(GSTEP) : "memory", "scc");
}
template <int LSTEP>
DI void dma_next_sc1_b(const void* sbase, u32 voff) {
    asm volatile("s_add_u32 m0, m0, %c2\n\ts_nop 0\n\tglobal_load_lds_dwordx4 %0, %1 sc1" :: "v"(voff), "s"(sbase), "i"(LSTEP) : "memory", "scc");
}
#define PIN_S(x) asm volatile("" : "+s"(x))
#define SB() __builtin_amdgcn_sched_barrier(0)
#define WAIT_VM(n) asm volatile("s_waitcnt vmcnt(" #n ")" ::: "memory")
template <int N> DI void wait_vm() { asm volatile("s_waitcnt vmcnt(%c0)" :: "i"(N) : "memory"); }
#define BAR0() asm volatile("s_barrier" ::: "memory")
#define BAR() asm volatile("s_waitcnt lgkmcnt(0)\n\ts_barrier" ::: "memory")
DI float rows_add(float v) {
    const u32 a = __float_as_uint(v);
    const auto r = __builtin_amdgcn_permlane16_swap(a, a, false, false);
    const u32 b2 = __float_as_uint(__uint_as_float(r[0]) + __uint_as_float(r[1]));
    const auto q = __builtin_amdgcn_permlane32_swap(b2, b2, false, false);
    return __uint_as_float(q[0]) + __uint_as_float(q[1]);
}
DI float rows_max(float v) {
    const u32 a = __float_as_uint(v);
    const auto r = __builtin_amdgcn_permlane16_swap(a, a, false, false);
    const u32 b2 = __float_as_uint(fmaxf(__uint_as_float(r[0]), __uint_as_float(r[1])));
    const auto q = __builtin_amdgcn_permlane32_swap(b2, b2, false, false);
    return fmaxf(__uint_as_float(q[0]), __uint_as_float(q[1]));
}
DI float oct_add(float v) {
    v += __uint_as_float(__builtin_amdgcn_update_dpp(0, __float_as_uint(v), 0xB1, 0xf, 0xf, true));
    v += __uint_as_float(__builtin_amdgcn_update_dpp(0, __float_as_uint(v), 0x4E, 0xf, 0xf, true));
    v += __uint_as_float(__builtin_amdgcn_update_dpp(0, __float_as_uint(v), 0x141, 0xf, 0xf, true));
    return v;
}

typedef __amdgpu_buffer_rsrc_t rsrc_t;
DI u32x4 ld_sc1(rsrc_t r, u32 off) { return __builtin_amdgcn_raw_buffer_load_b128(r, off, 0, 16); }
DI void st_sc1(rsrc_t r, u32 off, u32x4 v) { __builtin_amdgcn_raw_buffer_store_b128(v, r, off, 0, 16); }
DI void st_sc1_64(rsrc_t r, u32 off, u32x2 v) { __builtin_amdgcn_raw_buffer_store_b64(v, r, off, 0, 16); }
DI void set_flag(rsrc_t rs, u32 off, u32 tag, int lane) {
    if (lane < 8) st_sc1(rs, off + lane * 16, (u32x4){tag, tag, tag, tag});
}
DI void wait_flags(char* ws, u32 off, u32 tag) {
    for (int it = 0; it < SPIN_MAX; ++it) {
        u32 v = __hip_atomic_load((GAS u32*)(ws + off), __ATOMIC_RELAXED, __HIP_MEMORY_SCOPE_AGENT);
        if (__all(v == tag)) break;
        __builtin_amdgcn_s_sleep(1);
    }
    asm volatile("" ::: "memory");
}

__global__ __launch_bounds__(512, 2) __attribute__((amdgpu_waves_per_eu(2, 2)))
void fused_kernel(const u16* __restrict__ x, const u16* __restrict__ wqkv, const u16* __restrict__ wo, const u16* __restrict__ w1,
                  const u16* __restrict__ b1, const u16* __restrict__ w2, const u16* __restrict__ b2, u16* __restrict__ out,
                  char* __restrict__ ws) {
    const int b = blockIdx.x, xcd = b & 7, loc = b >> 3;
    const int tid = threadIdx.x, w = __builtin_amdgcn_readfirstlane(tid >> 6), l = tid & 63, t = l & 15, g = l >> 4;
    const int q4 = t >> 2, p4 = l & 3;
    const rsrc_t rs = __builtin_amdgcn_make_buffer_rsrc(ws, 0, WS_END, 0x00020000);
    const int RB = 4 * (xcd >> 1) + (loc >> 3), h = 8 * (xcd & 1) + (loc & 7);
    const bool odd = h & 1;
    const int tp = w >> 1, ch = w & 1;
    {
        const char* xb = (const char*)x + (size_t)(128 * RB + 16 * w) * 896;
        const u32 xv = (l >> 2) * 896 + (((l & 3) ^ ((-(l >> 4)) & 3)) * 16);
        if (w < 7) {
            glds16_run<14>(xb, xv, w * 14336, 64, 1024);
            glds16_run<2>((const char*)x + (size_t)(128 * RB + 112) * 896 + w * 128, xv, 7 * 14336 + w * 2048, 64, 1024);
            asm volatile("s_barrier" ::: "memory");
            if (w < 6) {
                const char* wqb = (const char*)wqkv + 56 * h - (h & 1) * 8;
                const u32 wqv = (l >> 2) * 2688 + (((l & 3) ^ ((l >> 5) << 1)) * 16);
#pragma unroll
                for (int hf = 0; hf < 2; ++hf) glds16_run<3>(wqb + (32 * w + 16 * hf) * 2688, wqv, L_WA + w * 6144 + hf * 1024, 896, 2048);
            } else {
                WAIT_VM(0);
            }
            WAIT_VM(6);
        } else {
            u32 qd[2];
#pragma unroll
            for (int i = 0; i < 2; ++i) {
                const int q = 2 * b + i < 384 ? 2 * b + i : 383, qh = q / 24, r = q - 24 * qh, qs = r / 3, qm = r - 3 * qs;
                glds16_run<2>((const char*)wqkv + (size_t)(32 * (6 + qs)) * 2688 + qm * 896 + 56 * qh - (qh & 1) * 8, (l >> 2) * 2688 + (l & 3) * 16, L_RT + i * 2048, 16 * 2688, 1024);
                qd[i] = (u32)q;
            }
            asm volatile("s_mov_b32 m0, %2\n\ts_nop 0\n\tglobal_load_lds_dword %0, %1 sc1" :: "v"(0), "s"(ws + WS_GEN), "s"(L_GEN) : "memory");
            WAIT_VM(0);
            const u32 tg = __builtin_amdgcn_readfirstlane(*(LAS u32*)(uintptr_t)L_GEN) + 1u;
            if (2 * b < 384) {
#pragma unroll
                for (int i = 0; i < 2; ++i) {
                    const int q = (int)qd[i], qh = q / 24, r = q - 24 * qh, qs = r / 3, qm = r - 3 * qs;
#pragma unroll
                    for (int hf = 0; hf < 2; ++hf) {
                        const u32 a = L_RT + i * 2048 + (8 * g + q4) * 64 + hf * 32 + 8 * p4;
                        u32x4 f = trfrag(a, a + 256);
                        if (qm < 2 && ((qh & 1) ? (hf == 0 && t < 4) : (hf == 1 && t >= 12))) f = (u32x4){0x3f803f80u, 0x3f803f80u, 0x3f803f80u, 0x3f803f80u};
                        st_sc1(rs, WS_WQF + (u32)((qh * 8 + qs) * 6 + qm * 2 + hf) * 1024 + l * 16, f);
                    }
                }
            }
            asm volatile("s_barrier" ::: "memory");
            if (2 * b < 384) {
                WAIT_VM(0);
                if (l < 16) st_sc1(rs, WS_FQ + (u32)(2 * b) * 128 + l * 16, (u32x4){tg, tg, tg, tg});
            }
        }
    }
    const int pp = b + 256 * (w >> 2);
    const bool ppok = pp < 224 ? (pp & 31) < 28 : pp < 448 ? (pp & 31) < 28 : pp < 504;
    const char* ppsrc; u32 pppitch, ppdst, ppfl;
    {
        const int pe = ppok ? pp : 0, c = w & 3;
        if (pe < 224) { const int rc = pe >> 5, cl = pe & 31; ppsrc = (const char*)w1 + cl * 128 + (size_t)rc * (64 * 3584); pppitch = 3584;
                        ppdst = WS_W1F + (u32)(((cl >> 1) * 8 + (cl & 1) * 4 + c) * 14 + 2 * rc) * 1024; ppfl = (u32)(cl * 7 + rc); }
        else if (pe < 448) { const int g2 = (pe - 224) >> 5, rc = pe & 31; ppsrc = (const char*)w2 + g2 * 128 + (size_t)rc * (64 * 896); pppitch = 896;
                             ppdst = WS_W2F + (u32)((g2 * 4 + c) * 56 + 2 * rc) * 1024; ppfl = (u32)(196 + g2 * 28 + rc); }
        else { const int i = pe - 448; ppsrc = (const char*)wo + (i >> 3) * 128 + (size_t)(i & 7) * (56 * 896); pppitch = 896;
               ppdst = WS_WOF + (u32)(((i >> 3) * 4 + c) * 16 + 2 * (i & 7)) * 1024; ppfl = (u32)(392 + i); }
    }
    const bool ppshort = pp >= 448 && (w & 3) == 3;
    const u32 ppv = (l >> 3) * pppitch + (l & 7) * 16;
    u32 ppg0 = (u32)(16 * (w & 3)) * pppitch, ppg1 = ppshort ? ppg0 : ppg0 + 8 * pppitch;
    u32 ppl0 = L_PP + (w >> 2) * 8192 + (w & 3) * 2048, ppl1 = ppshort ? ppl0 : ppl0 + 1024;
    const u32 ppa = L_PP + (w >> 2) * 8192 + (w & 3) * 32 + 8 * p4 + (pp < 448 ? (8 * g + q4) * 128 : (4 * g + q4) * 128);
    const u32 ppk = pp < 448 ? 4096 : 3072, pph = pp < 448 ? 512 : 2048;
    PIN_S(ppg0); PIN_S(ppg1); PIN_S(ppl0); PIN_S(ppl1); PIN_S(ppdst); PIN_S(ppfl);
    BAR();
    u32x4 xf[2][14];
    {
        const u32 xl = t * 64 + ((g ^ ((-(t >> 2)) & 3)) * 16);
        const u32 xa = w * 14336 + xl, xb2 = (w ^ 1) * 14336 + xl;
#pragma unroll
        for (int s = 0; s < 14; ++s) { xf[0][s] = lds_ld128(xa + s * 1024); xf[1][s] = lds_ld128(xb2 + s * 1024); }
    }
    const u32 tag = __builtin_amdgcn_readfirstlane(*(LAS u32*)(uintptr_t)L_GEN) + 1u;
    WAIT_VM(0);
    if (w < 6) lds_st64(L_WA + w * 6144 + (l >> 5) * 2048 + (l & 31) * 64 + ((((odd ? 0 : 1) ^ ((l >> 3) & 1)) * 32) + (odd ? 0 : 24)), (u32x2){0x3f803f80u, 0x3f803f80u});
    if (w == 7) wait_flags(ws, WS_FQ + (u32)(h * 24 + (l < 24 ? l : 0)) * 128, tag);
    BAR();
    {
        u32 wv = l * 16 + w * 1024 - 8192;
        dma_seek(w * 1024 - 8192);
#pragma unroll
        for (int j = 0; j < 6; ++j) dma_next_sc1<8192, 8192>(ws + WS_WQF + (size_t)(h * 48) * 1024, wv);
    }
    SB();
    {
        f32x4 acc[2][3], cs[3];
        u32 wb1[3] = {(u32)(2 * ch) * 1024 + l * 16, (u32)(2 * ch + 1) * 1024 + l * 16, (u32)(4 + ch) * 1024 + l * 16};
        u32 wb0[3];
        {
            const u32 h0 = (8 * g + q4) * 64 + ((g & 1) * 32) + p4 * 8, h1 = (8 * g + q4) * 64 + (((g & 1) ^ 1) * 32) + p4 * 8;
            wb0[0] = L_WA + h0 + ch * 2048; wb0[1] = L_WA + h1 + ch * 2048; wb0[2] = L_WA + (ch ? h1 : h0) + 4096;
        }
        OPAQUE(wb0[0]); OPAQUE(wb0[1]); OPAQUE(wb0[2]); OPAQUE(wb1[0]); OPAQUE(wb1[1]); OPAQUE(wb1[2]);
#define WQ_FRAG(s, c) ((s) < 6 ? trfrag(wb0[c] + (s) * 6144, wb0[c] + (s) * 6144 + 256) : lds_ld128(wb1[c] + ((s) - 6) * 6144))
        u32x4 ones = {0x3f803f80u, 0x3f803f80u, 0x3f803f80u, 0x3f803f80u};
        OPAQUE(ones);
        f32x4 gram;
        u32x4 wf[2][3];
#pragma unroll
        for (int c = 0; c < 3; ++c) wf[0][c] = WQ_FRAG(0, c);
#pragma unroll
        for (int c = 0; c < 3; ++c) { cs[c] = (f32x4){0.f, 0.f, 0.f, 0.f}; OPAQUE(cs[c]); }
        asm volatile("s_nop 1");
        SB();
#pragma unroll
        for (int s = 0; s < 14; ++s) {
            if (s == 5) { wait_vm<3>(); BAR0(); }
            if (s == 9) { wait_vm<2>(); BAR0(); }
            if (s == 11) { wait_vm<0>(); BAR(); }
            if (s == 6) glds16_one(ppsrc, ppv, ppg0, ppl0);
            if (s == 7) glds16_one(ppsrc, ppv, ppg1, ppl1);
#pragma unroll
            for (int i = 0; i < 6; ++i) {
                const int c = i >> 1, tt = i & 1;
                if (c < 2) { if (s == 0) mfma_first(acc[tt][c], wf[s & 1][c], xf[tt][s]); else mfma_acc(acc[tt][c], wf[s & 1][c], xf[tt][s]); }
                else { if (s == 0) mfma_first(acc[tt][c], xf[tt][s], wf[s & 1][c]); else mfma_acc(acc[tt][c], xf[tt][s], wf[s & 1][c]); }
                if (s + 1 < 14 && tt == 0) wf[(s + 1) & 1][c] = WQ_FRAG(s + 1, c);
                if (i == 5) { if (s == 0) mfma_first(gram, xf[0][s], xf[0][s]); else mfma_acc(gram, xf[0][s], xf[0][s]); }
                SB();
            }
            if ((s == 12 || s == 13) && ppok)
                st_sc1(rs, ppdst + (s - 12) * 1024 + l * 16, trfrag(ppa + (s - 12) * ppk, ppa + (s - 12) * ppk + pph));
            if ((s & 3) == tp) {
                mfma_acc(cs[0], wf[s & 1][0], ones); mfma_acc(cs[1], wf[s & 1][1], ones); mfma_acc(cs[2], ones, wf[s & 1][2]);
            }
            SB();
        }
        asm volatile("s_nop 15\n\ts_nop 15" : "+v"(acc[0][0]), "+v"(acc[0][1]), "+v"(acc[0][2]), "+v"(acc[1][0]), "+v"(acc[1][1]), "+v"(acc[1][2]), "+v"(gram),
                     "+v"(cs[0]), "+v"(cs[1]), "+v"(cs[2]));
        {
            const float sum = rows_add(g == (odd ? 0 : 3) ? (odd ? acc[0][0][0] : acc[0][1][0]) : 0.f);
            const float dg = (t & 2) ? ((t & 1) ? gram[3] : gram[2]) : ((t & 1) ? gram[1] : gram[0]);
            const float sq = rows_add(g == (t >> 2) ? dg : 0.f);
            const float mean = sum * (1.f / 448.f);
            const float rstd = rsqrtf(fmaxf(sq * (1.f / 448.f) - mean * mean, 0.f) + 1e-5f);
            if (g == 0) lds_st64(L_ST1 + (16 * w + t) * 8, (u32x2){__float_as_uint(rstd), __float_as_uint(-mean * rstd)});
            if (t == 0) { lds_stf4(L_CS1 + ((tp * 6 + ch * 3 + 0) * 16 + 4 * g) * 4, cs[0]); lds_stf4(L_CS1 + ((tp * 6 + ch * 3 + 1) * 16 + 4 * g) * 4, cs[1]); }
            if (g == 0) lds_stf(L_CS1 + ((tp * 6 + ch * 3 + 2) * 16 + t) * 4, cs[2][0]);
        }
        BAR();
        f32x4 cp0[4], cp1[4]; float cp2[4];
        u32x2 stq[2]; u32x4 sa[2], sb[2];
#pragma unroll
        for (int p = 0; p < 4; ++p) {
            cp0[p] = lds_ldf4(L_CS1 + ((p * 6 + ch * 3 + 0) * 16 + 4 * g) * 4);
            cp1[p] = lds_ldf4(L_CS1 + ((p * 6 + ch * 3 + 1) * 16 + 4 * g) * 4);
            cp2[p] = *(LAS float*)(uintptr_t)(L_CS1 + ((p * 6 + ch * 3 + 2) * 16 + t) * 4);
        }
#pragma unroll
        for (int tt = 0; tt < 2; ++tt) {
            const int tile = w ^ tt;
            stq[tt] = *(LAS u32x2*)(uintptr_t)(L_ST1 + (16 * tile + t) * 8);
            sa[tt] = lds_ld128(L_ST1 + (16 * tile + 4 * g) * 8); sb[tt] = lds_ld128(L_ST1 + (16 * tile + 4 * g + 2) * 8);
        }
        SB();
        f32x4 csum[2];
        csum[0] = (cp0[0] + cp0[1]) + (cp0[2] + cp0[3]); csum[1] = (cp1[0] + cp1[1]) + (cp1[2] + cp1[3]);
        const float csv = (cp2[0] + cp2[1]) + (cp2[2] + cp2[3]);
        const float qs = ch ? 1.f : 0.18898223650461362f * 1.4426950408889634f;
        const float m0 = (odd && g == 0) ? 0.f : qs, m1 = (!odd && g == 3) ? 0.f : qs;
        const bool vpad = ch == 0 ? (odd && t < 4) : (!odd && t >= 12);
        const u32 blob = WS_KV + (u32)(RB * 16 + h) * 16384;
        u32x2 vv[2];
#pragma unroll
        for (int tt = 0; tt < 2; ++tt) {
            const int tile = w ^ tt;
            const float rstd = __uint_as_float(stq[tt][0]), mr = __uint_as_float(stq[tt][1]);
            float a[8];
#pragma unroll
            for (int i = 0; i < 4; ++i) { a[i] = fmaf(acc[tt][0][i], rstd, csum[0][i] * mr) * m0; a[4 + i] = fmaf(acc[tt][1][i], rstd, csum[1][i] * mr) * m1; }
            const u32x4 f = {pack2(a[0], a[1]), pack2(a[2], a[3]), pack2(a[4], a[5]), pack2(a[6], a[7])};
            if (ch) { st_sc1(rs, blob + tile * 1024 + l * 16, f); lds_st128(L_K + (16 + tile) * 1024 + l * 16, f); }
            else lds_st128(L_Q + tile * 1024 + l * 16, f);
            const float v0 = fmaf(acc[tt][2][0], __uint_as_float(sa[tt][0]), csv * __uint_as_float(sa[tt][1]));
            const float v1 = fmaf(acc[tt][2][1], __uint_as_float(sa[tt][2]), csv * __uint_as_float(sa[tt][3]));
            const float v2 = fmaf(acc[tt][2][2], __uint_as_float(sb[tt][0]), csv * __uint_as_float(sb[tt][1]));
            const float v3 = fmaf(acc[tt][2][3], __uint_as_float(sb[tt][2]), csv * __uint_as_float(sb[tt][3]));
            vv[tt] = (u32x2){pack2(v0, v1), pack2(v2, v3)};
            if (vpad) vv[tt] = (u32x2){0x3f803f80u, 0x3f803f80u};
        }
        const u32x4 vfr = ch ? (u32x4){vv[1][0], vv[1][1], vv[0][0], vv[0][1]} : (u32x4){vv[0][0], vv[0][1], vv[1][0], vv[1][1]};
        st_sc1(rs, blob + 8192 + (tp * 2 + ch) * 1024 + l * 16, vfr);
        lds_st128(L_V + ((8 + tp) * 2 + ch) * 1024 + l * 16, vfr);
    }
    {
        WAIT_VM(0);
        set_flag(rs, WS_F1 + (u32)(RB * 16 + h) * FGRP + w * FSTRIDE, tag, l);
        BAR();
        if ((w & 3) == 0 && ppok) set_flag(rs, WS_FW + ppfl * 128, tag, l);
    }
    const u32x4 qf = lds_ld128(L_Q + w * 1024 + l * 16);
    SB();
    const int cg4 = 7 * (xcd & 1) + loc % 7, RB4 = 4 * (xcd >> 1) + loc / 7;
    const int rq = loc / 7, cg = loc % 7;
    const int cw = w & 3, kh = w >> 2;
    const bool worker = loc < 28;
    {
        const int rbl = RB - 2 + (l & 1);
        const int rsafe = rbl >= 0 ? rbl : RB;
        wait_flags(ws, WS_F1 + (u32)(rsafe * 16 + h) * FGRP + ((l & 2) ? (w | 1) : w) * FSTRIDE, tag);
#pragma unroll
        for (int blk = 0; blk < 2; ++blk) {
            const int rb = RB - 2 + blk;
            if (rb >= 0) glds16_run_sc1<2>(ws + WS_KV + (size_t)(rb * 16 + h) * 16384 + w * 1024, l * 16, L_K + blk * 8192 + w * 1024, 8192, L_V - L_K);
            else { lds_st128(L_K + blk * 8192 + tid * 16, (u32x4){0, 0, 0, 0}); lds_st128(L_V + blk * 8192 + tid * 16, (u32x4){0, 0, 0, 0}); }
        }
        WAIT_VM(0);
    }
    BAR();
    u32 fwo;
    {
        const int cgs = worker ? cg : 0, cg4s = worker ? cg4 : 0;
        int pi = 392 + cgs * 8 + 4 * kh + (l & 3);
        if (l >= 4 && l < 11) pi = (2 * cg4s + (w >> 2)) * 7 + (l - 4);
        if (l >= 11 && l < 25) pi = 196 + cgs * 28 + 14 * kh + (l - 11);
        fwo = WS_FW + (u32)pi * 128;
    }
    const u32 fwv = __hip_atomic_load((GAS u32*)(ws + fwo), __ATOMIC_RELAXED, __HIP_MEMORY_SCOPE_AGENT);
    SB();
    u32x4 wof[8];
    u32x4 w1f[14];
    const u32 wofa = WS_WOF + (u32)(((worker ? cg : 0) * 4 + cw) * 16 + 8 * kh) * 1024 + l * 16;
    const u32 w1fa = WS_W1F + (u32)(((worker ? cg4 : 0) * 8 + w) * 14) * 1024 + l * 16;
#define PF2(i) do { if ((i) < 8) wof[(i) < 8 ? (i) : 0] = ld_sc1(rs, wofa + (i) * 1024); else w1f[(i) >= 8 ? (i) - 8 : 0] = ld_sc1(rs, w1fa + ((i) - 8) * 1024); } while (0)
    {
        const int base = w & ~1;
        f32x4 sc[18];
        {
            u32x4 kf[18];
#pragma unroll
            for (int a = 0; a < 18; ++a) kf[a] = lds_ld128(L_K + (base + a) * 1024 + l * 16);
            SB();
#pragma unroll
            for (int a = 0; a < 18; ++a) {
                sc[a] = mfma16(kf[a], qf, (f32x4){0.f, 0.f, 0.f, 0.f});
                SB();
            }
        }
        if (!__all(fwv == tag)) wait_flags(ws, fwo, tag);
        asm volatile("" ::: "memory");
        PF2(0); PF2(1);
        SB();
        u32x4 vf[18];
#pragma unroll
        for (int a = 0; a < 8; ++a) vf[a] = lds_ld128(L_V + (base >> 1) * 2048 + a * 1024 + l * 16);
        SB();
        const float NEG = -1e30f;
        const int qq = 256 + 16 * w + t;
#pragma unroll
        for (int a = 0; a < 2; ++a)
#pragma unroll
            for (int i = 0; i < 4; ++i) { const int kk = 16 * (base + a) + 4 * g + i; if (kk <= qq - 256) sc[a][i] = NEG; }
#pragma unroll
        for (int a = 16; a < 18; ++a)
#pragma unroll
            for (int i = 0; i < 4; ++i) { const int kk = 16 * (base + a) + 4 * g + i; if (kk > qq) sc[a][i] = NEG; }
        if (RB < 2) {
            asm volatile("" ::: "memory");
            const int tmin = 8 * (2 - RB);
#pragma unroll
            for (int a = 0; a < 18; ++a) if (base + a < tmin) sc[a] = (f32x4){NEG, NEG, NEG, NEG};
        }
        PF2(2); PF2(3);
        SB();
        float ma = NEG, mb = NEG;
#pragma unroll
        for (int a = 0; a < 18; a += 2) {
            ma = fmaxf(fmaxf(sc[a][0], sc[a][1]), ma); ma = fmaxf(fmaxf(sc[a][2], sc[a][3]), ma);
            mb = fmaxf(fmaxf(sc[a + 1][0], sc[a + 1][1]), mb); mb = fmaxf(fmaxf(sc[a + 1][2], sc[a + 1][3]), mb);
        }
        PF2(4); PF2(5);
        SB();
        float m = fmaxf(ma, mb);
        m = rows_max(m);
        f32x4 o0 = {0.f, 0.f, 0.f, 0.f}, o1 = {0.f, 0.f, 0.f, 0.f};
#pragma unroll
        for (int a = 0; a < 9; ++a) {
            if (2 * a + 8 < 18) {
                vf[2 * a + 8] = lds_ld128(L_V + (base >> 1) * 2048 + (2 * a + 8) * 1024 + l * 16);
                vf[2 * a + 9] = lds_ld128(L_V + (base >> 1) * 2048 + (2 * a + 9) * 1024 + l * 16);
            }
            if (a < 8) { PF2(6 + 2 * a); PF2(7 + 2 * a); }
            u32x4 pf;
            pf[0] = pack2(__builtin_amdgcn_exp2f(sc[2 * a][0] - m), __builtin_amdgcn_exp2f(sc[2 * a][1] - m));
            pf[1] = pack2(__builtin_amdgcn_exp2f(sc[2 * a][2] - m), __builtin_amdgcn_exp2f(sc[2 * a][3] - m));
            pf[2] = pack2(__builtin_amdgcn_exp2f(sc[2 * a + 1][0] - m), __builtin_amdgcn_exp2f(sc[2 * a + 1][1] - m));
            pf[3] = pack2(__builtin_amdgcn_exp2f(sc[2 * a + 1][2] - m), __builtin_amdgcn_exp2f(sc[2 * a + 1][3] - m));
            o0 = mfma16(vf[2 * a], pf, o0);
            o1 = mfma16(vf[2 * a + 1], pf, o1);
            SB();
        }
        const float den = rows_add(g == (odd ? 0 : 3) ? (odd ? o0[0] : o1[0]) : 0.f);
        const float r = 1.f / den;
        const float m0 = (odd && g == 0) ? 0.f : r, m1 = (!odd && g == 3) ? 0.f : r;
        const u32x4 of = {pack2(o0[0] * m0, o0[1] * m0), pack2(o0[2] * m0, o0[3] * m0), pack2(o1[0] * m1, o1[1] * m1), pack2(o1[2] * m1, o1[3] * m1)};
        st_sc1(rs, WS_O + (u32)((RB * 8 + w) * 16 + h) * 1024 + l * 16, of);
        WAIT_VM(0);
        set_flag(rs, WS_F2 + (u32)(RB * 8 + w) * FGRP + h * FSTRIDE, tag, l);
    }
    SB();
    if (!worker) {
        if (b == 255) {
#pragma unroll
            for (int i = 0; i < 4; ++i) wait_flags(ws, WS_F1 + (u32)(w * 32 + i * 8 + (l >> 3)) * FGRP + (l & 7) * FSTRIDE, tag);
            BAR();
            if (w == 0) set_flag(rs, WS_GEN, tag, l);
        }
        return;
    }
    const int RBW = 2 * xcd + (rq >> 1);
#define TTG(tt) (8 * RBW + 4 * ((tt) >> 1) + 2 * (rq & 1) + ((tt) & 1))
    const int fr = l >> 3, fo8 = l & 7;
    const int TTf = TTG(w >> 1), tl = 8 * (w & 1) + fr;
    const int tok = 16 * TTf + tl;
    const u32x4 xr = *(const u32x4*)((const char*)x + (size_t)tok * 896 + cg * 128 + fo8 * 16);
    SB();
    BAR();
    const int TTc = TTG(cw);
    wait_flags(ws, WS_F2 + (u32)TTc * FGRP + (8 * kh + (l & 7)) * FSTRIDE, tag);
    SB();
    glds16_run_sc1<8>(ws + WS_O + (size_t)(TTc * 16 + 8 * kh) * 1024, l * 16, (cw * 16 + 8 * kh) * 1024, 1024, 1024);
    WAIT_VM(0);
    BAR();
    u32x4 w2f[28];
    const int rot5 = 4 * ((cg + 2 * rq) % 7);
    const u32 w2fa = WS_W2F + (u32)((cg * 4 + cw) * 56 + 28 * kh) * 1024 + l * 16;
#define W2F(i) ld_sc1(rs, w2fa + (u32)((i) + rot5 < 28 ? (i) + rot5 : (i) + rot5 - 28) * 1024)
    u32x4 hres;
    {
        f32x4 acc[4];
        u32x4 af[8];
        const u32 ab = (8 * kh) * 1024 + l * 16;
#pragma unroll
        for (int n = 0; n < 6; ++n) af[n] = lds_ld128(ab + (n & 3) * 16384 + (n >> 2) * 1024);
        SB();
#pragma unroll
        for (int n = 0; n < 32; ++n) {
            if (n < 4) mfma_first(acc[n & 3], wof[n >> 2], af[n & 7]); else mfma_acc(acc[n & 3], wof[n >> 2], af[n & 7]);
            if (n + 6 < 32) af[(n + 6) & 7] = lds_ld128(ab + ((n + 6) & 3) * 16384 + ((n + 6) >> 2) * 1024);
            if ((n & 3) == 1 && (n >> 2) < 7) w2f[n >> 2] = W2F(n >> 2);
            SB();
        }
        MFMA_FENCE4(acc);
#pragma unroll
        for (int tt = 0; tt < 4; ++tt) lds_stf4(L_RED3 + (w * 4 + tt) * 1024 + l * 16, acc[tt]);
        BAR();
        const u32 ra = L_RED3 + (w >> 1) * 1024 + (fo8 >> 1) * 4096 + ((2 * (fo8 & 1)) * 16 + tl) * 16;
        const f32x4 a0 = lds_ldf4(ra) + lds_ldf4(ra + 16384), a1 = lds_ldf4(ra + 256) + lds_ldf4(ra + 16384 + 256);
        hres = (u32x4){pack2(bflo(xr[0]) + a0[0], bfhi(xr[0]) + a0[1]), pack2(bflo(xr[1]) + a0[2], bfhi(xr[1]) + a0[3]),
                       pack2(bflo(xr[2]) + a1[0], bfhi(xr[2]) + a1[1]), pack2(bflo(xr[3]) + a1[2], bfhi(xr[3]) + a1[3])};
        st_sc1(rs, WS_H + (u32)(TTf * 14 + 2 * cg + (fo8 >> 2)) * 1024 + ((fo8 & 3) * 16 + tl) * 16, hres);
        float sum = 0.f, sq = 0.f;
#pragma unroll
        for (int j = 0; j < 4; ++j) { const float a = bflo(hres[j]), c = bfhi(hres[j]); sum += a + c; sq = fmaf(a, a, sq); sq = fmaf(c, c, sq); }
        sum = oct_add(sum); sq = oct_add(sq);
        if (fo8 < 2) st_sc1_64(rs, WS_ST + (u32)((TTf * 7 + cg) * 2 + (w & 1)) * 128 + fo8 * 64 + fr * 8, (u32x2){__float_as_uint(sum), __float_as_uint(sq)});
        WAIT_VM(0);
        set_flag(rs, WS_F3 + (u32)TTf * FGRP + (2 * cg + (w & 1)) * FSTRIDE, tag, l);
    }
    SB();
#pragma unroll
    for (int s = 7; s < 14; ++s) w2f[s] = W2F(s);
    SB();
    f32x4 cs4, bias4;
    {
        const u32x4 ones = {0x3f803f80u, 0x3f803f80u, 0x3f803f80u, 0x3f803f80u};
        f32x4 c4 = {0.f, 0.f, 0.f, 0.f}, c5 = {0.f, 0.f, 0.f, 0.f};
#pragma unroll
        for (int s = 0; s < 14; s += 2) { c4 = mfma16(w1f[s], ones, c4); c5 = mfma16(w1f[s + 1], ones, c5); }
        cs4 = c4 + c5;
        const u32x2 bb = *(const u32x2*)((const char*)b1 + cg4 * 256 + w * 32 + g * 8);
        bias4 = (f32x4){bflo(bb[0]), bfhi(bb[0]), bflo(bb[1]), bfhi(bb[1])};
    }
    BAR();
    wait_flags(ws, WS_F3 + (u32)(8 * RB4 + (w & 3) + ((l & 16) ? 4 : 0)) * FGRP + ((l & 15) < 14 ? (l & 15) : 0) * FSTRIDE, tag);
    SB();
    {
        glds16_run_sc1<1>(ws + WS_ST + (size_t)((8 * RB4 + w) * 14) * 128, (l < 56 ? (l >> 2) : 13) * 128 + (l & 3) * 16, L_ST4 + 1024 + w * 1024, 0, 0);
        const char* hsA = ws + WS_H + (size_t)((8 * RB4 + (w & 3)) * 14 + (w >> 2)) * 1024;
        const char* hsB = hsA + 4 * 14 * 1024;
        u32 hv = l * 16 - 2048;
        dma_seek((w >> 2) * 8192 + (w & 3) * 1024 - 16384);
#pragma unroll
        for (int i = 0; i < 4; ++i) dma_next_sc1<16384, 2048>(hsA, hv);
        f32x4 acc[8];
        u32x4 af[8];
        u32 ab0 = l * 16, ab1 = l * 16 + 65536;
        OPAQUE(ab1);
#define H4_BLK(n) ((((n) % 56) >> 2) * 8 + ((n) / 56) * 4 + ((n) & 3))
#define H4_ADDR(n) (H4_BLK(n) < 64 ? ab0 + H4_BLK(n) * 1024 : ab1 + (H4_BLK(n) - 64) * 1024)
#define P4_EPILOGUE(M0) do { _Pragma("unroll") for (int m = (M0); m < (M0) + 2; ++m) { \
            u32 a[2][2]; \
            _Pragma("unroll") for (int u = 0; u < 2; ++u) { \
                const int tt = 2 * m + u; \
                const u32x2 st = *(LAS u32x2*)(uintptr_t)(L_ST4 + (16 * tt + t) * 8); \
                const float rstd = __uint_as_float(st[0]), mr = __uint_as_float(st[1]); \
                float v[4]; \
                _Pragma("unroll") for (int i = 0; i < 4; ++i) { float z = fmaf(acc[tt][i], rstd, fmaf(cs4[i], mr, bias4[i])); z = fmaxf(z, 0.f); v[i] = z * z; } \
                a[u][0] = pack2(v[0], v[1]); a[u][1] = pack2(v[2], v[3]); \
            } \
              \
            const auto r0 = __builtin_amdgcn_permlane16_swap(a[0][0], a[1][0], false, false); \
            const auto r1 = __builtin_amdgcn_permlane16_swap(a[0][1], a[1][1], false, false); \
            const u32x4 o = {r0[0], r1[0], r0[1], r1[1]}; \
            const int TTo = 8 * RB4 + 2 * m + (g & 1); \
            st_sc1(rs, WS_HH + (u32)(TTo * 56 + 4 * cg4 + (w >> 1)) * 1024 + ((2 * (w & 1) + (g >> 1)) * 16 + t) * 16, o); \
        } } while (0)
        wait_vm<2>();
        BAR();
#pragma unroll
        for (int n = 0; n < 6; ++n) af[n] = lds_ld128(H4_ADDR(n));
        SB();
#pragma unroll
        for (int seg = 0; seg < 7; ++seg) {
            constexpr int SB0[8] = {0, 10, 26, 42, 50, 66, 82, 112};
            if (seg == 1) {
                wait_vm<2>();
                const u32x2 p0 = *(LAS u32x2*)(uintptr_t)(L_ST4 + 1024 + w * 1024 + g * 128 + t * 8);
                const u32x2 p1 = *(LAS u32x2*)(uintptr_t)(L_ST4 + 1024 + w * 1024 + (g + 4) * 128 + t * 8);
                float sum = __uint_as_float(p0[0]), sq = __uint_as_float(p0[1]);
                if (g < 3) { sum += __uint_as_float(p1[0]); sq += __uint_as_float(p1[1]); }
                sum = rows_add(sum); sq = rows_add(sq);
                const float mean = sum * (1.f / 448.f);
                const float rstd = rsqrtf(fmaxf(sq * (1.f / 448.f) - mean * mean, 0.f) + 1e-5f);
                if (g == 0) lds_st64(L_ST4 + (16 * w + t) * 8, (u32x2){__float_as_uint(rstd), __float_as_uint(-mean * rstd)});
                BAR0();
            }
            if (seg == 2) { wait_vm<4>(); BAR(); }
            if (seg == 3) { wait_vm<7>(); BAR0(); }
            if (seg == 4) { wait_vm<5>(); BAR0(); }
            if (seg == 5) { wait_vm<7>(); BAR0(); }
            if (seg == 6) {
                wait_vm<4>(); BAR0();
                if (w == 0) set_flag(rs, WS_F4 + (RB4 * 2) * FGRP + cg4 * FSTRIDE, tag, l);
            }
#pragma unroll
            for (int n = SB0[seg]; n < SB0[seg + 1]; ++n) {
                if (n == 56) {
                    asm volatile("s_nop 15\n\ts_nop 15" : "+v"(acc[0]), "+v"(acc[1]), "+v"(acc[2]), "+v"(acc[3]));
                    P4_EPILOGUE(0);
                    SB();
                }
                const int ai = (n / 56) * 4 + (n & 3), ks = (n % 56) >> 2;
                if (ks == 0) mfma_first(acc[ai], w1f[ks], af[n & 7]); else mfma_acc(acc[ai], w1f[ks], af[n & 7]);
                if (n + 6 < 112) af[(n + 6) & 7] = lds_ld128(H4_ADDR(n + 6));
                if (n == 4 || n == 8 || n == 12) dma_next_sc1<16384, 2048>(hsA, hv);
                if (n == 16) { hv = l * 16 - 2048; dma_seek((w >> 2) * 8192 + (4 + (w & 3)) * 1024 - 16384); }
                if (n >= 16 && n <= 40 && (n & 3) == 0) dma_next_sc1<16384, 2048>(hsB, hv);
                if (n >= 58 && (n - 58) % 6 == 0 && (n - 58) / 6 < 7) w2f[14 + (n - 58) / 6] = W2F(14 + (n - 58) / 6);
                SB();
            }
        }
        asm volatile("s_nop 15\n\ts_nop 15" : "+v"(acc[4]), "+v"(acc[5]), "+v"(acc[6]), "+v"(acc[7]));
        P4_EPILOGUE(2);
        WAIT_VM(0);
        BAR();
        if (w == 0) set_flag(rs, WS_F4 + (RB4 * 2 + 1) * FGRP + cg4 * FSTRIDE, tag, l);
    }
    SB();
#pragma unroll
    for (int s = 21; s < 28; ++s) w2f[s] = W2F(s);
    SB();
    {
        const u32x4 bb = *(const u32x4*)((const char*)b2 + cg * 128 + fo8 * 16);
        const u32 f4o = WS_F4 + (u32)(RBW * 2) * FGRP + (7 * kh + (l < 7 ? l : 0)) * FSTRIDE;
        wait_flags(ws, f4o, tag);
        SB();
        u32 f4b = 0;
        const char* hsA = ws + WS_HH + (size_t)(TTG(cw & 1) * 56 + 28 * kh + (cw >> 1) + rot5) * 1024;
        const char* hsB = ws + WS_HH + (size_t)(TTG(2 + (cw & 1)) * 56 + 28 * kh + (cw >> 1) + rot5) * 1024;
        const int wrap5 = (28 - rot5) >> 1;
#define H5SRC(base, i) ((base) + ((i) < wrap5 ? (i) * 2048 : (i) * 2048 - 28 * 1024))
        const u32 hl = kh * L_H5 + (cw >> 1) * 2048 + (cw & 1) * 1024;
        const u32 hv = l * 16;
        dma_seek(hl - 4096);
#pragma unroll
        for (int i = 0; i < 6; ++i) dma_next_sc1_b<4096>(H5SRC(hsA, i), hv);
        f32x4 acc[4];
        u32x4 af[8];
        u32 ab0 = kh * L_H5 + l * 16, ab1 = kh * L_H5 + l * 16 + 65536;
        OPAQUE(ab0); OPAQUE(ab1);
#define H5_KS(n) (((n) % 56) >> 1)
#define H5_SLOT(n) ((n) < 56 ? H5_KS(n) : H5_KS(n) < 12 ? 28 + H5_KS(n) : H5_KS(n) - 12)
#define H5_BLK(n) (H5_SLOT(n) * 2 + ((n) & 1))
#define H5_ADDR(n) (H5_BLK(n) < 64 ? ab0 + H5_BLK(n) * 1024 : ab1 + (H5_BLK(n) - 64) * 1024)
        wait_vm<4>();
        BAR();
#pragma unroll
        for (int n = 0; n < 6; ++n) af[n] = lds_ld128(H5_ADDR(n));
        SB();
#pragma unroll
        for (int seg = 0; seg < 9; ++seg) {
            constexpr int SB0[10] = {0, 2, 10, 26, 42, 50, 66, 82, 98, 112};
            if (seg == 1) { wait_vm<3>(); BAR0(); }
            if (seg == 2) {
                wait_vm<2>(); BAR0();
                asm volatile("global_load_dword %0, %1, %2 sc1" : "=v"(f4b) : "v"(f4o + FGRP), "s"(ws) : "memory");
            }
            if (seg == 3) {
                wait_vm<2>();
                asm volatile("" : "+v"(f4b));
                if (!__all(f4b == tag)) wait_flags(ws, f4o + FGRP, tag);
                BAR0();
            }
            if (seg == 4) { wait_vm<5>(); BAR0(); }
            if (seg == 5) { wait_vm<4>(); BAR0(); }
            if (seg == 6) { wait_vm<5>(); BAR0(); }
            if (seg == 7) { wait_vm<2>(); BAR0(); }
            if (seg == 8) { wait_vm<0>(); BAR0(); }
#pragma unroll
            for (int n = SB0[seg]; n < SB0[seg + 1]; ++n) {
                const int ai = (n / 56) * 2 + (n & 1);
                if (H5_KS(n) == 0) mfma_first(acc[ai], w2f[H5_KS(n)], af[n & 7]); else mfma_acc(acc[ai], w2f[H5_KS(n)], af[n & 7]);
                if (n + 6 < 112) af[(n + 6) & 7] = lds_ld128(H5_ADDR(n + 6));
                constexpr int NB0 = 28;
                if (n <= 21 && n % 3 == 0) dma_next_sc1_b<4096>(H5SRC(hsA, 6 + n / 3), hv);
                if (n == NB0) dma_seek(hl + 28 * 2048 - 4096);
                if (n == NB0 + 18) dma_seek(hl - 4096);
                if (n >= NB0 && n <= NB0 + 39 && (n - NB0) % 3 == 0) dma_next_sc1_b<4096>(H5SRC(hsB, (n - NB0) / 3), hv);
                SB();
            }
        }
        MFMA_FENCE4(acc);
        const u32 rw = (w < 6 ? 32768 + w * 4096 : L_H5 + 32768 + (w - 6) * 4096);
#pragma unroll
        for (int tt = 0; tt < 4; ++tt) lds_stf4(rw + tt * 1024 + l * 16, acc[tt]);
        BAR();
        const int c5 = fo8 >> 1;
        const u32 lp = (w >> 1) * 1024 + ((2 * (fo8 & 1)) * 16 + tl) * 16;
        const u32 r0a = 32768 + c5 * 4096 + lp, r1a = (c5 < 2 ? 32768 + (4 + c5) * 4096 : L_H5 + 32768 + (c5 - 2) * 4096) + lp;
        const f32x4 a0 = lds_ldf4(r0a) + lds_ldf4(r1a), a1 = lds_ldf4(r0a + 256) + lds_ldf4(r1a + 256);
        const u32x4 o = {pack2(a0[0] + bflo(bb[0]) + bflo(hres[0]), a0[1] + bfhi(bb[0]) + bfhi(hres[0])),
                         pack2(a0[2] + bflo(bb[1]) + bflo(hres[1]), a0[3] + bfhi(bb[1]) + bfhi(hres[1])),
                         pack2(a1[0] + bflo(bb[2]) + bflo(hres[2]), a1[1] + bfhi(bb[2]) + bfhi(hres[2])),
                         pack2(a1[2] + bflo(bb[3]) + bflo(hres[3]), a1[3] + bfhi(bb[3]) + bfhi(hres[3]))};
        __builtin_amdgcn_raw_buffer_store_b128(o, __builtin_amdgcn_make_buffer_rsrc(out, 0, 2048 * 896, 0x00020000), (u32)tok * 896 + cg * 128 + fo8 * 16, 0, 16);
    }
}

extern "C" void kernel_launch(void* const* d_in, const int* in_sizes, int n_in, void* d_out, int out_size, void* d_ws, size_t ws_size,
                              hipStream_t stream) {
    (void)hipFuncSetAttribute((const void*)fused_kernel, hipFuncAttributeMaxDynamicSharedMemorySize, LDS_BYTES);
    fused_kernel<<<256, 512, LDS_BYTES, stream>>>((const u16*)d_in[0], (const u16*)d_in[1], (const u16*)d_in[2], (const u16*)d_in[4],
                                                  (const u16*)d_in[5], (const u16*)d_in[6], (const u16*)d_in[7], (u16*)d_out, (char*)d_ws);
}
```

```cpp
#include <hip/hip_runtime.h>
#include <stdint.h>

typedef unsigned short u16;
typedef unsigned int u32;
typedef __attribute__((ext_vector_type(8))) __bf16 bf16x8;
typedef __attribute__((ext_vector_type(2))) __bf16 bf16x2;
typedef __attribute__((ext_vector_type(4))) short s16x4;
typedef __attribute__((ext_vector_type(4))) float f32x4;
typedef __attribute__((ext_vector_type(2))) float f32x2;
typedef __attribute__((ext_vector_type(4))) u32 u32x4;
typedef __attribute__((ext_vector_type(2))) u32 u32x2;
#define LAS __attribute__((address_space(3)))
#define GAS __attribute__((address_space(1)))
#define DI __device__ __forceinline__

constexpr u32 FSTRIDE = 128;
constexpr u32 FGRP = 16 * FSTRIDE;
constexpr u32 WS_GEN = 0;
constexpr u32 WS_FW = 4096;
constexpr u32 WS_FQ = WS_FW + 448 * 128;
constexpr u32 WS_F1 = WS_FQ + 480 * 128;
constexpr u32 WS_F2 = WS_F1 + 256 * FGRP;
constexpr u32 WS_F3 = WS_F2 + 128 * FGRP;
constexpr u32 WS_F4 = WS_F3 + 128 * FGRP;
constexpr u32 WS_KV = WS_F4 + 32 * FGRP;
constexpr u32 WS_O = WS_KV + 16 * 16 * 16384;
constexpr u32 WS_H = WS_O + 128 * 16 * 1024;
constexpr u32 WS_ST = WS_H + 128 * 14 * 1024;
constexpr u32 WS_HH = WS_ST + 128 * 14 * 128;
constexpr u32 WS_W1F = WS_HH + 128 * 56 * 1024;
constexpr u32 WS_W2F = WS_W1F + 112 * 14 * 1024;
constexpr u32 WS_WOF = WS_W2F + 28 * 56 * 1024;
constexpr u32 WS_WQF = WS_WOF + 28 * 16 * 1024;
constexpr u32 WS_END = WS_WQF + 16 * 60 * 1024;
constexpr u32 L_WA = 114688;
constexpr u32 L_RT = 151552;
constexpr u32 L_GEN = 157696;
constexpr u32 L_ST1 = 157952;
constexpr u32 L_CS1 = 158976;
constexpr u32 L_Q = 49152;
constexpr u32 L_PP = 98304;
constexpr u32 L_K = 0, L_V = 24576;
constexpr u32 L_RED3 = 65536;
constexpr u32 L_ST4 = 114688;
constexpr u32 L_H5 = 81920;
constexpr u32 LDS_BYTES = 163840;
constexpr int SPIN_MAX = 1 << 14;

DI f32x4 mfma16(u32x4 a, u32x4 b, f32x4 c) {
    return __builtin_amdgcn_mfma_f32_16x16x32_bf16(__builtin_bit_cast(bf16x8, a), __builtin_bit_cast(bf16x8, b), c, 0, 0, 0);
}
DI void mfma_acc(f32x4& acc, const u32x4& a, const u32x4& b) {
    asm volatile("v_mfma_f32_16x16x32_bf16 %0, %1, %2, %0" : "+v"(acc) : "v"(a), "v"(b));
}
DI void mfma_first(f32x4& acc, const u32x4& a, const u32x4& b) {
    asm volatile("v_mfma_f32_16x16x32_bf16 %0, %1, %2, 0" : "=&v"(acc) : "v"(a), "v"(b));
}
#define MFMA_FENCE4(a) asm volatile("s_nop 15\n\ts_nop 15" : "+v"(a[0]), "+v"(a[1]), "+v"(a[2]), "+v"(a[3]))
#define OPAQUE(x) asm volatile("" : "+v"(x))
DI u32 pack2(float lo, float hi) {
    f32x2 v = {lo, hi};
    bf16x2 r = __builtin_convertvector(v, bf16x2);
    return __builtin_bit_cast(u32, r);
}
DI float relu(float z) { float r; asm("v_max_f32 %0, 0, %1" : "=v"(r) : "v"(z)); return r; }
DI float bflo(u32 u) { return __uint_as_float(u << 16); }
DI float bfhi(u32 u) { return __uint_as_float(u & 0xffff0000u); }
DI u32x2 trread(u32 off) {
    s16x4 r = __builtin_amdgcn_ds_read_tr16_b64_v4i16((LAS s16x4*)(uintptr_t)off);
    return __builtin_bit_cast(u32x2, r);
}
DI u32x4 trfrag(u32 off0, u32 off1) {
    u32x2 a = trread(off0), b = trread(off1);
    u32x4 r = {a[0], a[1], b[0], b[1]};
    return r;
}
DI u32x4 lds_ld128(u32 off) { return *(LAS u32x4*)(uintptr_t)off; }
DI f32x4 lds_ldf4(u32 off) { return *(LAS f32x4*)(uintptr_t)off; }
DI void lds_st128(u32 off, u32x4 v) { *(LAS u32x4*)(uintptr_t)off = v; }
DI void lds_stf4(u32 off, f32x4 v) { *(LAS f32x4*)(uintptr_t)off = v; }
DI void lds_st64(u32 off, u32x2 v) { *(LAS u32x2*)(uintptr_t)off = v; }
DI void lds_stf(u32 off, float v) { *(LAS float*)(uintptr_t)off = v; }
template <int N>
DI void glds16_run(const void* sbase, u32 voff, u32 lds, u32 gstep, u32 lstep) {
    u32 keep;
    asm volatile("s_mov_b32 %0, m0\n\ts_mov_b32 m0, %3\n\t"
                 ".rept %c6\n\t"
                 "s_nop 0\n\t"
                 "global_load_lds_dwordx4 %1, %2\n\t"
                 "s_add_u32 m0, m0, %5\n\t"
                 "v_add_u32 %1, %4, %1\n\t"
                 ".endr\n\t"
                 "s_mov_b32 m0, %0"
                 : "=&s"(keep), "+v"(voff) : "s"(sbase), "s"(lds), "s"(gstep), "s"(lstep), "i"(N) : "memory", "scc");
}
template <int N>
DI void glds16_run_sc1(const void* sbase, u32 voff, u32 lds, u32 gstep, u32 lstep) {
    u32 keep;
    asm volatile("s_mov_b32 %0, m0\n\ts_mov_b32 m0, %3\n\t"
                 ".rept %c6\n\t"
                 "s_nop 0\n\t"
                 "global_load_lds_dwordx4 %1, %2 sc1\n\t"
                 "s_add_u32 m0, m0, %5\n\t"
                 "v_add_u32 %1, %4, %1\n\t"
                 ".endr\n\t"
                 "s_mov_b32 m0, %0"
                 : "=&s"(keep), "+v"(voff) : "s"(sbase), "s"(lds), "s"(gstep), "s"(lstep), "i"(N) : "memory", "scc");
}
DI void glds16_one(const void* sbase, u32 voff, u32 goff, u32 lds) {
    u32 tmp;
    asm volatile("s_mov_b32 m0, %4\n\tv_add_u32 %0, %3, %1\n\tglobal_load_lds_dwordx4 %0, %2" : "=&v"(tmp) : "v"(voff), "s"(sbase), "s"(goff), "s"(lds) : "memory");
}
DI void dma_seek(u32 lds) { asm volatile("s_mov_b32 m0, %0" :: "s"(lds) : "memory"); }
template <int LSTEP, int GSTEP>
DI void dma_next_sc1(const void* sbase, u32& voff) {
    asm volatile("s_add_u32 m0, m0, %c2\n\tv_add_u32 %0, %c3, %0\n\tglobal_load_lds_dwordx4 %0, %1 sc1" : "+v"(voff) : "s"(sbase), "i"(LSTEP), "i"(GSTEP) : "memory", "scc");
}
template <int LSTEP>
DI void dma_next_sc1_b(const void* sbase, u32 voff) {
    asm volatile("s_add_u32 m0, m0, %c2\n\ts_nop 0\n\tglobal_load_lds_dwordx4 %0, %1 sc1" :: "v"(voff), "s"(sbase), "i"(LSTEP) : "memory", "scc");
}
#define PIN_S(x) asm volatile("" : "+s"(x))
#define SB() __builtin_amdgcn_sched_barrier(0)
#define WAIT_VM(n) asm volatile("s_waitcnt vmcnt(" #n ")" ::: "memory")
template <int N> DI void wait_vm() { asm volatile("s_waitcnt vmcnt(%c0)" :: "i"(N) : "memory"); }
#define BAR0() asm volatile("s_barrier" ::: "memory")
#define BAR() asm volatile("s_waitcnt lgkmcnt(0)\n\ts_barrier" ::: "memory")
DI float rows_add(float v) {
    const u32 a = __float_as_uint(v);
    const auto r = __builtin_amdgcn_permlane16_swap(a, a, false, false);
    const u32 b2 = __float_as_uint(__uint_as_float(r[0]) + __uint_as_float(r[1]));
    const auto q = __builtin_amdgcn_permlane32_swap(b2, b2, false, false);
    return __uint_as_float(q[0]) + __uint_as_float(q[1]);
}
DI float rows_max(float v) {
    const u32 a = __float_as_uint(v);
    const auto r = __builtin_amdgcn_permlane16_swap(a, a, false, false);
    const u32 b2 = __float_as_uint(fmaxf(__uint_as_float(r[0]), __uint_as_float(r[1])));
    const auto q = __builtin_amdgcn_permlane32_swap(b2, b2, false, false);
    return fmaxf(__uint_as_float(q[0]), __uint_as_float(q[1]));
}
DI float oct_add(float v) {
    v += __uint_as_float(__builtin_amdgcn_update_dpp(0, __float_as_uint(v), 0xB1, 0xf, 0xf, true));
    v += __uint_as_float(__builtin_amdgcn_update_dpp(0, __float_as_uint(v), 0x4E, 0xf, 0xf, true));
    v += __uint_as_float(__builtin_amdgcn_update_dpp(0, __float_as_uint(v), 0x141, 0xf, 0xf, true));
    return v;
}

typedef __amdgpu_buffer_rsrc_t rsrc_t;
DI u32x4 ld_sc1(rsrc_t r, u32 off) { return __builtin_amdgcn_raw_buffer_load_b128(r, off, 0, 16); }
DI void st_sc1(rsrc_t r, u32 off, u32x4 v) { __builtin_amdgcn_raw_buffer_store_b128(v, r, off, 0, 16); }
DI void st_sc1_64(rsrc_t r, u32 off, u32x2 v) { __builtin_amdgcn_raw_buffer_store_b64(v, r, off, 0, 16); }
DI void set_flag(rsrc_t rs, u32 off, u32 tag, int lane) {
    if (lane < 8) st_sc1(rs, off + lane * 16, (u32x4){tag, tag, tag, tag});
}
DI void wait_flags(char* ws, u32 off, u32 tag) {
    for (int it = 0; it < SPIN_MAX; ++it) {
        u32 v = __hip_atomic_load((GAS u32*)(ws + off), __ATOMIC_RELAXED, __HIP_MEMORY_SCOPE_AGENT);
        if (__all(v == tag)) break;
        __builtin_amdgcn_s_sleep(1);
    }
    asm volatile("" ::: "memory");
}

__global__ __launch_bounds__(512, 2) __attribute__((amdgpu_waves_per_eu(2, 2)))
void fused_kernel(const u16* __restrict__ x, const u16* __restrict__ wqkv, const u16* __restrict__ wo, const u16* __restrict__ w1,
                  const u16* __restrict__ b1, const u16* __restrict__ w2, const u16* __restrict__ b2, u16* __restrict__ out,
                  char* __restrict__ ws) {
    const int b = blockIdx.x, xcd = b & 7, loc = b >> 3;
    const int tid = threadIdx.x, w = __builtin_amdgcn_readfirstlane(tid >> 6), l = tid & 63, t = l & 15, g = l >> 4;
    const int q4 = t >> 2, p4 = l & 3;
    const rsrc_t rs = __builtin_amdgcn_make_buffer_rsrc(ws, 0, WS_END, 0x00020000);
    const int RB = 4 * (xcd >> 1) + (loc >> 3), h = 8 * (xcd & 1) + (loc & 7);
    const bool odd = h & 1;
    const int tp = w >> 1, ch = w & 1;
    {
        const char* xb = (const char*)x + (size_t)(128 * RB + 16 * w) * 896;
        const u32 xv = (l >> 2) * 896 + (((l & 3) ^ ((-(l >> 4)) & 3)) * 16);
        if (w < 7) {
            glds16_run<14>(xb, xv, w * 14336, 64, 1024);
            glds16_run<2>((const char*)x + (size_t)(128 * RB + 112) * 896 + w * 128, xv, 7 * 14336 + w * 2048, 64, 1024);
            asm volatile("s_barrier" ::: "memory");
            if (w < 4) {
                const char* wqb = (const char*)wqkv + 56 * h - (h & 1) * 8;
                const u32 wqv = (l >> 2) * 2688 + (((l & 3) ^ ((l >> 5) << 1)) * 16);
#pragma unroll
                for (int hf = 0; hf < 2; ++hf) glds16_run<3>(wqb + (32 * w + 16 * hf) * 2688, wqv, L_WA + w * 6144 + hf * 1024, 896, 2048);
            } else {
                WAIT_VM(0);
            }
            WAIT_VM(6);
        } else {
            u32 qd[2];
#pragma unroll
            for (int i = 0; i < 2; ++i) {
                const int q = 2 * b + i < 480 ? 2 * b + i : 479, qh = q / 30, r = q - 30 * qh, qs = r / 3, qm = r - 3 * qs;
                glds16_run<2>((const char*)wqkv + (size_t)(32 * (4 + qs)) * 2688 + qm * 896 + 56 * qh - (qh & 1) * 8, (l >> 2) * 2688 + (l & 3) * 16, L_RT + i * 2048, 16 * 2688, 1024);
                qd[i] = (u32)q;
            }
            asm volatile("s_mov_b32 m0, %2\n\ts_nop 0\n\tglobal_load_lds_dword %0, %1 sc1" :: "v"(0), "s"(ws + WS_GEN), "s"(L_GEN) : "memory");
            WAIT_VM(0);
            const u32 tg = __builtin_amdgcn_readfirstlane(*(LAS u32*)(uintptr_t)L_GEN) + 1u;
            if (2 * b < 480) {
#pragma unroll
                for (int i = 0; i < 2; ++i) {
                    const int q = (int)qd[i], qh = q / 30, r = q - 30 * qh, qs = r / 3, qm = r - 3 * qs;
#pragma unroll
                    for (int hf = 0; hf < 2; ++hf) {
                        const u32 a = L_RT + i * 2048 + (8 * g + q4) * 64 + hf * 32 + 8 * p4;
                        u32x4 f = trfrag(a, a + 256);
                        if (qm < 2 && ((qh & 1) ? (hf == 0 && t < 4) : (hf == 1 && t >= 12))) f = (u32x4){0x3f803f80u, 0x3f803f80u, 0x3f803f80u, 0x3f803f80u};
                        st_sc1(rs, WS_WQF + (u32)((qh * 10 + qs) * 6 + qm * 2 + hf) * 1024 + l * 16, f);
                    }
                }
            }
            asm volatile("s_barrier" ::: "memory");
            if (2 * b < 480) {
                WAIT_VM(0);
                if (l < 16) st_sc1(rs, WS_FQ + (u32)(2 * b) * 128 + l * 16, (u32x4){tg, tg, tg, tg});
            }
        }
    }
    const int pp = b + 256 * (w >> 2);
    const bool ppok = pp < 224 ? (pp & 31) < 28 : pp < 448 ? (pp & 31) < 28 : pp < 504;
    const char* ppsrc; u32 pppitch, ppdst, ppfl;
    {
        const int pe = ppok ? pp : 0, c = w & 3;
        if (pe < 224) { const int rc = pe >> 5, cl = pe & 31; ppsrc = (const char*)w1 + cl * 128 + (size_t)rc * (64 * 3584); pppitch = 3584;
                        ppdst = WS_W1F + (u32)(((cl >> 1) * 8 + (cl & 1) * 4 + c) * 14 + 2 * rc) * 1024; ppfl = (u32)(cl * 7 + rc); }
        else if (pe < 448) { const int g2 = (pe - 224) >> 5, rc = pe & 31; ppsrc = (const char*)w2 + g2 * 128 + (size_t)rc * (64 * 896); pppitch = 896;
                             ppdst = WS_W2F + (u32)((g2 * 4 + c) * 56 + 2 * rc) * 1024; ppfl = (u32)(196 + g2 * 28 + rc); }
        else { const int i = pe - 448; ppsrc = (const char*)wo + (i >> 3) * 128 + (size_t)(i & 7) * (56 * 896); pppitch = 896;
               ppdst = WS_WOF + (u32)(((i >> 3) * 4 + c) * 16 + 2 * (i & 7)) * 1024; ppfl = (u32)(392 + i); }
    }
    const bool ppshort = pp >= 448 && (w & 3) == 3;
    const u32 ppv = (l >> 3) * pppitch + (l & 7) * 16;
    u32 ppg0 = (u32)(16 * (w & 3)) * pppitch, ppg1 = ppshort ? ppg0 : ppg0 + 8 * pppitch;
    u32 ppl0 = L_PP + (w >> 2) * 8192 + (w & 3) * 2048, ppl1 = ppshort ? ppl0 : ppl0 + 1024;
    const u32 ppa = L_PP + (w >> 2) * 8192 + (w & 3) * 32 + 8 * p4 + (pp < 448 ? (8 * g + q4) * 128 : (4 * g + q4) * 128);
    const u32 ppk = pp < 448 ? 4096 : 3072, pph = pp < 448 ? 512 : 2048;
    PIN_S(ppg0); PIN_S(ppg1); PIN_S(ppl0); PIN_S(ppl1); PIN_S(ppdst); PIN_S(ppfl);
    BAR();
    u32x4 xf[2][14];
    {
        const u32 xl = t * 64 + ((g ^ ((-(t >> 2)) & 3)) * 16);
        const u32 xa = w * 14336 + xl, xb2 = (w ^ 1) * 14336 + xl;
#pragma unroll
        for (int s = 0; s < 14; ++s) { xf[0][s] = lds_ld128(xa + s * 1024); xf[1][s] = lds_ld128(xb2 + s * 1024); }
    }
    const u32 tag = __builtin_amdgcn_readfirstlane(*(LAS u32*)(uintptr_t)L_GEN) + 1u;
    WAIT_VM(0);
    if (w < 4) lds_st64(L_WA + w * 6144 + (l >> 5) * 2048 + (l & 31) * 64 + ((((odd ? 0 : 1) ^ ((l >> 3) & 1)) * 32) + (odd ? 0 : 24)), (u32x2){0x3f803f80u, 0x3f803f80u});
    if (w == 7) wait_flags(ws, WS_FQ + (u32)(h * 30 + (l < 30 ? l : 0)) * 128, tag);
    BAR();
    {
        u32 wv = l * 16 + w * 1024 - 8192;
        dma_seek(w * 1024 - 8192);
#pragma unroll
        for (int j = 0; j < 7; ++j) dma_next_sc1<8192, 8192>(ws + WS_WQF + (size_t)(h * 60) * 1024, wv);
        dma_seek((w < 4 ? 56 + w : 59) * 1024 - 8192);
        dma_next_sc1_b<8192>(ws + WS_WQF + (size_t)(h * 60 + (w < 4 ? 56 + w : 59)) * 1024, l * 16);
    }
    SB();
    {
        f32x4 acc[2][3], cs[3];
        u32 wb1[3] = {(u32)(2 * ch) * 1024 + l * 16, (u32)(2 * ch + 1) * 1024 + l * 16, (u32)(4 + ch) * 1024 + l * 16};
        u32 wb0[3];
        {
            const u32 h0 = (8 * g + q4) * 64 + ((g & 1) * 32) + p4 * 8, h1 = (8 * g + q4) * 64 + (((g & 1) ^ 1) * 32) + p4 * 8;
            wb0[0] = L_WA + h0 + ch * 2048; wb0[1] = L_WA + h1 + ch * 2048; wb0[2] = L_WA + (ch ? h1 : h0) + 4096;
        }
        OPAQUE(wb0[0]); OPAQUE(wb0[1]); OPAQUE(wb0[2]); OPAQUE(wb1[0]); OPAQUE(wb1[1]); OPAQUE(wb1[2]);
#define WQ_FRAG(s, c) ((s) < 4 ? trfrag(wb0[c] + (s) * 6144, wb0[c] + (s) * 6144 + 256) : lds_ld128(wb1[c] + ((s) - 4) * 6144))
        u32x4 ones = {0x3f803f80u, 0x3f803f80u, 0x3f803f80u, 0x3f803f80u};
        OPAQUE(ones);
        f32x4 gram;
        u32x4 wf[2][3];
#pragma unroll
        for (int c = 0; c < 3; ++c) wf[0][c] = WQ_FRAG(0, c);
#pragma unroll
        for (int c = 0; c < 3; ++c) { cs[c] = (f32x4){0.f, 0.f, 0.f, 0.f}; OPAQUE(cs[c]); }
        asm volatile("s_nop 1");
        SB();
#pragma unroll
        for (int s = 0; s < 14; ++s) {
            if (s == 3) { wait_vm<6>(); BAR0(); }
            if (s == 5) { wait_vm<3>(); BAR0(); }
            if (s == 9) { wait_vm<2>(); BAR0(); }
            if (s == 11) { wait_vm<0>(); BAR(); }
            if (s == 6) glds16_one(ppsrc, ppv, ppg0, ppl0);
            if (s == 7) glds16_one(ppsrc, ppv, ppg1, ppl1);
#pragma unroll
            for (int i = 0; i < 6; ++i) {
                const int c = i >> 1, tt = i & 1;
                if (c < 2) { if (s == 0) mfma_first(acc[tt][c], wf[s & 1][c], xf[tt][s]); else mfma_acc(acc[tt][c], wf[s & 1][c], xf[tt][s]); }
                else { if (s == 0) mfma_first(acc[tt][c], xf[tt][s], wf[s & 1][c]); else mfma_acc(acc[tt][c], xf[tt][s], wf[s & 1][c]); }
                if (s + 1 < 14 && tt == 0) wf[(s + 1) & 1][c] = WQ_FRAG(s + 1, c);
                if (i == 5) { if (s == 0) mfma_first(gram, xf[0][s], xf[0][s]); else mfma_acc(gram, xf[0][s], xf[0][s]); }
                SB();
            }
            if ((s == 12 || s == 13) && ppok)
                st_sc1(rs, ppdst + (s - 12) * 1024 + l * 16, trfrag(ppa + (s - 12) * ppk, ppa + (s - 12) * ppk + pph));
            if ((s & 3) == tp) {
                mfma_acc(cs[0], wf[s & 1][0], ones); mfma_acc(cs[1], wf[s & 1][1], ones); mfma_acc(cs[2], ones, wf[s & 1][2]);
            }
            SB();
        }
        asm volatile("s_nop 15\n\ts_nop 15" : "+v"(acc[0][0]), "+v"(acc[0][1]), "+v"(acc[0][2]), "+v"(acc[1][0]), "+v"(acc[1][1]), "+v"(acc[1][2]), "+v"(gram),
                     "+v"(cs[0]), "+v"(cs[1]), "+v"(cs[2]));
        {
            const float sum = rows_add(g == (odd ? 0 : 3) ? (odd ? acc[0][0][0] : acc[0][1][0]) : 0.f);
            const float dg = (t & 2) ? ((t & 1) ? gram[3] : gram[2]) : ((t & 1) ? gram[1] : gram[0]);
            const float sq = rows_add(g == (t >> 2) ? dg : 0.f);
            const float mean = sum * (1.f / 448.f);
            const float rstd = rsqrtf(fmaxf(sq * (1.f / 448.f) - mean * mean, 0.f) + 1e-5f);
            if (g == 0) lds_st64(L_ST1 + (16 * w + t) * 8, (u32x2){__float_as_uint(rstd), __float_as_uint(-mean * rstd)});
            if (t == 0) { lds_stf4(L_CS1 + ((tp * 6 + ch * 3 + 0) * 16 + 4 * g) * 4, cs[0]); lds_stf4(L_CS1 + ((tp * 6 + ch * 3 + 1) * 16 + 4 * g) * 4, cs[1]); }
            if (g == 0) lds_stf(L_CS1 + ((tp * 6 + ch * 3 + 2) * 16 + t) * 4, cs[2][0]);
        }
        BAR();
        f32x4 cp0[4], cp1[4]; float cp2[4];
        u32x2 stq[2]; u32x4 sa[2], sb[2];
#pragma unroll
        for (int p = 0; p < 4; ++p) {
            cp0[p] = lds_ldf4(L_CS1 + ((p * 6 + ch * 3 + 0) * 16 + 4 * g) * 4);
            cp1[p] = lds_ldf4(L_CS1 + ((p * 6 + ch * 3 + 1) * 16 + 4 * g) * 4);
            cp2[p] = *(LAS float*)(uintptr_t)(L_CS1 + ((p * 6 + ch * 3 + 2) * 16 + t) * 4);
        }
#pragma unroll
        for (int tt = 0; tt < 2; ++tt) {
            const int tile = w ^ tt;
            stq[tt] = *(LAS u32x2*)(uintptr_t)(L_ST1 + (16 * tile + t) * 8);
            sa[tt] = lds_ld128(L_ST1 + (16 * tile + 4 * g) * 8); sb[tt] = lds_ld128(L_ST1 + (16 * tile + 4 * g + 2) * 8);
        }
        SB();
        f32x4 csum[2];
        csum[0] = (cp0[0] + cp0[1]) + (cp0[2] + cp0[3]); csum[1] = (cp1[0] + cp1[1]) + (cp1[2] + cp1[3]);
        const float csv = (cp2[0] + cp2[1]) + (cp2[2] + cp2[3]);
        const float qs = ch ? 1.f : 0.18898223650461362f * 1.4426950408889634f;
        const float m0 = (odd && g == 0) ? 0.f : qs, m1 = (!odd && g == 3) ? 0.f : qs;
        const bool vpad = ch == 0 ? (odd && t < 4) : (!odd && t >= 12);
        const u32 blob = WS_KV + (u32)(RB * 16 + h) * 16384;
        u32x2 vv[2];
#pragma unroll
        for (int tt = 0; tt < 2; ++tt) {
            const int tile = w ^ tt;
            const float rstd = __uint_as_float(stq[tt][0]), mr = __uint_as_float(stq[tt][1]);
            float a[8];
#pragma unroll
            for (int i = 0; i < 4; ++i) { a[i] = fmaf(acc[tt][0][i], rstd, csum[0][i] * mr) * m0; a[4 + i] = fmaf(acc[tt][1][i], rstd, csum[1][i] * mr) * m1; }
            const u32x4 f = {pack2(a[0], a[1]), pack2(a[2], a[3]), pack2(a[4], a[5]), pack2(a[6], a[7])};
            if (ch) { st_sc1(rs, blob + tile * 1024 + l * 16, f); lds_st128(L_K + (16 + tile) * 1024 + l * 16, f); }
            else lds_st128(L_Q + tile * 1024 + l * 16, f);
            const float v0 = fmaf(acc[tt][2][0], __uint_as_float(sa[tt][0]), csv * __uint_as_float(sa[tt][1]));
            const float v1 = fmaf(acc[tt][2][1], __uint_as_float(sa[tt][2]), csv * __uint_as_float(sa[tt][3]));
            const float v2 = fmaf(acc[tt][2][2], __uint_as_float(sb[tt][0]), csv * __uint_as_float(sb[tt][1]));
            const float v3 = fmaf(acc[tt][2][3], __uint_as_float(sb[tt][2]), csv * __uint_as_float(sb[tt][3]));
            vv[tt] = (u32x2){pack2(v0, v1), pack2(v2, v3)};
            if (vpad) vv[tt] = (u32x2){0x3f803f80u, 0x3f803f80u};
        }
        const u32x4 vfr = ch ? (u32x4){vv[1][0], vv[1][1], vv[0][0], vv[0][1]} : (u32x4){vv[0][0], vv[0][1], vv[1][0], vv[1][1]};
        st_sc1(rs, blob + 8192 + (tp * 2 + ch) * 1024 + l * 16, vfr);
        lds_st128(L_V + ((8 + tp) * 2 + ch) * 1024 + l * 16, vfr);
    }
    {
        WAIT_VM(0);
        set_flag(rs, WS_F1 + (u32)(RB * 16 + h) * FGRP + w * FSTRIDE, tag, l);
        BAR();
        if ((w & 3) == 0 && ppok) set_flag(rs, WS_FW + ppfl * 128, tag, l);
    }
    const u32x4 qf = lds_ld128(L_Q + w * 1024 + l * 16);
    SB();
    const int cg4 = 7 * (xcd & 1) + loc % 7, RB4 = 4 * (xcd >> 1) + loc / 7;
    const int rq = loc / 7, cg = loc % 7;
    const int cw = w & 3, kh = w >> 2;
    const bool worker = loc < 28;
    {
        const int rbl = RB - 2 + (l & 1);
        const int rsafe = rbl >= 0 ? rbl : RB;
        wait_flags(ws, WS_F1 + (u32)(rsafe * 16 + h) * FGRP + ((l & 2) ? (w | 1) : w) * FSTRIDE, tag);
#pragma unroll
        for (int blk = 0; blk < 2; ++blk) {
            const int rb = RB - 2 + blk;
            if (rb >= 0) glds16_run_sc1<2>(ws + WS_KV + (size_t)(rb * 16 + h) * 16384 + w * 1024, l * 16, L_K + blk * 8192 + w * 1024, 8192, L_V - L_K);
            else { lds_st128(L_K + blk * 8192 + tid * 16, (u32x4){0, 0, 0, 0}); lds_st128(L_V + blk * 8192 + tid * 16, (u32x4){0, 0, 0, 0}); }
        }
        WAIT_VM(0);
    }
    BAR();
    u32 fwo;
    {
        const int cgs = worker ? cg : 0, cg4s = worker ? cg4 : 0;
        int pi = 392 + cgs * 8 + 4 * kh + (l & 3);
        if (l >= 4 && l < 11) pi = (2 * cg4s + (w >> 2)) * 7 + (l - 4);
        if (l >= 11 && l < 25) pi = 196 + cgs * 28 + 14 * kh + (l - 11);
        fwo = WS_FW + (u32)pi * 128;
    }
    const u32 fwv = __hip_atomic_load((GAS u32*)(ws + fwo), __ATOMIC_RELAXED, __HIP_MEMORY_SCOPE_AGENT);
    SB();
    u32x4 wof[8];
    u32x4 w1f[14];
    const u32 wofa = WS_WOF + (u32)(((worker ? cg : 0) * 4 + cw) * 16 + 8 * kh) * 1024 + l * 16;
    const u32 w1fa = WS_W1F + (u32)(((worker ? cg4 : 0) * 8 + w) * 14) * 1024 + l * 16;
#define PF2(i) do { if ((i) < 8) wof[(i) < 8 ? (i) : 0] = ld_sc1(rs, wofa + (i) * 1024); else w1f[(i) >= 8 ? (i) - 8 : 0] = ld_sc1(rs, w1fa + ((i) - 8) * 1024); } while (0)
    {
        const int base = w & ~1;
        f32x4 sc[18];
        {
            u32x4 kf[18];
#pragma unroll
            for (int a = 0; a < 18; ++a) kf[a] = lds_ld128(L_K + (base + a) * 1024 + l * 16);
            SB();
#pragma unroll
            for (int a = 0; a < 18; ++a) {
                sc[a] = mfma16(kf[a], qf, (f32x4){0.f, 0.f, 0.f, 0.f});
                SB();
            }
        }
        if (!__all(fwv == tag)) wait_flags(ws, fwo, tag);
        asm volatile("" ::: "memory");
        PF2(0); PF2(1);
        SB();
        u32x4 vf[18];
#pragma unroll
        for (int a = 0; a < 8; ++a) vf[a] = lds_ld128(L_V + (base >> 1) * 2048 + a * 1024 + l * 16);
        SB();
        const float NEG = -1e30f;
        const int qq = 256 + 16 * w + t;
#pragma unroll
        for (int a = 0; a < 2; ++a)
#pragma unroll
            for (int i = 0; i < 4; ++i) { const int kk = 16 * (base + a) + 4 * g + i; if (kk <= qq - 256) sc[a][i] = NEG; }
#pragma unroll
        for (int a = 16; a < 18; ++a)
#pragma unroll
            for (int i = 0; i < 4; ++i) { const int kk = 16 * (base + a) + 4 * g + i; if (kk > qq) sc[a][i] = NEG; }
        if (RB < 2) {
            asm volatile("" ::: "memory");
            const int tmin = 8 * (2 - RB);
#pragma unroll
            for (int a = 0; a < 18; ++a) if (base + a < tmin) sc[a] = (f32x4){NEG, NEG, NEG, NEG};
        }
        PF2(2); PF2(3);
        SB();
        float ma = NEG, mb = NEG;
#pragma unroll
        for (int a = 0; a < 18; a += 2) {
            ma = fmaxf(fmaxf(sc[a][0], sc[a][1]), ma); ma = fmaxf(fmaxf(sc[a][2], sc[a][3]), ma);
            mb = fmaxf(fmaxf(sc[a + 1][0], sc[a + 1][1]), mb); mb = fmaxf(fmaxf(sc[a + 1][2], sc[a + 1][3]), mb);
        }
        PF2(4); PF2(5);
        SB();
        float m = fmaxf(ma, mb);
        m = rows_max(m);
        f32x4 o0 = {0.f, 0.f, 0.f, 0.f}, o1 = {0.f, 0.f, 0.f, 0.f};
#pragma unroll
        for (int a = 0; a < 9; ++a) {
            if (2 * a + 8 < 18) {
                vf[2 * a + 8] = lds_ld128(L_V + (base >> 1) * 2048 + (2 * a + 8) * 1024 + l * 16);
                vf[2 * a + 9] = lds_ld128(L_V + (base >> 1) * 2048 + (2 * a + 9) * 1024 + l * 16);
            }
            if (a < 8) { PF2(6 + 2 * a); PF2(7 + 2 * a); }
            u32x4 pf;
            pf[0] = pack2(__builtin_amdgcn_exp2f(sc[2 * a][0] - m), __builtin_amdgcn_exp2f(sc[2 * a][1] - m));
            pf[1] = pack2(__builtin_amdgcn_exp2f(sc[2 * a][2] - m), __builtin_amdgcn_exp2f(sc[2 * a][3] - m));
            pf[2] = pack2(__builtin_amdgcn_exp2f(sc[2 * a + 1][0] - m), __builtin_amdgcn_exp2f(sc[2 * a + 1][1] - m));
            pf[3] = pack2(__builtin_amdgcn_exp2f(sc[2 * a + 1][2] - m), __builtin_amdgcn_exp2f(sc[2 * a + 1][3] - m));
            o0 = mfma16(vf[2 * a], pf, o0);
            o1 = mfma16(vf[2 * a + 1], pf, o1);
            SB();
        }
        const float den = rows_add(g == (odd ? 0 : 3) ? (odd ? o0[0] : o1[0]) : 0.f);
        const float r = 1.f / den;
        const float m0 = (odd && g == 0) ? 0.f : r, m1 = (!odd && g == 3) ? 0.f : r;
        const u32x4 of = {pack2(o0[0] * m0, o0[1] * m0), pack2(o0[2] * m0, o0[3] * m0), pack2(o1[0] * m1, o1[1] * m1), pack2(o1[2] * m1, o1[3] * m1)};
        st_sc1(rs, WS_O + (u32)((RB * 8 + w) * 16 + h) * 1024 + l * 16, of);
        WAIT_VM(0);
        set_flag(rs, WS_F2 + (u32)(RB * 8 + w) * FGRP + h * FSTRIDE, tag, l);
    }
    SB();
    if (!worker) {
        if (b == 255) {
#pragma unroll
            for (int i = 0; i < 4; ++i) wait_flags(ws, WS_F1 + (u32)(w * 32 + i * 8 + (l >> 3)) * FGRP + (l & 7) * FSTRIDE, tag);
            BAR();
            if (w == 0) set_flag(rs, WS_GEN, tag, l);
        }
        return;
    }
    const int RBW = 2 * xcd + (rq >> 1);
#define TTG(tt) (8 * RBW + 4 * ((tt) >> 1) + 2 * (rq & 1) + ((tt) & 1))
    const int fr = l >> 3, fo8 = l & 7;
    const int TTf = TTG(w >> 1), tl = 8 * (w & 1) + fr;
    const int tok = 16 * TTf + tl;
    const u32x4 xr = *(const u32x4*)((const char*)x + (size_t)tok * 896 + cg * 128 + fo8 * 16);
    SB();
    BAR();
    const int TTc = TTG(cw);
    wait_flags(ws, WS_F2 + (u32)TTc * FGRP + (8 * kh + (l & 7)) * FSTRIDE, tag);
    SB();
    glds16_run_sc1<8>(ws + WS_O + (size_t)(TTc * 16 + 8 * kh) * 1024, l * 16, (cw * 16 + 8 * kh) * 1024, 1024, 1024);
    WAIT_VM(0);
    BAR();
    u32x4 w2f[28];
    const int rot5 = 4 * ((cg + 2 * rq) % 7);
    const u32 w2fa = WS_W2F + (u32)((cg * 4 + cw) * 56 + 28 * kh) * 1024 + l * 16;
#define W2F(i) ld_sc1(rs, w2fa + (u32)((i) + rot5 < 28 ? (i) + rot5 : (i) + rot5 - 28) * 1024)
    u32x4 hres;
    {
        f32x4 acc[4];
        u32x4 af[8];
        const u32 ab = (8 * kh) * 1024 + l * 16;
#pragma unroll
        for (int n = 0; n < 6; ++n) af[n] = lds_ld128(ab + (n & 3) * 16384 + (n >> 2) * 1024);
        SB();
#pragma unroll
        for (int n = 0; n < 32; ++n) {
            if (n < 4) mfma_first(acc[n & 3], wof[n >> 2], af[n & 7]); else mfma_acc(acc[n & 3], wof[n >> 2], af[n & 7]);
            if (n + 6 < 32) af[(n + 6) & 7] = lds_ld128(ab + ((n + 6) & 3) * 16384 + ((n + 6) >> 2) * 1024);
            if ((n & 3) == 1 && (n >> 2) < 7) w2f[n >> 2] = W2F(n >> 2);
            SB();
        }
        MFMA_FENCE4(acc);
#pragma unroll
        for (int tt = 0; tt < 4; ++tt) lds_stf4(L_RED3 + (w * 4 + tt) * 1024 + l * 16, acc[tt]);
        BAR();
        const u32 ra = L_RED3 + (w >> 1) * 1024 + (fo8 >> 1) * 4096 + ((2 * (fo8 & 1)) * 16 + tl) * 16;
        const f32x4 a0 = lds_ldf4(ra) + lds_ldf4(ra + 16384), a1 = lds_ldf4(ra + 256) + lds_ldf4(ra + 16384 + 256);
        hres = (u32x4){pack2(bflo(xr[0]) + a0[0], bfhi(xr[0]) + a0[1]), pack2(bflo(xr[1]) + a0[2], bfhi(xr[1]) + a0[3]),
                       pack2(bflo(xr[2]) + a1[0], bfhi(xr[2]) + a1[1]), pack2(bflo(xr[3]) + a1[2], bfhi(xr[3]) + a1[3])};
        st_sc1(rs, WS_H + (u32)(TTf * 14 + 2 * cg + (fo8 >> 2)) * 1024 + ((fo8 & 3) * 16 + tl) * 16, hres);
        float sum = 0.f, sq = 0.f;
#pragma unroll
        for (int j = 0; j < 4; ++j) { const float a = bflo(hres[j]), c = bfhi(hres[j]); sum += a + c; sq = fmaf(a, a, sq); sq = fmaf(c, c, sq); }
        sum = oct_add(sum); sq = oct_add(sq);
        if (fo8 < 2) st_sc1_64(rs, WS_ST + (u32)((TTf * 7 + cg) * 2 + (w & 1)) * 128 + fo8 * 64 + fr * 8, (u32x2){__float_as_uint(sum), __float_as_uint(sq)});
        WAIT_VM(0);
        set_flag(rs, WS_F3 + (u32)TTf * FGRP + (2 * cg + (w & 1)) * FSTRIDE, tag, l);
    }
    SB();
#pragma unroll
    for (int s = 7; s < 14; ++s) w2f[s] = W2F(s);
    SB();
    f32x4 cs4, bias4;
    {
        const u32x4 ones = {0x3f803f80u, 0x3f803f80u, 0x3f803f80u, 0x3f803f80u};
        f32x4 c4 = {0.f, 0.f, 0.f, 0.f}, c5 = {0.f, 0.f, 0.f, 0.f};
#pragma unroll
        for (int s = 0; s < 14; s += 2) { c4 = mfma16(w1f[s], ones, c4); c5 = mfma16(w1f[s + 1], ones, c5); }
        cs4 = c4 + c5;
        const u32x2 bb = *(const u32x2*)((const char*)b1 + cg4 * 256 + w * 32 + g * 8);
        bias4 = (f32x4){bflo(bb[0]), bfhi(bb[0]), bflo(bb[1]), bfhi(bb[1])};
    }
    BAR();
    wait_flags(ws, WS_F3 + (u32)(8 * RB4 + (w & 3) + ((l & 16) ? 4 : 0)) * FGRP + ((l & 15) < 14 ? (l & 15) : 0) * FSTRIDE, tag);
    SB();
    {
        glds16_run_sc1<1>(ws + WS_ST + (size_t)((8 * RB4 + w) * 14) * 128, (l < 56 ? (l >> 2) : 13) * 128 + (l & 3) * 16, L_ST4 + 1024 + w * 1024, 0, 0);
        const char* hsA = ws + WS_H + (size_t)((8 * RB4 + (w & 3)) * 14 + (w >> 2)) * 1024;
        const char* hsB = hsA + 4 * 14 * 1024;
        u32 hv = l * 16 - 2048;
        dma_seek((w >> 2) * 8192 + (w & 3) * 1024 - 16384);
#pragma unroll
        for (int i = 0; i < 4; ++i) dma_next_sc1<16384, 2048>(hsA, hv);
        f32x4 acc[8];
        u32x4 af[8];
        u32 ab0 = l * 16, ab1 = l * 16 + 65536;
        OPAQUE(ab1);
#define H4_BLK(n) ((((n) % 56) >> 2) * 8 + ((n) / 56) * 4 + ((n) & 3))
#define H4_ADDR(n) (H4_BLK(n) < 64 ? ab0 + H4_BLK(n) * 1024 : ab1 + (H4_BLK(n) - 64) * 1024)
#define P4_EPILOGUE(M0) do { _Pragma("unroll") for (int m = (M0); m < (M0) + 2; ++m) { \
            u32 a[2][2]; \
            _Pragma("unroll") for (int u = 0; u < 2; ++u) { \
                const int tt = 2 * m + u; \
                const u32x2 st = *(LAS u32x2*)(uintptr_t)(L_ST4 + (16 * tt + t) * 8); \
                const float rstd = __uint_as_float(st[0]), mr = __uint_as_float(st[1]); \
                float v[4]; \
                _Pragma("unroll") for (int i = 0; i < 4; ++i) { float z = fmaf(acc[tt][i], rstd, fmaf(cs4[i], mr, bias4[i])); z = fmaxf(z, 0.f); v[i] = z * z; } \
                a[u][0] = pack2(v[0], v[1]); a[u][1] = pack2(v[2], v[3]); \
            } \
              \
            const auto r0 = __builtin_amdgcn_permlane16_swap(a[0][0], a[1][0], false, false); \
            const auto r1 = __builtin_amdgcn_permlane16_swap(a[0][1], a[1][1], false, false); \
            const u32x4 o = {r0[0], r1[0], r0[1], r1[1]}; \
            const int TTo = 8 * RB4 + 2 * m + (g & 1); \
            st_sc1(rs, WS_HH + (u32)(TTo * 56 + 4 * cg4 + (w >> 1)) * 1024 + ((2 * (w & 1) + (g >> 1)) * 16 + t) * 16, o); \
        } } while (0)
        wait_vm<2>();
        BAR();
#pragma unroll
        for (int n = 0; n < 6; ++n) af[n] = lds_ld128(H4_ADDR(n));
        SB();
#pragma unroll
        for (int seg = 0; seg < 7; ++seg) {
            constexpr int SB0[8] = {0, 10, 26, 42, 50, 66, 82, 112};
            if (seg == 1) {
                wait_vm<2>();
                const u32x2 p0 = *(LAS u32x2*)(uintptr_t)(L_ST4 + 1024 + w * 1024 + g * 128 + t * 8);
                const u32x2 p1 = *(LAS u32x2*)(uintptr_t)(L_ST4 + 1024 + w * 1024 + (g + 4) * 128 + t * 8);
                float sum = __uint_as_float(p0[0]), sq = __uint_as_float(p0[1]);
                if (g < 3) { sum += __uint_as_float(p1[0]); sq += __uint_as_float(p1[1]); }
                sum = rows_add(sum); sq = rows_add(sq);
                const float mean = sum * (1.f / 448.f);
                const float rstd = rsqrtf(fmaxf(sq * (1.f / 448.f) - mean * mean, 0.f) + 1e-5f);
                if (g == 0) lds_st64(L_ST4 + (16 * w + t) * 8, (u32x2){__float_as_uint(rstd), __float_as_uint(-mean * rstd)});
                BAR0();
            }
            if (seg == 2) { wait_vm<4>(); BAR(); }
            if (seg == 3) { wait_vm<7>(); BAR0(); }
            if (seg == 4) { wait_vm<5>(); BAR0(); }
            if (seg == 5) { wait_vm<7>(); BAR0(); }
            if (seg == 6) {
                wait_vm<4>(); BAR0();
                if (w == 0) set_flag(rs, WS_F4 + (RB4 * 2) * FGRP + cg4 * FSTRIDE, tag, l);
            }
#pragma unroll
            for (int n = SB0[seg]; n < SB0[seg + 1]; ++n) {
                if (n == 56) {
                    asm volatile("s_nop 15\n\ts_nop 15" : "+v"(acc[0]), "+v"(acc[1]), "+v"(acc[2]), "+v"(acc[3]));
                    P4_EPILOGUE(0);
                    SB();
                }
                const int ai = (n / 56) * 4 + (n & 3), ks = (n % 56) >> 2;
                if (ks == 0) mfma_first(acc[ai], w1f[ks], af[n & 7]); else mfma_acc(acc[ai], w1f[ks], af[n & 7]);
                if (n + 6 < 112) af[(n + 6) & 7] = lds_ld128(H4_ADDR(n + 6));
                if (n == 4 || n == 8 || n == 12) dma_next_sc1<16384, 2048>(hsA, hv);
                if (n == 16) { hv = l * 16 - 2048; dma_seek((w >> 2) * 8192 + (4 + (w & 3)) * 1024 - 16384); }
                if (n >= 16 && n <= 40 && (n & 3) == 0) dma_next_sc1<16384, 2048>(hsB, hv);
                if (n >= 58 && (n - 58) % 6 == 0 && (n - 58) / 6 < 7) w2f[14 + (n - 58) / 6] = W2F(14 + (n - 58) / 6);
                SB();
            }
        }
        asm volatile("s_nop 15\n\ts_nop 15" : "+v"(acc[4]), "+v"(acc[5]), "+v"(acc[6]), "+v"(acc[7]));
        P4_EPILOGUE(2);
        WAIT_VM(0);
        BAR();
        if (w == 0) set_flag(rs, WS_F4 + (RB4 * 2 + 1) * FGRP + cg4 * FSTRIDE, tag, l);
    }
    SB();
#pragma unroll
    for (int s = 21; s < 28; ++s) w2f[s] = W2F(s);
    SB();
    {
        const u32x4 bb = *(const u32x4*)((const char*)b2 + cg * 128 + fo8 * 16);
        const u32 f4o = WS_F4 + (u32)(RBW * 2) * FGRP + (7 * kh + (l < 7 ? l : 0)) * FSTRIDE;
        wait_flags(ws, f4o, tag);
        SB();
        u32 f4b = 0;
        const char* hsA = ws + WS_HH + (size_t)(TTG(cw & 1) * 56 + 28 * kh + (cw >> 1) + rot5) * 1024;
        const char* hsB = ws + WS_HH + (size_t)(TTG(2 + (cw & 1)) * 56 + 28 * kh + (cw >> 1) + rot5) * 1024;
        const int wrap5 = (28 - rot5) >> 1;
#define H5SRC(base, i) ((base) + ((i) < wrap5 ? (i) * 2048 : (i) * 2048 - 28 * 1024))
        const u32 hl = kh * L_H5 + (cw >> 1) * 2048 + (cw & 1) * 1024;
        const u32 hv = l * 16;
        dma_seek(hl - 4096);
#pragma unroll
        for (int i = 0; i < 6; ++i) dma_next_sc1_b<4096>(H5SRC(hsA, i), hv);
        f32x4 acc[4];
        u32x4 af[8];
        u32 ab0 = kh * L_H5 + l * 16, ab1 = kh * L_H5 + l * 16 + 65536;
        OPAQUE(ab0); OPAQUE(ab1);
#define H5_KS(n) (((n) % 56) >> 1)
#define H5_SLOT(n) ((n) < 56 ? H5_KS(n) : H5_KS(n) < 12 ? 28 + H5_KS(n) : H5_KS(n) - 12)
#define H5_BLK(n) (H5_SLOT(n) * 2 + ((n) & 1))
#define H5_ADDR(n) (H5_BLK(n) < 64 ? ab0 + H5_BLK(n) * 1024 : ab1 + (H5_BLK(n) - 64) * 1024)
        wait_vm<4>();
        BAR();
#pragma unroll
        for (int n = 0; n < 6; ++n) af[n] = lds_ld128(H5_ADDR(n));
        SB();
#pragma unroll
        for (int seg = 0; seg < 9; ++seg) {
            constexpr int SB0[10] = {0, 2, 10, 26, 42, 50, 66, 82, 98, 112};
            if (seg == 1) { wait_vm<3>(); BAR0(); }
            if (seg == 2) {
                wait_vm<2>(); BAR0();
                asm volatile("global_load_dword %0, %1, %2 sc1" : "=v"(f4b) : "v"(f4o + FGRP), "s"(ws) : "memory");
            }
            if (seg == 3) {
                wait_vm<2>();
                asm volatile("" : "+v"(f4b));
                if (!__all(f4b == tag)) wait_flags(ws, f4o + FGRP, tag);
                BAR0();
            }
            if (seg == 4) { wait_vm<5>(); BAR0(); }
            if (seg == 5) { wait_vm<4>(); BAR0(); }
            if (seg == 6) { wait_vm<5>(); BAR0(); }
            if (seg == 7) { wait_vm<2>(); BAR0(); }
            if (seg == 8) { wait_vm<0>(); BAR0(); }
#pragma unroll
            for (int n = SB0[seg]; n < SB0[seg + 1]; ++n) {
                const int ai = (n / 56) * 2 + (n & 1);
                if (H5_KS(n) == 0) mfma_first(acc[ai], w2f[H5_KS(n)], af[n & 7]); else mfma_acc(acc[ai], w2f[H5_KS(n)], af[n & 7]);
                if (n + 6 < 112) af[(n + 6) & 7] = lds_ld128(H5_ADDR(n + 6));
                constexpr int NB0 = 28;
                if (n <= 21 && n % 3 == 0) dma_next_sc1_b<4096>(H5SRC(hsA, 6 + n / 3), hv);
                if (n == NB0) dma_seek(hl + 28 * 2048 - 4096);
                if (n == NB0 + 18) dma_seek(hl - 4096);
                if (n >= NB0 && n <= NB0 + 39 && (n - NB0) % 3 == 0) dma_next_sc1_b<4096>(H5SRC(hsB, (n - NB0) / 3), hv);
                SB();
            }
        }
        MFMA_FENCE4(acc);
        const u32 rw = (w < 6 ? 32768 + w * 4096 : L_H5 + 32768 + (w - 6) * 4096);
#pragma unroll
        for (int tt = 0; tt < 4; ++tt) lds_stf4(rw + tt * 1024 + l * 16, acc[tt]);
        BAR();
        const int c5 = fo8 >> 1;
        const u32 lp = (w >> 1) * 1024 + ((2 * (fo8 & 1)) * 16 + tl) * 16;
        const u32 r0a = 32768 + c5 * 4096 + lp, r1a = (c5 < 2 ? 32768 + (4 + c5) * 4096 : L_H5 + 32768 + (c5 - 2) * 4096) + lp;
        const f32x4 a0 = lds_ldf4(r0a) + lds_ldf4(r1a), a1 = lds_ldf4(r0a + 256) + lds_ldf4(r1a + 256);
        const u32x4 o = {pack2(a0[0] + bflo(bb[0]) + bflo(hres[0]), a0[1] + bfhi(bb[0]) + bfhi(hres[0])),
                         pack2(a0[2] + bflo(bb[1]) + bflo(hres[1]), a0[3] + bfhi(bb[1]) + bfhi(hres[1])),
                         pack2(a1[0] + bflo(bb[2]) + bflo(hres[2]), a1[1] + bfhi(bb[2]) + bfhi(hres[2])),
                         pack2(a1[2] + bflo(bb[3]) + bflo(hres[3]), a1[3] + bfhi(bb[3]) + bfhi(hres[3]))};
        __builtin_amdgcn_raw_buffer_store_b128(o, __builtin_amdgcn_make_buffer_rsrc(out, 0, 2048 * 896, 0x00020000), (u32)tok * 896 + cg * 128 + fo8 * 16, 0, 16);
    }
}

extern "C" void kernel_launch(void* const* d_in, const int* in_sizes, int n_in, void* d_out, int out_size, void* d_ws, size_t ws_size,
                              hipStream_t stream) {
    (void)hipFuncSetAttribute((const void*)fused_kernel, hipFuncAttributeMaxDynamicSharedMemorySize, LDS_BYTES);
    fused_kernel<<<256, 512, LDS_BYTES, stream>>>((const u16*)d_in[0], (const u16*)d_in[1], (const u16*)d_in[2], (const u16*)d_in[4],
                                                  (const u16*)d_in[5], (const u16*)d_in[6], (const u16*)d_in[7], (u16*)d_out, (char*)d_ws);
}
```
